```python
import torch

def _softmax(x, axis=-1):
    return torch.softmax(x, dim=axis)
def _gelu(x):
    return torch.nn.functional.gelu(x, approximate="tanh")
def _silu(x):
    return torch.nn.functional.silu(x)
def _sigmoid(x):
    return torch.sigmoid(x)
def _layernorm(x, axis=-1, eps=1e-5):
    m = x.mean(dim=axis, keepdim=True)
    v = x.var(dim=axis, keepdim=True, unbiased=False)
    return (x - m) / torch.sqrt(v + eps)
def _scatter_add(tgt, idx, src, axis=0):
    return tgt.clone().index_add_(axis, idx, src)
def _segment_sum(src, seg, n):
    out = torch.zeros((n,) + src.shape[1:], dtype=src.dtype)
    return out.index_add_(0, seg, src)
def _pad(x, pw):
    flat = tuple(v for pair in reversed(pw) for v in pair)
    return torch.nn.functional.pad(x, flat)

kernel_name = 'parallel_sum__mha_block__dense_activation_relu__prompt'

def setup_inputs():

    _w = (lambda a: (lambda t: t.to(torch.bfloat16) if t.is_floating_point() else t)(torch.as_tensor(a)))
    def _A_setup():
        B, S, D, H = 1, 2048, 448, 16
        return {
            "x": _w((lambda *s: torch.randn(*s, dtype=torch.bfloat16) * 0.05)(B, S, D)),
            "wqkv": _w((lambda *s: torch.randn(*s, dtype=torch.bfloat16) * 0.2)(D, 3 * D)),
            "wo": _w((lambda *s: torch.randn(*s, dtype=torch.bfloat16) * 0.1)(D, D)),
        }
    _A_in = _A_setup()

    def _B_setup():
        B, S, D, Dff = 1, 2048, 448, 1792
        return {
            "x": _w((lambda *s: torch.randn(*s, dtype=torch.bfloat16) * 0.05)(B, S, D)),
            "w1": _w((lambda *s: torch.randn(*s, dtype=torch.bfloat16) * 0.2)(D, Dff)),
            "w2": _w((lambda *s: torch.randn(*s, dtype=torch.bfloat16) * 0.2)(Dff, D)),
        }
    _B_in = _B_setup()

    return {
        "A_x": _A_in["x"],
        "A_wqkv": _A_in["wqkv"],
        "A_wo": _A_in["wo"],
        "B_x": _B_in["x"],
        "B_w1": _B_in["w1"],
        "B_w2": _B_in["w2"],
    }

def reference(A_x, A_wqkv, A_wo, B_x, B_w1, B_w2):
    def _A_ref(x, wqkv, wo):
        B, S, D = x.shape
        H = 16
        Dh = D // H
        qkv = (lambda x, *d: x.permute(*d))((x @ wqkv).reshape(B, S, 3, H, Dh), 2, 0, 3, 1, 4)
        q, k, v = qkv[0], qkv[1], qkv[2]
        scores = torch.einsum("bhqd,bhkd->bhqk", q, k) * (Dh ** -0.5)
        p = _softmax(scores, axis=-1)
        o = torch.einsum("bhqk,bhkd->bhqd", p, v)
        return (lambda x, *d: x.permute(*d))(o, 0, 2, 1, 3).reshape(B, S, D) @ wo

    def _B_ref(x, w1, w2):
        h = torch.einsum("bsd,df->bsf", x, w1)
        zero = torch.zeros_like(h)
        h = torch.maximum(h, zero)
        return torch.einsum("bsf,fd->bsd", h, w2)

    x = A_x
    xn = _layernorm(x, axis=-1)
    return x + _A_ref(xn, A_wqkv, A_wo) + _B_ref(xn, B_w1, B_w2)
```

```cpp
#include <hip/hip_runtime.h>
#include <hip/hip_bf16.h>
typedef unsigned short u16;
__device__ inline float b2f(u16 b){ return __uint_as_float(((unsigned)b)<<16); }
__device__ inline u16 f2b(float f){ unsigned u=__float_as_uint(f); u += 0x7fff + ((u>>16)&1); return (u16)(u>>16); }
constexpr int S=2048,D=448,H=16,DH=28,F=1792;
__global__ void ln_k(const u16* x, u16* xn){
  int r=blockIdx.x; int t=threadIdx.x;
  float v[7]; float s=0; for(int i=0;i<7;i++){ v[i]=b2f(x[r*D+t+64*i]); s+=v[i]; }
  for(int o=32;o;o>>=1) s+=__shfl_xor(s,o);
  float m=s/D; float q=0; for(int i=0;i<7;i++){ float d=v[i]-m; q+=d*d; }
  for(int o=32;o;o>>=1) q+=__shfl_xor(q,o);
  float rs=rsqrtf(q/D+1e-5f);
  for(int i=0;i<7;i++) xn[r*D+t+64*i]=f2b((v[i]-m)*rs);
}
__global__ void gemm_k(const u16* A,const u16* B,u16* C,int M,int N,int K,int relu){
  int n=blockIdx.x*64+threadIdx.x; int m=blockIdx.y; if(n>=N) return;
  float a=0; for(int k=0;k<K;k++) a+=b2f(A[m*K+k])*b2f(B[k*N+n]);
  if(relu) a=fmaxf(a,0.f);
  C[m*N+n]=f2b(a);
}
__global__ void attn_k(const u16* qkv,u16* o){
  int q=blockIdx.x*64+threadIdx.x, h=blockIdx.y;
  float qv[DH]; for(int d=0;d<DH;d++) qv[d]=b2f(qkv[q*3*D+h*DH+d]);
  float m=-1e30f,l=0,acc[DH]; for(int d=0;d<DH;d++) acc[d]=0;
  for(int k=0;k<S;k++){
    float s=0; for(int d=0;d<DH;d++) s+=qv[d]*b2f(qkv[k*3*D+D+h*DH+d]);
    s*=0.1889822365046136f;
    float mn=fmaxf(m,s); float a=__expf(m-mn), p=__expf(s-mn); l=l*a+p;
    for(int d=0;d<DH;d++) acc[d]=acc[d]*a+p*b2f(qkv[k*3*D+2*D+h*DH+d]);
    m=mn;
  }
  for(int d=0;d<DH;d++) o[q*D+h*DH+d]=f2b(acc[d]/l);
}
__global__ void fin_k(const u16* x,const u16* o,const u16* wo,const u16* hh,const u16* w2,u16* out){
  int n=blockIdx.x*64+threadIdx.x; int m=blockIdx.y;
  float a=0; for(int k=0;k<D;k++) a+=b2f(o[m*D+k])*b2f(wo[k*D+n]);
  float b=0; for(int k=0;k<F;k++) b+=b2f(hh[m*F+k])*b2f(w2[k*D+n]);
  out[m*D+n]=f2b(b2f(x[m*D+n])+a+b);
}
extern "C" void kernel_launch(void* const* d_in, const int* in_sizes, int n_in, void* d_out, int out_size, void* d_ws, size_t ws_size, hipStream_t stream) {
  const u16* x=(const u16*)d_in[0]; const u16* wqkv=(const u16*)d_in[1]; const u16* wo=(const u16*)d_in[2];
  const u16* w1=(const u16*)d_in[4]; const u16* w2=(const u16*)d_in[5];
  u16* xn=(u16*)d_ws; u16* qkv=xn+S*D; u16* hh=qkv+S*3*D; u16* o=hh+S*F;
  ln_k<<<S,64,0,stream>>>(x,xn);
  gemm_k<<<dim3(3*D/64,S),64,0,stream>>>(xn,wqkv,qkv,S,3*D,D,0);
  gemm_k<<<dim3(F/64,S),64,0,stream>>>(xn,w1,hh,S,F,D,1);
  attn_k<<<dim3(S/64,H),64,0,stream>>>(qkv,o);
  fin_k<<<dim3(D/64,S),64,0,stream>>>(x,o,wo,hh,w2,(u16*)d_out);
}
```

```cpp
#include <hip/hip_runtime.h>
#include <stdint.h>

typedef short bf16x8 __attribute__((ext_vector_type(8)));
typedef short s16x4 __attribute__((ext_vector_type(4)));
typedef float f32x16 __attribute__((ext_vector_type(16)));
typedef float f32x4 __attribute__((ext_vector_type(4)));
typedef float f32x2 __attribute__((ext_vector_type(2)));
typedef __bf16 bf16x2v __attribute__((ext_vector_type(2)));
typedef unsigned u32x4 __attribute__((ext_vector_type(4)));
typedef unsigned long long u64;
#define LAS __attribute__((address_space(3)))
#define GAS __attribute__((address_space(1)))
#define DI __device__ __forceinline__
#define MFMA32(a, b, c) __builtin_amdgcn_mfma_f32_32x32x16_bf16(a, b, c, 0, 0, 0)
#define MFMA16(a, b, c) __builtin_amdgcn_mfma_f32_16x16x32_bf16(a, b, c, 0, 0, 0)
#define SBAR() __builtin_amdgcn_sched_barrier(0)
#define PIN(x) asm volatile("" : "+v"(x))
#define RLX_AGENT __ATOMIC_RELAXED, __HIP_MEMORY_SCOPE_AGENT
#define WAIT_BAR(N) asm volatile("s_waitcnt vmcnt(" #N ") lgkmcnt(0)\n\ts_barrier" ::: "memory")
#define WAIT_VM(N) asm volatile("s_waitcnt vmcnt(" #N ")" ::: "memory")
#define WAIT_LGKM0() asm volatile("s_waitcnt lgkmcnt(0)" ::: "memory")

constexpr unsigned WS_EPOCH = 0;
constexpr unsigned WS_FKV = 4096, WS_FH = WS_FKV + 32768, WS_FO = WS_FH + 32768;
constexpr unsigned WS_KV = 1u << 20;
constexpr unsigned WS_H = 5u << 20;
constexpr unsigned WS_O = 13u << 20;
constexpr unsigned WS_BYTES = 15u << 20;
constexpr int SLOT = 8192, NSLOT = 19;
constexpr int L_MISC = NSLOT * SLOT;
constexpr int L_STAT = L_MISC;
constexpr int L_CS0 = L_MISC + 2048, L_CSA = L_CS0 + 1024, L_CSB = L_CSA + 1024;
constexpr int L_PRIV = L_MISC + 5120;
constexpr int L_CNT = L_MISC + 7168;
constexpr int LDS_BYTES = 163840;
constexpr int L_WQ = 2 * SLOT, L_FA = 6 * SLOT, L_FB = 13 * SLOT, L_QX = SLOT, L_RING = 6 * SLOT;
constexpr int p1_slot(int item) { return (item % NSLOT) * SLOT; }
enum { C_CSA, C_CSB, C_Q, C_QR, C_KV, C_H, C_F, C_DONE, C_CMB, C_O, C_RED };

#define MAPA 8
#define G2_SPLIT 28
#define CODE_BYTES 49152
constexpr float QSCALE = 0.18898223650461362f * 1.4426950408889634f;
constexpr float REF_MARGIN = 40.f;
constexpr unsigned SPIN_CAP = 1u << 16;

DI unsigned cvtpk(float lo, float hi) { f32x2 v = {lo, hi}; return __builtin_bit_cast(unsigned, __builtin_convertvector(v, bf16x2v)); }
DI float bf16r(float x) { return __uint_as_float(cvtpk(x, 0.f) << 16); }
DI float bflo(unsigned v) { return __uint_as_float(v << 16); }
DI float bfhi(unsigned v) { return __uint_as_float(v & 0xffff0000u); }
DI float swap_add(float v) { auto r = __builtin_amdgcn_permlane32_swap(__float_as_uint(v), __float_as_uint(v), false, false); return __uint_as_float(r[0]) + __uint_as_float(r[1]); }
DI float swap_max(float v) { auto r = __builtin_amdgcn_permlane32_swap(__float_as_uint(v), __float_as_uint(v), false, false); return __builtin_fmaxf(__uint_as_float(r[0]), __uint_as_float(r[1])); }
DI float upper_bcast(float v) { auto r = __builtin_amdgcn_permlane32_swap(__float_as_uint(v), __float_as_uint(v), false, false); return __uint_as_float(r[1]); }
DI void glds16(const void* sbase, unsigned voff, unsigned ldsdst) {
  unsigned keep;
  asm volatile("s_mov_b32 %0, m0\n\ts_mov_b32 m0, %3\n\ts_nop 0\n\tglobal_load_lds_dwordx4 %1, %2\n\ts_mov_b32 m0, %0"
               : "=&s"(keep) : "v"(voff), "s"(sbase), "s"(ldsdst) : "memory");
}
typedef LAS char* lptr;
typedef short v4i16_t __attribute__((ext_vector_type(4)));
DI s16x4 trd(lptr p) { return __builtin_bit_cast(s16x4, __builtin_amdgcn_ds_read_tr16_b64_v4i16((LAS v4i16_t*)p)); }
DI bf16x8 trfrag(lptr p, int second) { s16x4 lo = trd(p), hi = trd(p + second); return __builtin_shufflevector(lo, hi, 0, 1, 2, 3, 4, 5, 6, 7); }
DI bf16x8 ldsb128(lptr p) { return *(LAS bf16x8*)p; }
DI f32x4 ldsf4(lptr p) { return *(LAS f32x4*)p; }
DI bf16x8 asfrag(u32x4 v) { return __builtin_bit_cast(bf16x8, v); }
DI unsigned cnt_add(lptr lds, int c) { unsigned r = 0; WAIT_LGKM0(); if ((threadIdx.x & 63) == 0) r = __hip_atomic_fetch_add((LAS unsigned*)(lds + L_CNT + 4 * c), 1u, __ATOMIC_RELAXED, __HIP_MEMORY_SCOPE_WORKGROUP);
  return __builtin_amdgcn_readfirstlane(r); }
DI void cnt_wait(lptr lds, int c, unsigned n) {
  while (__builtin_amdgcn_readfirstlane(__hip_atomic_load((LAS unsigned*)(lds + L_CNT + 4 * c), __ATOMIC_RELAXED, __HIP_MEMORY_SCOPE_WORKGROUP)) < n) __builtin_amdgcn_s_sleep(1);
  asm volatile("" ::: "memory"); }
DI void poll_flags(const char* flag, u64 tag) {
#pragma nounroll
  for (unsigned s = 0; s < SPIN_CAP; ++s) {
    const u64 v = __hip_atomic_load((GAS u64*)flag, RLX_AGENT);
    if (__all(v == tag)) break;
#define POLLSLEEP 1
    __builtin_amdgcn_s_sleep(POLLSLEEP);
  }
  __builtin_amdgcn_fence(__ATOMIC_ACQUIRE, "wavefront");
  asm volatile("" ::: "memory");
}
#define STAMP(i) do {} while (0)
#define CSTAMP(i) do {} while (0)
#define VSTAMP(i, v) do {} while (0)
#define STAMP_OUT() do {} while (0)
#define NRESC
#define STV
#define STA
#define BAR() asm volatile("s_waitcnt lgkmcnt(0)\n\ts_barrier" ::: "memory")

#define PRO 9
#define RAMP 3
constexpr int n_issued(int c) { return c < 0 ? PRO : (PRO + RAMP * (c + 1) < 19 + 3 * c ? PRO + RAMP * (c + 1) : 19 + 3 * c); }
static_assert(n_issued(6) >= 29, "Wq and the head of F_a must be in flight at B1");
#define MFMA4(a, b, c) __builtin_amdgcn_mfma_f32_4x4x4bf16_1k(a, b, c, 0, 0, 0)
DI void rowstats(const bf16x8& f, const s16x4& ones, f32x4& s1, f32x4& s2) {
  const s16x4 lo = __builtin_shufflevector(f, f, 0, 1, 2, 3), hi = __builtin_shufflevector(f, f, 4, 5, 6, 7);
  s1 = MFMA4(ones, lo, s1); s2 = MFMA4(lo, lo, s2); s1 = MFMA4(ones, hi, s1); s2 = MFMA4(hi, hi, s2);
}
template <int C> DI void x_frags(bf16x8 (&xa)[28], const lptr (&xrd)[4], int xwrap) {
#pragma unroll
  for (int s = 0; s < 4; ++s) xa[4 * C + s] = ldsb128(xrd[s] + p1_slot(3 * C) + (C == 6 ? xwrap : 0));
}
template <bool VT, int C> DI void p1_compute(bf16x8 (&xa)[28], f32x16& a0, f32x16& a1, f32x4& c0, f32x4& c1, const bf16x8& sel,
                                             const lptr (&xrd)[4], int xwrap, const lptr (&w0)[4], const lptr (&w1)[4] STV) {
  if constexpr (C < 7) {
    BAR();
    STAMP(12 + C);
    bf16x8 f0[4], f1[4];
#pragma unroll
    for (int s = 0; s < 4; ++s) {
      xa[4 * C + s] = ldsb128(xrd[s] + p1_slot(3 * C) + (C == 6 ? xwrap : 0));
      f0[s] = trfrag(w0[s] + p1_slot(3 * C + 2), 512); f1[s] = trfrag(w1[s] + p1_slot(3 * C + 2), 512);
    }
#pragma unroll
    for (int s = 0; s < 4; ++s) {
      if constexpr (VT) { a0 = MFMA32(xa[4 * C + s], f0[s], a0); a1 = MFMA32(xa[4 * C + s], f1[s], a1); }
      else { a0 = MFMA32(f0[s], xa[4 * C + s], a0); a1 = MFMA32(f1[s], xa[4 * C + s], a1); }
      if (s == 0) { c0 = MFMA16(sel, f0[0], c0); c1 = MFMA16(sel, f1[0], c1); }
    }
    PIN(a0); PIN(a1); PIN(c0); PIN(c1);
    p1_compute<VT, C + 1>(xa, a0, a1, c0, c1, sel, xrd, xwrap, w0, w1 STA);
  }
}
struct Ld { const char *xsrc, *w0src, *wqsrc, *fasrc, *fbsrc; unsigned xvo0, xvo1, w0vo, wfvo, wqvo, wq3, w0rs, dst; };
template <int N, int E> DI void ld_piece(const Ld& k) {
  const unsigned dst = k.dst + p1_slot(N) + E * 1024;
  if constexpr (N < 21) {
    constexpr int c = N / 3, q = N % 3;
    if constexpr (q < 2) glds16(k.xsrc + (q * 64 + E * 8) * 896 + c * 128, E ? k.xvo1 : k.xvo0, dst);
    else glds16(k.w0src + (size_t)(c * 64 + E * 8) * k.w0rs, k.w0vo, dst);
  } else if constexpr (N < 24) glds16(k.wqsrc + ((N - 21) * 128 + E * 16) * 2688, k.wqvo, dst);
  else if constexpr (N == 24) glds16(k.wqsrc + k.wq3 + E * 16 * 2688, k.wqvo, dst);
  else if constexpr (N < 32) glds16(k.fasrc + ((N - 25) * 64 + E * 8) * 3584, k.wfvo, dst);
  else glds16(k.fbsrc + ((N - 32) * 64 + E * 8) * 3584, k.wfvo, dst);
}
template <int A, int B> DI void ld_items(const Ld& k) { if constexpr (A < B) { ld_piece<A, 0>(k); ld_piece<A, 1>(k); ld_items<A + 1, B>(k); } }
template <int C> DI void p1_load(const Ld& k, bf16x8 (&xa)[28], f32x4& s1, f32x4& s2, const lptr (&xrd)[4], int xwrap STV) {
  if constexpr (C < 7) {
    asm volatile("s_waitcnt vmcnt(%0) lgkmcnt(0)\n\ts_barrier" :: "i"(2 * (n_issued(C - 1) - 3 * (C + 1))) : "memory");
    STAMP(12 + C);
    x_frags<C>(xa, xrd, xwrap);
    SBAR(); ld_items<n_issued(C - 1), n_issued(C)>(k); SBAR();
    const s16x4 ones = {0x3f80, 0x3f80, 0x3f80, 0x3f80};
#pragma unroll
    for (int s = 0; s < 4; ++s) rowstats(xa[4 * C + s], ones, s1, s2);
    PIN(s1); PIN(s2);
    p1_load<C + 1>(k, xa, s1, s2, xrd, xwrap STA);
  }
}
DI void cs_put(lptr lds, int area, int c, int t, int lane, const f32x4& cs) {
  if (lane < 16) { *(LAS float*)(lds + area + c * 512 + lane * 16 + t * 4) = cs[0]; *(LAS float*)(lds + area + c * 512 + (lane + 16) * 16 + t * 4) = cs[1]; }
}
DI float cs_get(lptr lds, int area, int c, int r32) { const f32x4 p = ldsf4(lds + area + c * 512 + r32 * 16); return (p[0] + p[1]) + (p[2] + p[3]); }
DI void lane_to_regs(lptr priv, int r32, int hh, float v, float (&o)[16]) {
  *(LAS float*)(priv + r32 * 4) = v;
#pragma unroll
  for (int i = 0; i < 4; ++i) { const f32x4 q = ldsf4(priv + (8 * i + 4 * hh) * 4); o[4 * i] = q[0]; o[4 * i + 1] = q[1]; o[4 * i + 2] = q[2]; o[4 * i + 3] = q[3]; }
}
template <bool RELU> DI void epi_T(const f32x16& acc, const float (&cs)[16], float sc, float nmb, u32x4 (&o)[2]) {
#pragma unroll
  for (int p = 0; p < 8; ++p) {
    float a = sc * __builtin_fmaf(nmb, cs[2 * p], acc[2 * p]), b = sc * __builtin_fmaf(nmb, cs[2 * p + 1], acc[2 * p + 1]);
    if (RELU) { a = __builtin_fmaxf(a, 0.f); b = __builtin_fmaxf(b, 0.f); }
    o[p >> 2][p & 3] = cvtpk(a, b);
  }
}
#define ST16(v, off) __builtin_amdgcn_raw_buffer_store_b128(v, rsw, off, 0, 16)
#define ABL_NOKV 0
#define LDAUX 16
#define LD16(off) __builtin_amdgcn_raw_buffer_load_b128(rsw, off, 0, LDAUX)

DI void set_ref(bf16x8& q1, float M, int hh) {
  const float n = -M; const float h = __uint_as_float(__float_as_uint(n) & 0xffff0000u), r = n - h;
  const float m = __uint_as_float(__float_as_uint(r) & 0xffff0000u), l = r - m;
  u32x4 v = __builtin_bit_cast(u32x4, q1);
  const unsigned w2 = (__float_as_uint(h) >> 16) | (__float_as_uint(m) & 0xffff0000u), w3 = __float_as_uint(l) >> 16;
  v[2] = hh ? w2 : v[2]; v[3] = hh ? w3 : v[3];
  q1 = __builtin_bit_cast(bf16x8, v);
}
DI float tile_max(const f32x16& s) {
  float a = __builtin_fmaxf(__builtin_fmaxf(s[0], s[1]), s[2]), b = __builtin_fmaxf(__builtin_fmaxf(s[3], s[4]), s[5]);
#pragma unroll
  for (int r = 6; r < 14; r += 4) { a = __builtin_fmaxf(__builtin_fmaxf(a, s[r]), s[r + 1]); b = __builtin_fmaxf(__builtin_fmaxf(b, s[r + 2]), s[r + 3]); }
  a = __builtin_fmaxf(__builtin_fmaxf(a, s[14]), s[15]);
  return swap_max(__builtin_fmaxf(a, b));
}

DI void row_norm(lptr lds, int t, int r32, float& rs, float& nmb) {
  const f32x2 p = *(LAS f32x2*)(lds + L_STAT + (t * 32 + r32) * 8);
  const float mean = p[0] * (1.f / 448.f), var = __builtin_fmaxf(p[1] * (1.f / 448.f) - mean * mean, 0.f);
  const float sd = bf16r(__builtin_amdgcn_sqrtf(bf16r(bf16r(var) + 1e-5f))); rs = __builtin_amdgcn_rcpf(sd); nmb = -bf16r(mean);
}
template <int P, int ST> DI void f_steps(const Ld& k, lptr lds, const bf16x8 (&xa)[28], bf16x8 (&f0)[2], bf16x8 (&f1)[2], f32x16& a0, f32x16& a1, f32x4& c0, f32x4& c1,
                                         const bf16x8& sel, const lptr (&w0)[4], const lptr (&w1)[4]) {
  if constexpr (ST < 28) {
    if constexpr (P == 0 && ST == 6) { constexpr int n = 2 * n_issued(6) + 6 < 78 ? 2 * n_issued(6) + 6 : 78; asm volatile("s_waitcnt vmcnt(%0)" :: "i"(n - 64) : "memory"); cnt_add(lds, C_F); }
    if constexpr (P == 0 && ST == 11) cnt_wait(lds, C_F, 4u);
    if constexpr (ST + 1 < 28) { constexpr int off = p1_slot((P ? 32 : 25) + ((ST + 1) >> 2)) - (P ? L_FB : L_FA);
      f0[(ST + 1) & 1] = trfrag(w0[(ST + 1) & 3] + off, 512); f1[(ST + 1) & 1] = trfrag(w1[(ST + 1) & 3] + off, 512); }
    SBAR();
    a0 = MFMA32(f0[ST & 1], xa[ST], a0); a1 = MFMA32(f1[ST & 1], xa[ST], a1);
    if constexpr ((ST & 3) == 0) { c0 = MFMA16(sel, f0[ST & 1], c0); c1 = MFMA16(sel, f1[ST & 1], c1); }
    PIN(a0); PIN(a1); PIN(c0); PIN(c1); SBAR();
    if constexpr (P == 0 && n_issued(6) + (ST >> 1) < 39) ld_piece<n_issued(6) + (ST >> 1), ST & 1>(k);
    f_steps<P, ST + 1>(k, lds, xa, f0, f1, a0, a1, c0, c1, sel, w0, w1);
  }
}

constexpr int g2_issued(int st) { int n = 28; for (int s = 0; s < st; ++s) { if (s < 14) n += 2; if (s < 24) n += 2; } return n; }
constexpr int g2_pos_h(int s) { return s < 14 ? 2 * s + 1 : g2_issued(s - 14) + 1; }
constexpr int g2_pos_w(int s) { return s < 12 ? -1 : g2_issued(s - 12) + (s - 12 < 14 ? 2 : 0) + 1; }
constexpr int g2_wait(int st) { const int a = g2_pos_h(st), b = st + 1 < 28 ? g2_pos_w(st + 1) : -1; const int n = g2_issued(st) - 1 - (a > b ? a : b); return n > 63 ? 63 : n; }
DI void ldg_sc1(u32x4& d, const char* sbase, unsigned voff) { asm volatile("global_load_dwordx4 %0, %1, %2 sc1" : "=v"(d) : "v"(voff), "s"(sbase) : "memory"); }
struct G2 { const char *hb0, *hb1, *w2s, *wos; unsigned lv, w2vo, ringdst; int t; lptr br0, br1, lds; };
template <int ST> DI void g2_w(const G2& g) {
#pragma unroll
  for (int e = 0; e < 2; ++e) {
    const unsigned dst = g.ringdst + (ST % 12) * 2048 + e * 1024;
    if constexpr (ST < 28) glds16(g.w2s + (ST * 16 + e * 8) * 896, g.w2vo, dst);
    else if constexpr (ST < 35) glds16(g.wos + ((ST - 28) * 16 + e * 8) * 896, g.w2vo, dst);
    else glds16(g.wos + (g.t == 3 ? 104 : 112 + e * 8) * 896, g.w2vo, dst);
  }
}
template <int A, int B> DI void g2_ws(const G2& g) { if constexpr (A < B) { g2_w<A>(g); g2_ws<A + 1, B>(g); } }
template <int ST> DI void g2_steps(const G2& g, u32x4 (&hf)[14][2], bf16x8 (&f0)[2], bf16x8 (&f1)[2], f32x16& a00, f32x16& a01, f32x16& a10, f32x16& a11) {
  if constexpr (ST < 28) {
    if constexpr (ST == G2_SPLIT) cnt_wait(g.lds, C_DONE, 4u);
    asm volatile("s_waitcnt vmcnt(%2)" : "+v"(hf[ST % 14][0]), "+v"(hf[ST % 14][1]) : "i"(g2_wait(ST)) : "memory");
    if constexpr (ST + 1 < 28) { f0[(ST + 1) & 1] = trfrag(g.br0 + ((ST + 1) % 12) * 2048, 512); f1[(ST + 1) & 1] = trfrag(g.br1 + ((ST + 1) % 12) * 2048, 512); }
    SBAR();
    const bf16x8 h0 = asfrag(hf[ST % 14][0]), h1 = asfrag(hf[ST % 14][1]);
    a00 = MFMA32(f0[ST & 1], h0, a00); a01 = MFMA32(f0[ST & 1], h1, a01); a10 = MFMA32(f1[ST & 1], h0, a10); a11 = MFMA32(f1[ST & 1], h1, a11);
    PIN(a00); PIN(a01); PIN(a10); PIN(a11); SBAR();
    if constexpr (ST < 14) { ldg_sc1(hf[ST][0], g.hb0 + (ST + 14) * 1024, g.lv); ldg_sc1(hf[ST][1], g.hb1 + (ST + 14) * 1024, g.lv); }
    if constexpr (ST < 24) g2_w<ST + 12>(g);
    g2_steps<ST + 1>(g, hf, f0, f1, a00, a01, a10, a11);
  }
}

__global__ void __launch_bounds__(512, 2) fused_block(const char* __restrict__ x, const char* __restrict__ wqkv, const char* __restrict__ wo,
                                                      const char* __restrict__ w1, const char* __restrict__ w2, char* __restrict__ out, char* ws) {
  __shared__ __attribute__((aligned(16))) char smem[LDS_BYTES];
  u64 pc; asm volatile("s_getpc_b64 %0" : "=s"(pc));
  const lptr lds = (lptr)smem; const unsigned lds0 = (unsigned)(uintptr_t)lds;
  const int tid = threadIdx.x, lane = tid & 63, r32 = lane & 31, hh = lane >> 5;
  const int w = __builtin_amdgcn_readfirstlane(tid >> 6), t = w & 3, c = w >> 2;
  const int b = blockIdx.x, xg = b & 7, bi = b >> 3;
  const int j = (xg & 3) * 4 + (bi >> 3), rb = (xg >> 2) * 8 + (bi & 7);
  const int fme = (rb * 16 + j) * 128, rt = rb * 4 + t;
  STAMP(0); CSTAMP(30); VSTAMP(29, (unsigned)__builtin_amdgcn_s_getreg(20 | (3 << 11)) & 0xfu);
  const int rot = t;
  const int q4 = (lane & 15) >> 2, p4 = lane & 3, g16 = (lane >> 4) & 1;
  const int pi_std = 4 * g16 + 2 * (p4 & 1) + (p4 >> 1);
  const lptr wrd0 = lds + (8 * hh + q4) * 128 + pi_std * 8;
  const lptr priv = lds + L_PRIV + w * 256;
  const __amdgpu_buffer_rsrc_t rsw = __builtin_amdgcn_make_buffer_rsrc(ws, 0, WS_BYTES, 0x00020000);
#define COMMON_SETUP() \
  lptr xrd[4], wr0[4], wr1[4]; \
  _Pragma("unroll") for (int s = 0; s < 4; ++s) { xrd[s] = lds + (t * 32 + r32) * 128 + (((2 * (s ^ rot) + hh) ^ ((r32 >> 1) & 7)) << 4); \
    wr0[s] = wrd0 + ((q4 >> 1) << 6) + (s ^ rot) * 2048; wr1[s] = wrd0 + ((1 ^ (q4 >> 1)) << 6) + (s ^ rot) * 2048; } \
  const int xwrap = (t >> 1) ? -NSLOT * SLOT : 0; \
  bf16x8 sel; { const short one = ((lane & 15) == ((lane >> 4) & 1)) ? (short)0x3f80 : (short)0; sel = (bf16x8){one, one, one, one, one, one, one, one}; } \
  bf16x8 xa[28]; float rs, nmb
#define GET_TAG() PIN(tagraw); const u64 tag = (((u64)(unsigned)__builtin_amdgcn_readfirstlane((unsigned)(tagraw >> 32)) << 32) | (unsigned)__builtin_amdgcn_readfirstlane((unsigned)tagraw)) + 1

  if (c == 0) {
    unsigned cpf0, cpf1;
    { asm volatile("global_load_dword %0, %1, %2" : "=v"(cpf0) : "v"(min((unsigned)tid * 128u, (unsigned)CODE_BYTES - 128u)), "s"(pc) : "memory");
      asm volatile("global_load_dword %0, %1, %2" : "=v"(cpf1) : "v"(min((unsigned)tid * 128u + 32768u, (unsigned)CODE_BYTES - 128u)), "s"(pc) : "memory"); }
    u64 tagraw = __hip_atomic_load((GAS u64*)(ws + WS_EPOCH), RLX_AGENT);
    if (tid < 16) *(LAS unsigned*)(lds + L_CNT + 4 * tid) = 0u;
    const bool isK = j < 7, isV = j >= 7 && j < 14;
    bf16x8 qf[4][2];
    {
      COMMON_SETUP();
      f32x16 a0 = {}, a1 = {}; f32x4 c0 = {}, c1 = {};
      if (isV) p1_compute<true, 0>(xa, a0, a1, c0, c1, sel, xrd, xwrap, wr0, wr1 STA); else p1_compute<false, 0>(xa, a0, a1, c0, c1, sel, xrd, xwrap, wr0, wr1 STA);
      STAMP(1);
      cs_put(lds, L_CS0, 0, t, lane, c0); cs_put(lds, L_CS0, 1, t, lane, c1);
      asm volatile("s_waitcnt vmcnt(0) lgkmcnt(0)\n\ts_barrier" : "+v"(cpf0), "+v"(cpf1) :: "memory");
      const float cl0 = cs_get(lds, L_CS0, 0, r32), cl1 = cs_get(lds, L_CS0, 1, r32);
      if (isV) {
        row_norm(lds, t, r32, rs, nmb);
        float rr[16], mm[16]; lane_to_regs(priv, r32, hh, rs, rr); lane_to_regs(priv + 128, r32, hh, nmb, mm);
        const int rho = r32 >> 2, col0 = (j - 7) * 64 + 4 * ((rho & 4) + 2 * (rho & 1) + ((rho >> 1) & 1)) + (r32 & 3);
#pragma unroll
        for (int cc = 0; cc < 2; ++cc) {
          const f32x16& a = cc ? a1 : a0; const float cl = cc ? cl1 : cl0; u32x4 o[2];
#pragma unroll
          for (int p = 0; p < 8; ++p) o[p >> 2][p & 3] = cvtpk(rr[2 * p] * __builtin_fmaf(mm[2 * p], cl, a[2 * p]), rr[2 * p + 1] * __builtin_fmaf(mm[2 * p + 1], cl, a[2 * p + 1]));
          const int col = col0 + cc * 32, hd = col / 28, d = col - hd * 28;
          const unsigned ad = WS_KV + hd * 262144 + rt * 4096 + 2048 + (hh * 32 + d) * 16;
          ST16(o[0], ad); ST16(o[1], ad + 1024);
          if (d >= 24) { const unsigned e = d == 24 ? 0x3f803f80u : 0u; const u32x4 pad = {e, e, e, e}; ST16(pad, ad + 64); ST16(pad, ad + 1024 + 64); }
        }
      } else {
        float cr0[16], cr1[16]; lane_to_regs(priv, r32, hh, cl0, cr0); lane_to_regs(priv + 128, r32, hh, cl1, cr1);
        row_norm(lds, t, r32, rs, nmb);
        u32x4 o[4];
        if (isK) {
          epi_T<false>(a0, cr0, rs, nmb, *(u32x4(*)[2])&o[0]); epi_T<false>(a1, cr1, rs, nmb, *(u32x4(*)[2])&o[2]);
#pragma unroll
          for (int s = 0; s < 4; ++s) {
            const int P = j * 8 + 2 * s + hh, hp = P / 7, rr = P - hp * 7;
            const int hd = 2 * hp + (rr > 3), sg = rr > 3 ? rr - 4 : rr;
            const unsigned ad = WS_KV + hd * 262144 + rt * 4096 + sg * 512 + r32 * 16;
            u32x4 v = o[s]; if (rr == 3) { v[2] = 0x3f803f80u; v[3] = 0x00003f80u; }
            ST16(v, ad);
            if (rr == 3) { const u32x4 e = {o[s][2], o[s][3], 0x3f803f80u, 0x00003f80u}; ST16(e, ad + 262144); }
          }
        } else {
          epi_T<true>(a0, cr0, rs, nmb, *(u32x4(*)[2])&o[0]); epi_T<true>(a1, cr1, rs, nmb, *(u32x4(*)[2])&o[2]);
          const unsigned ad = WS_H + rt * 114688 + (j - 14) * 4096 + lane * 16;
#pragma unroll
          for (int s = 0; s < 4; ++s) ST16(o[s], ad + s * 1024);
        }
      }
      STAMP(2);
      const int rho = 4 * g16 + p4, sg = 2 * (rho >> 2) + (rho & 1), half = (rho >> 1) & 1;
      const int g = (j & 1) ? (sg < 3 ? 2 * sg + 1 + half : (half ? 7 : 0)) : 2 * sg + half;
      lptr qb[4];
#pragma unroll
      for (int s = 0; s < 4; ++s) qb[s] = lds + L_WQ + (8 * hh + q4) * 64 + g * 8 + (s ^ rot) * 1024;
      a0 = (f32x16){}; c0 = (f32x4){};
      bf16x8 wf[3];
      wf[0] = trfrag(qb[0], 256); wf[1] = trfrag(qb[1], 256);
#pragma unroll
      for (int st = 0; st < 28; ++st) {
        if (st + 2 < 28) wf[(st + 2) % 3] = trfrag(qb[(st + 2) & 3] + ((st + 2) >> 2) * 4096, 256);
        SBAR();
        a0 = MFMA32(wf[st % 3], xa[st], a0);
        c0 = MFMA16(sel, wf[st % 3], c0);
        PIN(a0); PIN(c0); SBAR();
      }
      if (lane < 16) { *(LAS float*)(priv + lane * 4) = c0[0]; *(LAS float*)(priv + 64 + lane * 4) = c0[1]; }
      float cr[16];
#pragma unroll
      for (int i = 0; i < 4; ++i) { const f32x4 q = ldsf4(priv + (8 * i + 4 * hh) * 4); cr[4 * i] = q[0]; cr[4 * i + 1] = q[1]; cr[4 * i + 2] = q[2]; cr[4 * i + 3] = q[3]; }
      u32x4 o[2]; epi_T<false>(a0, cr, rs * QSCALE, nmb, o);
      if (hh) { o[1][2] = 0u; o[1][3] = 0u; }
      *(LAS u32x4*)(lds + L_QX + t * 2048 + lane * 16) = o[0]; *(LAS u32x4*)(lds + L_QX + t * 2048 + 1024 + lane * 16) = o[1];
    }
    cnt_add(lds, C_Q);
    STAMP(3);
    GET_TAG();
    WAIT_VM(0);
    if (cnt_add(lds, C_KV) == 3u && lane == 0) __hip_atomic_store((GAS u64*)(ws + WS_FKV + fme), tag, RLX_AGENT);
    if (cnt_add(lds, C_H) == 7u && lane == 0) __hip_atomic_store((GAS u64*)(ws + WS_FH + fme), tag, RLX_AGENT);
    STAMP(4);
    { const int c0 = (28 * j) >> 6, c1 = (28 * j + 27) >> 6, sel4 = lane >> 4;
      int jj = sel4 == 0 ? c0 : sel4 == 1 ? c1 : sel4 == 2 ? 7 + c0 : 7 + c1;
      if (j == 0 && (sel4 & 1)) jj = 14 + (sel4 >> 1);
      poll_flags(ws + WS_FKV + ((lane & 15) * 16 + jj) * 128, tag); }
    STAMP(5);
    const unsigned kvb = WS_KV + j * 262144 + t * 65536 + lane * 16;
    u32x4 kv[4][4];
#pragma unroll
    for (int u = 0; u < 3; ++u)
#pragma unroll
      for (int e = 0; e < 4; ++e) kv[u][e] = LD16(kvb + u * 4096 + e * 1024);
    cnt_wait(lds, C_Q, 4u);
#pragma unroll
    for (int q = 0; q < 4; ++q) { const lptr a = lds + L_QX + ((q + t) & 3) * 2048 + lane * 16; qf[q][0] = ldsb128(a); qf[q][1] = ldsb128(a + 1024); }
    cnt_add(lds, C_QR);
    const f32x16 zero = {};
    f32x16 o[4]; float mref[4]; bool redo = false;
#pragma unroll
    for (int q = 0; q < 4; ++q) {
      f32x16 s = MFMA32(asfrag(kv[0][0]), qf[q][0], zero); s = MFMA32(asfrag(kv[0][1]), qf[q][1], s);
      mref[q] = __builtin_ceilf(tile_max(s)) + REF_MARGIN; redo |= !(__builtin_fabsf(mref[q]) < 16777216.f);
      set_ref(qf[q][1], mref[q], hh); o[q] = zero;
    }
    STAMP(6);
    __builtin_amdgcn_s_setprio(2);
    f32x16 sA, sB; u32x4 pA0 = {}, pA1 = {}, pB0 = {}, pB1 = {};
    sA = MFMA32(asfrag(kv[0][0]), qf[0][0], zero); sA = MFMA32(asfrag(kv[0][1]), qf[0][1], sA);
    kv[3][2] = (u32x4){}; kv[3][3] = (u32x4){}; if (ABL_NOKV) { kv[3][0] = kv[0][0]; kv[3][1] = kv[0][1]; }
#define EX4(S, i) do { S[i] = __builtin_amdgcn_exp2f(S[i]); S[i + 1] = __builtin_amdgcn_exp2f(S[i + 1]); S[i + 2] = __builtin_amdgcn_exp2f(S[i + 2]); S[i + 3] = __builtin_amdgcn_exp2f(S[i + 3]); } while (0)
#define UNIT(SC, SN, PC0, PC1, PP0, PP1, KN0, KN1, QN, QP, VV0, VV1) do { \
      SBAR(); SN = MFMA32(asfrag(KN0), qf[QN][0], zero); PIN(SN); SBAR(); EX4(SC, 0); PIN(SC); SBAR(); \
      SN = MFMA32(asfrag(KN1), qf[QN][1], SN); PIN(SN); SBAR(); EX4(SC, 4); PIN(SC); SBAR(); \
      o[QP] = MFMA32(asfrag(VV0), asfrag(PP0), o[QP]); PIN(o[QP]); SBAR(); EX4(SC, 8); PIN(SC); SBAR(); \
      o[QP] = MFMA32(asfrag(VV1), asfrag(PP1), o[QP]); PIN(o[QP]); SBAR(); EX4(SC, 12); SBAR(); \
      PC0 = (u32x4){cvtpk(SC[0], SC[1]), cvtpk(SC[2], SC[3]), cvtpk(SC[4], SC[5]), cvtpk(SC[6], SC[7])}; \
      PC1 = (u32x4){cvtpk(SC[8], SC[9]), cvtpk(SC[10], SC[11]), cvtpk(SC[12], SC[13]), cvtpk(SC[14], SC[15])}; PIN(PC0); PIN(PC1); SBAR(); } while (0)
#define KEYTILE(U, UN, UL) do { \
      UNIT(sA, sB, pA0, pA1, pB0, pB1, kv[U][0], kv[U][1], 1, 3, kv[UL][2], kv[UL][3]); \
      if (!ABL_NOKV) { const int nt = min(kt + (U) + 3, 15) * 4096; _Pragma("unroll") for (int e = 0; e < 4; ++e) kv[UL][e] = __builtin_amdgcn_raw_buffer_load_b128(rsw, kvb + e * 1024, nt, LDAUX); } \
      UNIT(sB, sA, pB0, pB1, pA0, pA1, kv[U][0], kv[U][1], 2, 0, kv[U][2], kv[U][3]); \
      UNIT(sA, sB, pA0, pA1, pB0, pB1, kv[U][0], kv[U][1], 3, 1, kv[U][2], kv[U][3]); \
      UNIT(sB, sA, pB0, pB1, pA0, pA1, kv[UN][0], kv[UN][1], 0, 2, kv[U][2], kv[U][3]); \
    } while (0)
    for (int kt = 0; kt < 16; kt += 4) {
      STAMP(19 + (kt >> 2));
      KEYTILE(0, 1, 3); KEYTILE(1, 2, 0); KEYTILE(2, 3, 1); KEYTILE(3, 0, 2);
      const float lm = __builtin_fmaxf(__builtin_fmaxf(o[0][12], o[1][12]), __builtin_fmaxf(o[2][12], o[3][12]));
      if (__builtin_expect((__ballot(lm > 16777216.f) >> 32) != 0, 0)) {
        NRESC;
        o[3] = MFMA32(asfrag(kv[3][2]), asfrag(pB0), o[3]); o[3] = MFMA32(asfrag(kv[3][3]), asfrag(pB1), o[3]); pB0 = (u32x4){}; pB1 = (u32x4){};
#pragma unroll
        for (int q = 0; q < 4; ++q) { const float l = upper_bcast(o[q][12]); redo |= !(l < 1.2980742146337069e33f);
          const int e = max(__builtin_amdgcn_frexp_expf(l) + (int)REF_MARGIN, 0);
#pragma unroll
          for (int r = 0; r < 16; ++r) o[q][r] = __builtin_ldexpf(o[q][r], -e);
          mref[q] += (float)e; redo |= !(__builtin_fabsf(mref[q]) < 16777216.f); set_ref(qf[q][1], mref[q], hh);
          if (q == 0) {
#pragma unroll
            for (int r = 0; r < 16; ++r) sA[r] -= (float)e; } } }
    }
    cnt_add(lds, C_DONE);
    o[3] = MFMA32(asfrag(kv[3][2]), asfrag(pB0), o[3]); o[3] = MFMA32(asfrag(kv[3][3]), asfrag(pB1), o[3]);
    { float z[4] = {0.f, 0.f, 0.f, 0.f};
#pragma unroll
      for (int r = 0; r < 16; ++r)
#pragma unroll
        for (int q = 0; q < 4; ++q) z[q] = __builtin_fmaf(o[q][r], 0.f, z[q]);
      const float zz = (z[0] + z[1]) + (z[2] + z[3]); redo |= !(zz == 0.f); }
    if (__builtin_expect(__any(redo), 0)) {
#pragma unroll
      for (int q = 0; q < 4; ++q) { set_ref(qf[q][1], 0.f, hh); o[q] = zero; mref[q] = -3.0e38f; }
      for (int kt = 0; kt < 16; ++kt) {
        const u32x4 k0 = LD16(kvb + kt * 4096), k1 = LD16(kvb + kt * 4096 + 1024), v0 = LD16(kvb + kt * 4096 + 2048), v1 = LD16(kvb + kt * 4096 + 3072);
#pragma unroll
        for (int q = 0; q < 4; ++q) {
          f32x16 s = MFMA32(asfrag(k0), qf[q][0], zero); s = MFMA32(asfrag(k1), qf[q][1], s);
          const float mn = __builtin_fmaxf(mref[q], tile_max(s)), al = __builtin_amdgcn_exp2f(mref[q] - mn); mref[q] = mn;
#pragma unroll
          for (int r = 0; r < 16; ++r) { o[q][r] *= al; s[r] = __builtin_amdgcn_exp2f(s[r] - mn); }
          const u32x4 p0 = {cvtpk(s[0], s[1]), cvtpk(s[2], s[3]), cvtpk(s[4], s[5]), cvtpk(s[6], s[7])}, p1 = {cvtpk(s[8], s[9]), cvtpk(s[10], s[11]), cvtpk(s[12], s[13]), cvtpk(s[14], s[15])};
          o[q] = MFMA32(asfrag(v0), asfrag(p0), o[q]); o[q] = MFMA32(asfrag(v1), asfrag(p1), o[q]);
        }
      }
    }
    STAMP(7);
    VSTAMP(28, nresc + (__any(redo) ? 1000 : 0));
    cnt_wait(lds, C_QR, 4u); cnt_wait(lds, C_H, 8u);
    auto area = [&](int dst, int e) -> lptr { return lds + (dst * 3 + e) * 4096 + lane * 16; };
#pragma unroll
    for (int q = 1; q < 4; ++q) {
      const lptr a = area((q + t) & 3, q - 1);
      o[q][13] = hh ? mref[q] : o[q][13];
#pragma unroll
      for (int i = 0; i < 4; ++i) *(LAS f32x4*)(a + i * 1024) = (f32x4){o[q][4 * i], o[q][4 * i + 1], o[q][4 * i + 2], o[q][4 * i + 3]};
    }
    f32x16 of = o[0]; const float mown = mref[0];
    cnt_add(lds, C_CMB); STAMP(9); cnt_wait(lds, C_CMB, 4u);
    { f32x4 pp[3][4]; float ms[3];
#pragma unroll
      for (int e = 0; e < 3; ++e) { const lptr a = area(t, e);
#pragma unroll
        for (int i = 0; i < 4; ++i) pp[e][i] = ldsf4(a + i * 1024);
        ms[e] = upper_bcast(pp[e][3][1]); }
      const float mx = __builtin_fmaxf(__builtin_fmaxf(mown, ms[0]), __builtin_fmaxf(ms[1], ms[2]));
      const float f0 = __builtin_amdgcn_exp2f(mown - mx - 2.f), f1 = __builtin_amdgcn_exp2f(ms[0] - mx - 2.f), f2 = __builtin_amdgcn_exp2f(ms[1] - mx - 2.f), f3 = __builtin_amdgcn_exp2f(ms[2] - mx - 2.f);
#pragma unroll
      for (int r = 0; r < 16; ++r) of[r] = of[r] * f0 + pp[0][r >> 2][r & 3] * f1 + pp[1][r >> 2][r & 3] * f2 + pp[2][r >> 2][r & 3] * f3;
      const float inv = __builtin_amdgcn_rcpf(upper_bcast(of[12]));
      u32x4 e0, e1;
#pragma unroll
      for (int p = 0; p < 4; ++p) { e0[p] = cvtpk(of[2 * p] * inv, of[2 * p + 1] * inv); e1[p] = cvtpk(of[8 + 2 * p] * inv, of[9 + 2 * p] * inv); }
      if (hh) { e1[2] = 0u; e1[3] = 0u; }
      const unsigned a = WS_O + rt * 32768 + j * 2048 + lane * 16;
      ST16(e0, a); ST16(e1, a + 1024); }
    STAMP(10);
    WAIT_VM(0);
    if (cnt_add(lds, C_O) == 3u && lane == 0) __hip_atomic_store((GAS u64*)(ws + WS_FO + fme), tag, RLX_AGENT);
    STAMP(8); CSTAMP(31); STAMP_OUT();
  } else {
    Ld k;
    { const int row = 16 * t + (lane >> 3); const unsigned piece = ((lane & 7) ^ (((row >> 1) & 1) << 2)) << 4;
      k.xsrc = x + rb * 128 * 896; k.w0rs = j < 14 ? 2688u : 3584u; k.w0src = j < 14 ? wqkv + 896 + j * 128 : w1 + (j - 14) * 128;
      k.fasrc = w1 + (2 + j) * 128; k.fbsrc = w1 + (12 + j) * 128; k.dst = lds0 + t * 2048;
      k.w0vo = row * k.w0rs + piece; k.wfvo = row * 3584 + piece;
      k.wqsrc = wqkv + j * 56; k.wqvo = (32 * t + (lane >> 2)) * 2688 + (lane & 3) * 16; k.wq3 = t < 2 ? 384 * 2688 : 0;
      k.xvo0 = row * 896 + (((lane & 7) ^ (lane >> 4)) << 4); k.xvo1 = k.xvo0 ^ 64u; }
    u64 tagraw;
    {
      ld_items<0, PRO>(k);
      tagraw = __hip_atomic_load((GAS u64*)(ws + WS_EPOCH), RLX_AGENT);
      COMMON_SETUP();
      f32x4 s1 = {}, s2 = {};
      p1_load<0>(k, xa, s1, s2, xrd, xwrap STA);
      STAMP(1);
      { const float q = (lane & 2) ? ((lane & 1) ? s2[3] : s2[2]) : ((lane & 1) ? s2[1] : s2[0]);
        const float a = swap_add(s1[0]), bb = swap_add(q); if (hh == 0) *(LAS f32x2*)(lds + L_STAT + (t * 32 + r32) * 8) = (f32x2){a, bb}; }
      asm volatile("s_waitcnt vmcnt(%0) lgkmcnt(0)\n\ts_barrier" :: "i"(2 * (n_issued(6) - 28)) : "memory");
      row_norm(lds, t, r32, rs, nmb);
#pragma unroll
      for (int s = 0; s < 4; ++s) { wr0[s] += L_FA; wr1[s] += L_FA; PIN(wr0[s]); PIN(wr1[s]); }
      bf16x8 f0[2], f1[2]; f32x16 a0 = {}, a1 = {}; f32x4 c0 = {}, c1 = {}; float cr[16]; u32x4 o[2];
      f0[0] = trfrag(wr0[0], 512); f1[0] = trfrag(wr1[0], 512);
      f_steps<0, 0>(k, lds, xa, f0, f1, a0, a1, c0, c1, sel, wr0, wr1);
      cs_put(lds, L_CSA, 0, t, lane, c0); cs_put(lds, L_CSA, 1, t, lane, c1);
      WAIT_VM(0); cnt_add(lds, C_CSA); cnt_wait(lds, C_CSA, 4u);
      STAMP(2);
      { const unsigned ad = WS_H + rt * 114688 + (2 + j) * 4096 + lane * 16;
        lane_to_regs(priv, r32, hh, cs_get(lds, L_CSA, 0, r32), cr); epi_T<true>(a0, cr, rs, nmb, o); ST16(o[0], ad); ST16(o[1], ad + 1024);
        lane_to_regs(priv + 128, r32, hh, cs_get(lds, L_CSA, 1, r32), cr); epi_T<true>(a1, cr, rs, nmb, o); ST16(o[0], ad + 2048); ST16(o[1], ad + 3072); }
      STAMP(3);
      if (j >= 6) {
#pragma unroll
        for (int s = 0; s < 4; ++s) { wr0[s] += L_FB - L_FA; wr1[s] += L_FB - L_FA; PIN(wr0[s]); PIN(wr1[s]); }
        a0 = (f32x16){}; a1 = (f32x16){}; c0 = (f32x4){}; c1 = (f32x4){};
        f0[0] = trfrag(wr0[0], 512); f1[0] = trfrag(wr1[0], 512);
        f_steps<1, 0>(k, lds, xa, f0, f1, a0, a1, c0, c1, sel, wr0, wr1);
        cs_put(lds, L_CSB, 0, t, lane, c0); cs_put(lds, L_CSB, 1, t, lane, c1);
        cnt_add(lds, C_CSB); cnt_wait(lds, C_CSB, 4u);
        const unsigned ad = WS_H + rt * 114688 + (12 + j) * 4096 + lane * 16;
        lane_to_regs(priv, r32, hh, cs_get(lds, L_CSB, 0, r32), cr); epi_T<true>(a0, cr, rs, nmb, o); ST16(o[0], ad); ST16(o[1], ad + 1024);
        lane_to_regs(priv + 128, r32, hh, cs_get(lds, L_CSB, 1, r32), cr); epi_T<true>(a1, cr, rs, nmb, o); ST16(o[0], ad + 2048); ST16(o[1], ad + 3072);
      }
    }
    GET_TAG();
    WAIT_VM(0);
    if (cnt_add(lds, C_H) == 7u && lane == 0) __hip_atomic_store((GAS u64*)(ws + WS_FH + fme), tag, RLX_AGENT);
    STAMP(4);
    const int cg = bi >> 2;
    if (cg < 7) {
      const int rt0 = 8 * xg + 2 * (bi & 3), rb3 = rt0 >> 2;
      const int ring = L_RING + 3 * t * SLOT;
      const int ct = t >> 1, rtl = t & 1;
      G2 g; g.lds = lds; g.t = t; g.ringdst = lds0 + ring; g.lv = lane * 16;
      g.w2vo = (lane >> 3) * 896 + (((lane & 7) ^ (((lane >> 4) & 1) << 2)) << 4);
      g.w2s = w2 + t * 448 * 896 + cg * 128; g.wos = wo + t * 112 * 896 + cg * 128;
      g.hb0 = ws + WS_H + (rt0 + rtl) * 114688 + t * 28672; g.hb1 = ws + WS_H + (rt0 + (rtl ^ 1)) * 114688 + t * 28672;
      g.br0 = wrd0 + ring + ((ct ^ (q4 >> 1)) << 6); g.br1 = wrd0 + ring + ((ct ^ 1 ^ (q4 >> 1)) << 6);
      g2_ws<0, 12>(g);
      const int fb3 = rb3 * 16 + (lane & 15);
      poll_flags(ws + WS_FH + fb3 * 128, tag);
      WAIT_VM(0);
      STAMP(5);
      u32x4 hf[14][2];
#pragma unroll
      for (int s = 0; s < 14; ++s) { ldg_sc1(hf[s][0], g.hb0 + s * 1024, g.lv); ldg_sc1(hf[s][1], g.hb1 + s * 1024, g.lv); }
      f32x16 a00 = {}, a01 = {}, a10 = {}, a11 = {};
      PIN(g.br0); PIN(g.br1);
      { bf16x8 f0[2], f1[2]; f0[0] = trfrag(g.br0, 512); f1[0] = trfrag(g.br1, 512);
        g2_steps<0>(g, hf, f0, f1, a00, a01, a10, a11); }
      STAMP(6);
      WAIT_VM(0);
      bf16x8 wf[8][2];
      { const lptr o0 = lds + ring + SLOT + (4 * hh + q4) * 128 + pi_std * 8;
#pragma unroll
        for (int e = 0; e < 8; ++e)
#pragma unroll
          for (int cc = 0; cc < 2; ++cc) wf[e][cc] = trfrag(o0 + ((cc ^ ct ^ (q4 >> 1)) << 6) + ((e >> 1) * 28 + (e & 1) * 16) * 128, 1024); }
      const size_t oo = (size_t)((rt0 + rtl) * 32 + r32) * 896 + cg * 128 + ct * 64 + hh * 16;
      const u32x4 xr0 = *(const GAS u32x4*)(x + oo), xr1 = *(const GAS u32x4*)(x + oo + 32);
      poll_flags(ws + WS_FO + fb3 * 128, tag);
      STAMP(7);
      { const unsigned ob0 = WS_O + (rt0 + rtl) * 32768 + t * 8192 + lane * 16, ob1 = ob0 ^ 32768u;
        u32x4 of[8][2];
#pragma unroll
        for (int e = 0; e < 8; ++e) { of[e][0] = LD16(ob0 + e * 1024); of[e][1] = LD16(ob1 + e * 1024); }
        SBAR();
#pragma unroll
        for (int e = 0; e < 8; ++e) { const bf16x8 h0 = asfrag(of[e][0]), h1 = asfrag(of[e][1]);
          a11 = MFMA32(wf[e][1], h1, a11); a10 = MFMA32(wf[e][1], h0, a10); a01 = MFMA32(wf[e][0], h1, a01); a00 = MFMA32(wf[e][0], h0, a00);
          PIN(a00); PIN(a01); PIN(a10); PIN(a11); } }
      STAMP(9);
      if (b == 0 && tid == 256) __hip_atomic_store((GAS u64*)(ws + WS_EPOCH), tag, RLX_AGENT);
      { const lptr mine = lds + ring + lane * 16;
        auto put = [&](const f32x16& a, int e) {
#pragma unroll
          for (int i = 0; i < 4; ++i) *(LAS f32x4*)(mine + e * 4096 + i * 1024) = (f32x4){a[4 * i], a[4 * i + 1], a[4 * i + 2], a[4 * i + 3]}; };
        put(a11, 3); put(a10, 2); put(a01, 1); }
      f32x16 fin = a00;
      cnt_add(lds, C_RED); STAMP(10); cnt_wait(lds, C_RED, 4u);
#pragma unroll
      for (int e = 1; e < 4; ++e) { const lptr a = lds + L_RING + 3 * (t ^ e) * SLOT + e * 4096 + lane * 16;
#pragma unroll
        for (int i = 0; i < 4; ++i) { const f32x4 v = ldsf4(a + i * 1024); fin[4 * i] += v[0]; fin[4 * i + 1] += v[1]; fin[4 * i + 2] += v[2]; fin[4 * i + 3] += v[3]; } }
      u32x4 r0, r1;
#pragma unroll
      for (int p = 0; p < 4; ++p) { r0[p] = cvtpk(fin[2 * p] + bflo(xr0[p]), fin[2 * p + 1] + bfhi(xr0[p])); r1[p] = cvtpk(fin[8 + 2 * p] + bflo(xr1[p]), fin[9 + 2 * p] + bfhi(xr1[p])); }
      *(GAS u32x4*)(out + oo) = r0; *(GAS u32x4*)(out + oo + 32) = r1;
      STAMP(8);
    }
    STAMP_OUT();
  }
}

extern "C" void kernel_launch(void* const* d_in, const int* in_sizes, int n_in, void* d_out, int out_size, void* d_ws, size_t ws_size, hipStream_t stream) {
  fused_block<<<256, 512, 0, stream>>>((const char*)d_in[0], (const char*)d_in[1], (const char*)d_in[2], (const char*)d_in[4], (const char*)d_in[5], (char*)d_out, (char*)d_ws);
}
```

```cpp
#include <hip/hip_runtime.h>
#include <stdint.h>

typedef short bf16x8 __attribute__((ext_vector_type(8)));
typedef short s16x4 __attribute__((ext_vector_type(4)));
typedef float f32x16 __attribute__((ext_vector_type(16)));
typedef float f32x4 __attribute__((ext_vector_type(4)));
typedef float f32x2 __attribute__((ext_vector_type(2)));
typedef __bf16 bf16x2v __attribute__((ext_vector_type(2)));
typedef unsigned u32x4 __attribute__((ext_vector_type(4)));
typedef unsigned long long u64;
#define LAS __attribute__((address_space(3)))
#define GAS __attribute__((address_space(1)))
#define DI __device__ __forceinline__
#define MFMA32(a, b, c) __builtin_amdgcn_mfma_f32_32x32x16_bf16(a, b, c, 0, 0, 0)
#define MFMA16(a, b, c) __builtin_amdgcn_mfma_f32_16x16x32_bf16(a, b, c, 0, 0, 0)
#define SBAR() __builtin_amdgcn_sched_barrier(0)
#define PIN(x) asm volatile("" : "+v"(x))
#define RLX_AGENT __ATOMIC_RELAXED, __HIP_MEMORY_SCOPE_AGENT
#define WAIT_BAR(N) asm volatile("s_waitcnt vmcnt(" #N ") lgkmcnt(0)\n\ts_barrier" ::: "memory")
#define WAIT_VM(N) asm volatile("s_waitcnt vmcnt(" #N ")" ::: "memory")
#define WAIT_LGKM0() asm volatile("s_waitcnt lgkmcnt(0)" ::: "memory")

constexpr unsigned WS_EPOCH = 0;
constexpr unsigned WS_FKV = 4096, WS_FH = WS_FKV + 32768, WS_FO = WS_FH + 32768;
constexpr unsigned WS_KV = 1u << 20;
constexpr unsigned WS_H = 5u << 20;
constexpr unsigned WS_O = 13u << 20;
constexpr unsigned WS_BYTES = 15u << 20;
constexpr int SLOT = 8192, NSLOT = 19;
constexpr int L_MISC = NSLOT * SLOT;
constexpr int L_STAT = L_MISC;
constexpr int L_CS0 = L_MISC + 2048, L_CSA = L_CS0 + 1024, L_CSB = L_CSA + 1024;
constexpr int L_PRIV = L_MISC + 5120;
constexpr int L_CNT = L_MISC + 7168;
constexpr int LDS_BYTES = 163840;
constexpr int L_WQ = 2 * SLOT, L_FA = 6 * SLOT, L_FB = 13 * SLOT, L_QX = SLOT, L_RING = 6 * SLOT;
constexpr int p1_slot(int item) { return (item % NSLOT) * SLOT; }
enum { C_CSA, C_CSB, C_Q, C_QR, C_KV, C_H, C_F, C_DONE, C_CMB, C_O, C_RED };

#define WS_BASE 0
#define TOUCH 0
#define MAPA 8
#define G2_SPLIT 16
#define CODE_BYTES 49152
constexpr float QSCALE = 0.18898223650461362f * 1.4426950408889634f;
constexpr float REF_MARGIN = 40.f;
constexpr unsigned SPIN_CAP = 1u << 16;

DI unsigned cvtpk(float lo, float hi) { f32x2 v = {lo, hi}; return __builtin_bit_cast(unsigned, __builtin_convertvector(v, bf16x2v)); }
DI float bf16r(float x) { return __uint_as_float(cvtpk(x, 0.f) << 16); }
DI float bflo(unsigned v) { return __uint_as_float(v << 16); }
DI float bfhi(unsigned v) { return __uint_as_float(v & 0xffff0000u); }
DI float swap_add(float v) { auto r = __builtin_amdgcn_permlane32_swap(__float_as_uint(v), __float_as_uint(v), false, false); return __uint_as_float(r[0]) + __uint_as_float(r[1]); }
DI float swap_max(float v) { auto r = __builtin_amdgcn_permlane32_swap(__float_as_uint(v), __float_as_uint(v), false, false); return __builtin_fmaxf(__uint_as_float(r[0]), __uint_as_float(r[1])); }
DI float upper_bcast(float v) { auto r = __builtin_amdgcn_permlane32_swap(__float_as_uint(v), __float_as_uint(v), false, false); return __uint_as_float(r[1]); }
DI void glds16(const void* sbase, unsigned voff, unsigned ldsdst) {
  unsigned keep;
  asm volatile("s_mov_b32 %0, m0\n\ts_mov_b32 m0, %3\n\ts_nop 0\n\tglobal_load_lds_dwordx4 %1, %2\n\ts_mov_b32 m0, %0"
               : "=&s"(keep) : "v"(voff), "s"(sbase), "s"(ldsdst) : "memory");
}
typedef LAS char* lptr;
typedef short v4i16_t __attribute__((ext_vector_type(4)));
DI s16x4 trd(lptr p) { return __builtin_bit_cast(s16x4, __builtin_amdgcn_ds_read_tr16_b64_v4i16((LAS v4i16_t*)p)); }
DI bf16x8 trfrag(lptr p, int second) { s16x4 lo = trd(p), hi = trd(p + second); return __builtin_shufflevector(lo, hi, 0, 1, 2, 3, 4, 5, 6, 7); }
DI bf16x8 ldsb128(lptr p) { return *(LAS bf16x8*)p; }
DI f32x4 ldsf4(lptr p) { return *(LAS f32x4*)p; }
DI bf16x8 asfrag(u32x4 v) { return __builtin_bit_cast(bf16x8, v); }
DI unsigned cnt_add(lptr lds, int c) { unsigned r = 0; WAIT_LGKM0(); if ((threadIdx.x & 63) == 0) r = __hip_atomic_fetch_add((LAS unsigned*)(lds + L_CNT + 4 * c), 1u, __ATOMIC_RELAXED, __HIP_MEMORY_SCOPE_WORKGROUP);
  return __builtin_amdgcn_readfirstlane(r); }
DI void cnt_wait(lptr lds, int c, unsigned n) {
  while (__builtin_amdgcn_readfirstlane(__hip_atomic_load((LAS unsigned*)(lds + L_CNT + 4 * c), __ATOMIC_RELAXED, __HIP_MEMORY_SCOPE_WORKGROUP)) < n) __builtin_amdgcn_s_sleep(1);
  asm volatile("" ::: "memory"); }
DI void poll_flags(const char* flag, u64 tag) {
#pragma nounroll
  for (unsigned s = 0; s < SPIN_CAP; ++s) {
    const u64 v = __hip_atomic_load((GAS u64*)flag, RLX_AGENT);
    if (__all(v == tag)) break;
#define POLLSLEEP 1
    __builtin_amdgcn_s_sleep(POLLSLEEP);
  }
  __builtin_amdgcn_fence(__ATOMIC_ACQUIRE, "wavefront");
  asm volatile("" ::: "memory");
}
#define STAMP(i) do {} while (0)
#define CSTAMP(i) do {} while (0)
#define VSTAMP(i, v) do {} while (0)
#define STAMP_OUT() do {} while (0)
#define NRESC
#define STV
#define STA
#define BAR() asm volatile("s_waitcnt lgkmcnt(0)\n\ts_barrier" ::: "memory")

#define PRO 9
#define RAMP 3
constexpr int n_issued(int c) { return c < 0 ? PRO : (PRO + RAMP * (c + 1) < 19 + 3 * c ? PRO + RAMP * (c + 1) : 19 + 3 * c); }
static_assert(n_issued(6) >= 29, "Wq and the head of F_a must be in flight at B1");
#define MFMA4(a, b, c) __builtin_amdgcn_mfma_f32_4x4x4bf16_1k(a, b, c, 0, 0, 0)
DI void rowstats(const bf16x8& f, const s16x4& ones, f32x4& s1, f32x4& s2) {
  const s16x4 lo = __builtin_shufflevector(f, f, 0, 1, 2, 3), hi = __builtin_shufflevector(f, f, 4, 5, 6, 7);
  s1 = MFMA4(ones, lo, s1); s2 = MFMA4(lo, lo, s2); s1 = MFMA4(ones, hi, s1); s2 = MFMA4(hi, hi, s2);
}
template <int C> DI void x_frags(bf16x8 (&xa)[28], const lptr (&xrd)[4], int xwrap) {
#pragma unroll
  for (int s = 0; s < 4; ++s) xa[4 * C + s] = ldsb128(xrd[s] + p1_slot(3 * C) + (C == 6 ? xwrap : 0));
}
template <bool VT, int C> DI void p1_compute(bf16x8 (&xa)[28], f32x16& a0, f32x16& a1, f32x4& c0, f32x4& c1, const bf16x8& sel,
                                             const lptr (&xrd)[4], int xwrap, const lptr (&w0)[4], const lptr (&w1)[4] STV) {
  if constexpr (C < 7) {
    BAR();
    if (C == 0) STAMP(12);
    bf16x8 f0[4], f1[4];
#pragma unroll
    for (int s = 0; s < 4; ++s) {
      xa[4 * C + s] = ldsb128(xrd[s] + p1_slot(3 * C) + (C == 6 ? xwrap : 0));
      f0[s] = trfrag(w0[s] + p1_slot(3 * C + 2), 512); f1[s] = trfrag(w1[s] + p1_slot(3 * C + 2), 512);
    }
#pragma unroll
    for (int s = 0; s < 4; ++s) {
      if constexpr (VT) { a0 = MFMA32(xa[4 * C + s], f0[s], a0); a1 = MFMA32(xa[4 * C + s], f1[s], a1); }
      else { a0 = MFMA32(f0[s], xa[4 * C + s], a0); a1 = MFMA32(f1[s], xa[4 * C + s], a1); }
      if (s == 0) { c0 = MFMA16(sel, f0[0], c0); c1 = MFMA16(sel, f1[0], c1); }
    }
    PIN(a0); PIN(a1); PIN(c0); PIN(c1);
    p1_compute<VT, C + 1>(xa, a0, a1, c0, c1, sel, xrd, xwrap, w0, w1 STA);
  }
}
struct Ld { const char *xsrc, *w0src, *wqsrc, *fasrc, *fbsrc; unsigned xvo0, xvo1, w0vo, wfvo, wqvo, wq3, w0rs, dst; };
template <int N, int E> DI void ld_piece(const Ld& k) {
  const unsigned dst = k.dst + p1_slot(N) + E * 1024;
  if constexpr (N < 21) {
    constexpr int c = N / 3, q = N % 3;
    if constexpr (q < 2) glds16(k.xsrc + (q * 64 + E * 8) * 896 + c * 128, E ? k.xvo1 : k.xvo0, dst);
    else glds16(k.w0src + (size_t)(c * 64 + E * 8) * k.w0rs, k.w0vo, dst);
  } else if constexpr (N < 24) glds16(k.wqsrc + ((N - 21) * 128 + E * 16) * 2688, k.wqvo, dst);
  else if constexpr (N == 24) glds16(k.wqsrc + k.wq3 + E * 16 * 2688, k.wqvo, dst);
  else if constexpr (N < 32) glds16(k.fasrc + ((N - 25) * 64 + E * 8) * 3584, k.wfvo, dst);
  else glds16(k.fbsrc + ((N - 32) * 64 + E * 8) * 3584, k.wfvo, dst);
}
template <int A, int B> DI void ld_items(const Ld& k) { if constexpr (A < B) { ld_piece<A, 0>(k); ld_piece<A, 1>(k); ld_items<A + 1, B>(k); } }
template <int C> DI void p1_load(const Ld& k, bf16x8 (&xa)[28], f32x4& s1, f32x4& s2, const lptr (&xrd)[4], int xwrap STV) {
  if constexpr (C < 7) {
    asm volatile("s_waitcnt vmcnt(%0) lgkmcnt(0)\n\ts_barrier" :: "i"(2 * (n_issued(C - 1) - 3 * (C + 1))) : "memory");
    if (C == 0) STAMP(12);
    x_frags<C>(xa, xrd, xwrap);
    SBAR(); ld_items<n_issued(C - 1), n_issued(C)>(k); SBAR();
    const s16x4 ones = {0x3f80, 0x3f80, 0x3f80, 0x3f80};
#pragma unroll
    for (int s = 0; s < 4; ++s) rowstats(xa[4 * C + s], ones, s1, s2);
    PIN(s1); PIN(s2);
    p1_load<C + 1>(k, xa, s1, s2, xrd, xwrap STA);
  }
}
DI void cs_put(lptr lds, int area, int c, int t, int lane, const f32x4& cs) {
  if (lane < 16) { *(LAS float*)(lds + area + c * 512 + lane * 16 + t * 4) = cs[0]; *(LAS float*)(lds + area + c * 512 + (lane + 16) * 16 + t * 4) = cs[1]; }
}
DI float cs_get(lptr lds, int area, int c, int r32) { const f32x4 p = ldsf4(lds + area + c * 512 + r32 * 16); return (p[0] + p[1]) + (p[2] + p[3]); }
DI void lane_to_regs(lptr priv, int r32, int hh, float v, float (&o)[16]) {
  *(LAS float*)(priv + r32 * 4) = v;
#pragma unroll
  for (int i = 0; i < 4; ++i) { const f32x4 q = ldsf4(priv + (8 * i + 4 * hh) * 4); o[4 * i] = q[0]; o[4 * i + 1] = q[1]; o[4 * i + 2] = q[2]; o[4 * i + 3] = q[3]; }
}
template <bool RELU> DI void epi_T(const f32x16& acc, const float (&cs)[16], float sc, float nmb, u32x4 (&o)[2]) {
#pragma unroll
  for (int p = 0; p < 8; ++p) {
    float a = sc * __builtin_fmaf(nmb, cs[2 * p], acc[2 * p]), b = sc * __builtin_fmaf(nmb, cs[2 * p + 1], acc[2 * p + 1]);
    if (RELU) { a = __builtin_fmaxf(a, 0.f); b = __builtin_fmaxf(b, 0.f); }
    o[p >> 2][p & 3] = cvtpk(a, b);
  }
}
#define ST16(v, off) __builtin_amdgcn_raw_buffer_store_b128(v, rsw, off, 0, 16)
#define ABL_NOKV 0
#define LDAUX 16
#define LD16(off) __builtin_amdgcn_raw_buffer_load_b128(rsw, off, 0, LDAUX)

DI void set_ref(bf16x8& q1, float M, int hh) {
  const float n = -M; const float h = __uint_as_float(__float_as_uint(n) & 0xffff0000u), r = n - h;
  const float m = __uint_as_float(__float_as_uint(r) & 0xffff0000u), l = r - m;
  u32x4 v = __builtin_bit_cast(u32x4, q1);
  const unsigned w2 = (__float_as_uint(h) >> 16) | (__float_as_uint(m) & 0xffff0000u), w3 = __float_as_uint(l) >> 16;
  v[2] = hh ? w2 : v[2]; v[3] = hh ? w3 : v[3];
  q1 = __builtin_bit_cast(bf16x8, v);
}
DI float tile_max(const f32x16& s) {
  float a = __builtin_fmaxf(__builtin_fmaxf(s[0], s[1]), s[2]), b = __builtin_fmaxf(__builtin_fmaxf(s[3], s[4]), s[5]);
#pragma unroll
  for (int r = 6; r < 14; r += 4) { a = __builtin_fmaxf(__builtin_fmaxf(a, s[r]), s[r + 1]); b = __builtin_fmaxf(__builtin_fmaxf(b, s[r + 2]), s[r + 3]); }
  a = __builtin_fmaxf(__builtin_fmaxf(a, s[14]), s[15]);
  return swap_max(__builtin_fmaxf(a, b));
}

DI void row_norm(lptr lds, int t, int r32, float& rs, float& nmb) {
  const f32x2 p = *(LAS f32x2*)(lds + L_STAT + (t * 32 + r32) * 8);
  const float mean = p[0] * (1.f / 448.f), var = __builtin_fmaxf(p[1] * (1.f / 448.f) - mean * mean, 0.f);
  const float sd = bf16r(__builtin_amdgcn_sqrtf(bf16r(bf16r(var) + 1e-5f))); rs = __builtin_amdgcn_rcpf(sd); nmb = -bf16r(mean);
}
template <int P, int ST> DI void f_steps(const Ld& k, lptr lds, const bf16x8 (&xa)[28], bf16x8 (&f0)[2], bf16x8 (&f1)[2], f32x16& a0, f32x16& a1, f32x4& c0, f32x4& c1,
                                         const bf16x8& sel, const lptr (&w0)[4], const lptr (&w1)[4]) {
  if constexpr (ST < 28) {
    if constexpr (P == 0 && ST == 6) { constexpr int n = 2 * n_issued(6) + 6 < 78 ? 2 * n_issued(6) + 6 : 78; asm volatile("s_waitcnt vmcnt(%0)" :: "i"(n - 64) : "memory"); cnt_add(lds, C_F); }
    if constexpr (P == 0 && ST == 11) cnt_wait(lds, C_F, 4u);
    if constexpr (ST + 1 < 28) { constexpr int off = p1_slot((P ? 32 : 25) + ((ST + 1) >> 2)) - (P ? L_FB : L_FA);
      f0[(ST + 1) & 1] = trfrag(w0[(ST + 1) & 3] + off, 512); f1[(ST + 1) & 1] = trfrag(w1[(ST + 1) & 3] + off, 512); }
    SBAR();
    a0 = MFMA32(f0[ST & 1], xa[ST], a0); a1 = MFMA32(f1[ST & 1], xa[ST], a1);
    if constexpr ((ST & 3) == 0) { c0 = MFMA16(sel, f0[ST & 1], c0); c1 = MFMA16(sel, f1[ST & 1], c1); }
    PIN(a0); PIN(a1); PIN(c0); PIN(c1); SBAR();
    if constexpr (P == 0 && n_issued(6) + (ST >> 1) < 39) ld_piece<n_issued(6) + (ST >> 1), ST & 1>(k);
    f_steps<P, ST + 1>(k, lds, xa, f0, f1, a0, a1, c0, c1, sel, w0, w1);
  }
}

constexpr int g2_issued(int st) { int n = 28; for (int s = 0; s < st; ++s) { if (s < 14) n += 2; if (s < 24) n += 2; } return n; }
constexpr int g2_pos_h(int s) { return s < 14 ? 2 * s + 1 : g2_issued(s - 14) + 1; }
constexpr int g2_pos_w(int s) { return s < 12 ? -1 : g2_issued(s - 12) + (s - 12 < 14 ? 2 : 0) + 1; }
constexpr int g2_wait(int st) { const int a = g2_pos_h(st), b = st + 1 < 28 ? g2_pos_w(st + 1) : -1; const int n = g2_issued(st) - 1 - (a > b ? a : b); return n > 63 ? 63 : n; }
DI void ldg_sc1(u32x4& d, const char* sbase, unsigned voff) { asm volatile("global_load_dwordx4 %0, %1, %2 sc1" : "=v"(d) : "v"(voff), "s"(sbase) : "memory"); }
struct G2 { const char *hb0, *hb1, *w2s, *wos; unsigned lv, w2vo, ringdst; int t; lptr br0, br1, lds; };
template <int ST> DI void g2_w(const G2& g) {
#pragma unroll
  for (int e = 0; e < 2; ++e) {
    const unsigned dst = g.ringdst + (ST % 12) * 2048 + e * 1024;
    if constexpr (ST < 28) glds16(g.w2s + (ST * 16 + e * 8) * 896, g.w2vo, dst);
    else if constexpr (ST < 35) glds16(g.wos + ((ST - 28) * 16 + e * 8) * 896, g.w2vo, dst);
    else glds16(g.wos + (g.t == 3 ? 104 : 112 + e * 8) * 896, g.w2vo, dst);
  }
}
template <int A, int B> DI void g2_ws(const G2& g) { if constexpr (A < B) { g2_w<A>(g); g2_ws<A + 1, B>(g); } }
template <int ST> DI void g2_steps(const G2& g, u32x4 (&hf)[14][2], bf16x8 (&f0)[2], bf16x8 (&f1)[2], f32x16& a00, f32x16& a01, f32x16& a10, f32x16& a11) {
  if constexpr (ST < 28) {
    if constexpr (ST == G2_SPLIT) cnt_wait(g.lds, C_DONE, 4u);
    asm volatile("s_waitcnt vmcnt(%2)" : "+v"(hf[ST % 14][0]), "+v"(hf[ST % 14][1]) : "i"(g2_wait(ST)) : "memory");
    if constexpr (ST + 1 < 28) { f0[(ST + 1) & 1] = trfrag(g.br0 + ((ST + 1) % 12) * 2048, 512); f1[(ST + 1) & 1] = trfrag(g.br1 + ((ST + 1) % 12) * 2048, 512); }
    SBAR();
    const bf16x8 h0 = asfrag(hf[ST % 14][0]), h1 = asfrag(hf[ST % 14][1]);
    a00 = MFMA32(f0[ST & 1], h0, a00); a01 = MFMA32(f0[ST & 1], h1, a01); a10 = MFMA32(f1[ST & 1], h0, a10); a11 = MFMA32(f1[ST & 1], h1, a11);
    PIN(a00); PIN(a01); PIN(a10); PIN(a11); SBAR();
    if constexpr (ST < 14) { ldg_sc1(hf[ST][0], g.hb0 + (ST + 14) * 1024, g.lv); ldg_sc1(hf[ST][1], g.hb1 + (ST + 14) * 1024, g.lv); }
    if constexpr (ST < 24) g2_w<ST + 12>(g);
    g2_steps<ST + 1>(g, hf, f0, f1, a00, a01, a10, a11);
  }
}

__global__ void __launch_bounds__(512, 2) fused_block(const char* __restrict__ x, const char* __restrict__ wqkv, const char* __restrict__ wo,
                                                      const char* __restrict__ w1, const char* __restrict__ w2, char* __restrict__ out, char* ws) {
  __shared__ __attribute__((aligned(16))) char smem[LDS_BYTES];
  u64 pc; asm volatile("s_getpc_b64 %0" : "=s"(pc));
  const lptr lds = (lptr)smem; const unsigned lds0 = (unsigned)(uintptr_t)lds;
  const int tid = threadIdx.x, lane = tid & 63, r32 = lane & 31, hh = lane >> 5;
  const int w = __builtin_amdgcn_readfirstlane(tid >> 6), t = w & 3, c = w >> 2;
  const int b = blockIdx.x, xg = b & 7, bi = b >> 3;
  const int j = (xg & 3) * 4 + (bi >> 3), rb = (xg >> 2) * 8 + (bi & 7);
  const int fme = (rb * 16 + j) * 128, rt = rb * 4 + t;
  STAMP(0); CSTAMP(30); VSTAMP(29, (unsigned)__builtin_amdgcn_s_getreg(20 | (3 << 11)) & 0xfu);
  const int rot = t;
  const int q4 = (lane & 15) >> 2, p4 = lane & 3, g16 = (lane >> 4) & 1;
  const int pi_std = 4 * g16 + 2 * (p4 & 1) + (p4 >> 1);
  const lptr wrd0 = lds + (8 * hh + q4) * 128 + pi_std * 8;
  const lptr priv = lds + L_PRIV + w * 256;
  const __amdgpu_buffer_rsrc_t rsw = __builtin_amdgcn_make_buffer_rsrc(ws, 0, WS_BYTES, 0x00020000);
#define COMMON_SETUP() \
  lptr xrd[4], wr0[4], wr1[4]; \
  _Pragma("unroll") for (int s = 0; s < 4; ++s) { xrd[s] = lds + (t * 32 + r32) * 128 + (((2 * (s ^ rot) + hh) ^ ((r32 >> 1) & 7)) << 4); \
    wr0[s] = wrd0 + ((q4 >> 1) << 6) + (s ^ rot) * 2048; wr1[s] = wrd0 + ((1 ^ (q4 >> 1)) << 6) + (s ^ rot) * 2048; } \
  const int xwrap = (t >> 1) ? -NSLOT * SLOT : 0; \
  bf16x8 sel; { const short one = ((lane & 15) == ((lane >> 4) & 1)) ? (short)0x3f80 : (short)0; sel = (bf16x8){one, one, one, one, one, one, one, one}; } \
  bf16x8 xa[28]; float rs, nmb
#define GET_TAG() PIN(tagraw); const u64 tag = (((u64)(unsigned)__builtin_amdgcn_readfirstlane((unsigned)(tagraw >> 32)) << 32) | (unsigned)__builtin_amdgcn_readfirstlane((unsigned)tagraw)) + 1

  if (c == 0) {
    unsigned cpf0, cpf1;
    { asm volatile("global_load_dword %0, %1, %2" : "=v"(cpf0) : "v"(min((unsigned)tid * 128u, (unsigned)CODE_BYTES - 128u)), "s"(pc) : "memory");
      asm volatile("global_load_dword %0, %1, %2" : "=v"(cpf1) : "v"(min((unsigned)tid * 128u + 32768u, (unsigned)CODE_BYTES - 128u)), "s"(pc) : "memory"); }
    unsigned tch = 0;
    u64 tagraw = __hip_atomic_load((GAS u64*)(ws + WS_EPOCH), RLX_AGENT);
    if (tid < 16) *(LAS unsigned*)(lds + L_CNT + 4 * tid) = 0u;
    const bool isK = j < 7, isV = j >= 7 && j < 14;
    bf16x8 qf[4][2];
    {
      COMMON_SETUP();
      f32x16 a0 = {}, a1 = {}; f32x4 c0 = {}, c1 = {};
      if (isV) p1_compute<true, 0>(xa, a0, a1, c0, c1, sel, xrd, xwrap, wr0, wr1 STA); else p1_compute<false, 0>(xa, a0, a1, c0, c1, sel, xrd, xwrap, wr0, wr1 STA);
      STAMP(1);
      cs_put(lds, L_CS0, 0, t, lane, c0); cs_put(lds, L_CS0, 1, t, lane, c1);
      asm volatile("s_waitcnt vmcnt(0) lgkmcnt(0)\n\ts_barrier" : "+v"(cpf0), "+v"(cpf1), "+v"(tch) :: "memory");
      const float cl0 = cs_get(lds, L_CS0, 0, r32), cl1 = cs_get(lds, L_CS0, 1, r32);
      if (isV) {
        row_norm(lds, t, r32, rs, nmb);
        float rr[16], mm[16]; lane_to_regs(priv, r32, hh, rs, rr); lane_to_regs(priv + 128, r32, hh, nmb, mm);
        const int rho = r32 >> 2, col0 = (j - 7) * 64 + 4 * ((rho & 4) + 2 * (rho & 1) + ((rho >> 1) & 1)) + (r32 & 3);
#pragma unroll
        for (int cc = 0; cc < 2; ++cc) {
          const f32x16& a = cc ? a1 : a0; const float cl = cc ? cl1 : cl0; u32x4 o[2];
#pragma unroll
          for (int p = 0; p < 8; ++p) o[p >> 2][p & 3] = cvtpk(rr[2 * p] * __builtin_fmaf(mm[2 * p], cl, a[2 * p]), rr[2 * p + 1] * __builtin_fmaf(mm[2 * p + 1], cl, a[2 * p + 1]));
          const int col = col0 + cc * 32, hd = col / 28, d = col - hd * 28;
          const unsigned ad = WS_KV + hd * 262144 + rt * 4096 + 2048 + (hh * 32 + d) * 16;
          ST16(o[0], ad); ST16(o[1], ad + 1024);
          if (d >= 24) { const unsigned e = d == 24 ? 0x3f803f80u : 0u; const u32x4 pad = {e, e, e, e}; ST16(pad, ad + 64); ST16(pad, ad + 1024 + 64); }
        }
      } else {
        float cr0[16], cr1[16]; lane_to_regs(priv, r32, hh, cl0, cr0); lane_to_regs(priv + 128, r32, hh, cl1, cr1);
        row_norm(lds, t, r32, rs, nmb);
        u32x4 o[4];
        if (isK) {
          epi_T<false>(a0, cr0, rs, nmb, *(u32x4(*)[2])&o[0]); epi_T<false>(a1, cr1, rs, nmb, *(u32x4(*)[2])&o[2]);
#pragma unroll
          for (int s = 0; s < 4; ++s) {
            const int P = j * 8 + 2 * s + hh, hp = P / 7, rr = P - hp * 7;
            const int hd = 2 * hp + (rr > 3), sg = rr > 3 ? rr - 4 : rr;
            const unsigned ad = WS_KV + hd * 262144 + rt * 4096 + sg * 512 + r32 * 16;
            u32x4 v = o[s]; if (rr == 3) { v[2] = 0x3f803f80u; v[3] = 0x00003f80u; }
            ST16(v, ad);
            if (rr == 3) { const u32x4 e = {o[s][2], o[s][3], 0x3f803f80u, 0x00003f80u}; ST16(e, ad + 262144); }
          }
        } else {
          epi_T<true>(a0, cr0, rs, nmb, *(u32x4(*)[2])&o[0]); epi_T<true>(a1, cr1, rs, nmb, *(u32x4(*)[2])&o[2]);
          const unsigned ad = WS_H + rt * 114688 + (j - 14) * 4096 + lane * 16;
#pragma unroll
          for (int s = 0; s < 4; ++s) ST16(o[s], ad + s * 1024);
        }
      }
      STAMP(2);
      const int rho = 4 * g16 + p4, sg = 2 * (rho >> 2) + (rho & 1), half = (rho >> 1) & 1;
      const int g = (j & 1) ? (sg < 3 ? 2 * sg + 1 + half : (half ? 7 : 0)) : 2 * sg + half;
      lptr qb[4];
#pragma unroll
      for (int s = 0; s < 4; ++s) qb[s] = lds + L_WQ + (8 * hh + q4) * 64 + g * 8 + (s ^ rot) * 1024;
      a0 = (f32x16){}; c0 = (f32x4){};
      bf16x8 wf[3];
      wf[0] = trfrag(qb[0], 256); wf[1] = trfrag(qb[1], 256);
#pragma unroll
      for (int st = 0; st < 28; ++st) {
        if (st + 2 < 28) wf[(st + 2) % 3] = trfrag(qb[(st + 2) & 3] + ((st + 2) >> 2) * 4096, 256);
        SBAR();
        a0 = MFMA32(wf[st % 3], xa[st], a0);
        c0 = MFMA16(sel, wf[st % 3], c0);
        PIN(a0); PIN(c0); SBAR();
      }
      if (lane < 16) { *(LAS float*)(priv + lane * 4) = c0[0]; *(LAS float*)(priv + 64 + lane * 4) = c0[1]; }
      float cr[16];
#pragma unroll
      for (int i = 0; i < 4; ++i) { const f32x4 q = ldsf4(priv + (8 * i + 4 * hh) * 4); cr[4 * i] = q[0]; cr[4 * i + 1] = q[1]; cr[4 * i + 2] = q[2]; cr[4 * i + 3] = q[3]; }
      u32x4 o[2]; epi_T<false>(a0, cr, rs * QSCALE, nmb, o);
      if (hh) { o[1][2] = 0u; o[1][3] = 0u; }
      *(LAS u32x4*)(lds + L_QX + t * 2048 + lane * 16) = o[0]; *(LAS u32x4*)(lds + L_QX + t * 2048 + 1024 + lane * 16) = o[1];
    }
    cnt_add(lds, C_Q);
    STAMP(3);
    GET_TAG();
    WAIT_VM(0);
    if (cnt_add(lds, C_KV) == 3u && lane == 0) __hip_atomic_store((GAS u64*)(ws + WS_FKV + fme), tag, RLX_AGENT);
    if (cnt_add(lds, C_H) == 7u && lane == 0) __hip_atomic_store((GAS u64*)(ws + WS_FH + fme), tag, RLX_AGENT);
    STAMP(4);
    { const int c0 = (28 * j) >> 6, c1 = (28 * j + 27) >> 6, sel4 = lane >> 4;
      int jj = sel4 == 0 ? c0 : sel4 == 1 ? c1 : sel4 == 2 ? 7 + c0 : 7 + c1;
      if (j == 0 && (sel4 & 1)) jj = 14 + (sel4 >> 1);
      poll_flags(ws + WS_FKV + ((lane & 15) * 16 + jj) * 128, tag); }
    STAMP(5);
    const unsigned kvb = WS_KV + j * 262144 + t * 65536 + lane * 16;
    u32x4 kv[4][4];
#pragma unroll
    for (int u = 0; u < 3; ++u)
#pragma unroll
      for (int e = 0; e < 4; ++e) kv[u][e] = LD16(kvb + u * 4096 + e * 1024);
    cnt_wait(lds, C_Q, 4u);
#pragma unroll
    for (int q = 0; q < 4; ++q) { const lptr a = lds + L_QX + ((q + t) & 3) * 2048 + lane * 16; qf[q][0] = ldsb128(a); qf[q][1] = ldsb128(a + 1024); }
    cnt_add(lds, C_QR);
    const f32x16 zero = {};
    f32x16 o[4]; float mref[4]; bool redo = false;
#pragma unroll
    for (int q = 0; q < 4; ++q) {
      f32x16 s = MFMA32(asfrag(kv[0][0]), qf[q][0], zero); s = MFMA32(asfrag(kv[0][1]), qf[q][1], s);
      mref[q] = __builtin_ceilf(tile_max(s)) + REF_MARGIN; redo |= !(__builtin_fabsf(mref[q]) < 16777216.f);
      set_ref(qf[q][1], mref[q], hh); o[q] = zero;
    }
    STAMP(6);
    __builtin_amdgcn_s_setprio(2);
    f32x16 sA, sB; u32x4 pA0 = {}, pA1 = {}, pB0 = {}, pB1 = {};
    sA = MFMA32(asfrag(kv[0][0]), qf[0][0], zero); sA = MFMA32(asfrag(kv[0][1]), qf[0][1], sA);
    kv[3][2] = (u32x4){}; kv[3][3] = (u32x4){}; if (ABL_NOKV) { kv[3][0] = kv[0][0]; kv[3][1] = kv[0][1]; }
#define EX4(S, i) do { S[i] = __builtin_amdgcn_exp2f(S[i]); S[i + 1] = __builtin_amdgcn_exp2f(S[i + 1]); S[i + 2] = __builtin_amdgcn_exp2f(S[i + 2]); S[i + 3] = __builtin_amdgcn_exp2f(S[i + 3]); } while (0)
#define UNIT(SC, SN, PC0, PC1, PP0, PP1, KN0, KN1, QN, QP, VV0, VV1) do { \
      SBAR(); SN = MFMA32(asfrag(KN0), qf[QN][0], zero); PIN(SN); SBAR(); EX4(SC, 0); PIN(SC); SBAR(); \
      SN = MFMA32(asfrag(KN1), qf[QN][1], SN); PIN(SN); SBAR(); EX4(SC, 4); PIN(SC); SBAR(); \
      o[QP] = MFMA32(asfrag(VV0), asfrag(PP0), o[QP]); PIN(o[QP]); SBAR(); EX4(SC, 8); PIN(SC); SBAR(); \
      o[QP] = MFMA32(asfrag(VV1), asfrag(PP1), o[QP]); PIN(o[QP]); SBAR(); EX4(SC, 12); SBAR(); \
      PC0 = (u32x4){cvtpk(SC[0], SC[1]), cvtpk(SC[2], SC[3]), cvtpk(SC[4], SC[5]), cvtpk(SC[6], SC[7])}; \
      PC1 = (u32x4){cvtpk(SC[8], SC[9]), cvtpk(SC[10], SC[11]), cvtpk(SC[12], SC[13]), cvtpk(SC[14], SC[15])}; PIN(PC0); PIN(PC1); SBAR(); } while (0)
#define KEYTILE(U, UN, UL) do { \
      UNIT(sA, sB, pA0, pA1, pB0, pB1, kv[U][0], kv[U][1], 1, 3, kv[UL][2], kv[UL][3]); \
      if (!ABL_NOKV) { const int nt = min(kt + (U) + 3, 15) * 4096; _Pragma("unroll") for (int e = 0; e < 4; ++e) kv[UL][e] = __builtin_amdgcn_raw_buffer_load_b128(rsw, kvb + e * 1024, nt, LDAUX); } \
      UNIT(sB, sA, pB0, pB1, pA0, pA1, kv[U][0], kv[U][1], 2, 0, kv[U][2], kv[U][3]); \
      UNIT(sA, sB, pA0, pA1, pB0, pB1, kv[U][0], kv[U][1], 3, 1, kv[U][2], kv[U][3]); \
      UNIT(sB, sA, pB0, pB1, pA0, pA1, kv[UN][0], kv[UN][1], 0, 2, kv[U][2], kv[U][3]); \
    } while (0)
    for (int kt = 0; kt < 16; kt += 4) {
      KEYTILE(0, 1, 3); KEYTILE(1, 2, 0); KEYTILE(2, 3, 1); KEYTILE(3, 0, 2);
      const float lm = __builtin_fmaxf(__builtin_fmaxf(o[0][12], o[1][12]), __builtin_fmaxf(o[2][12], o[3][12]));
      if (__builtin_expect((__ballot(lm > 16777216.f) >> 32) != 0, 0)) {
        NRESC;
        o[3] = MFMA32(asfrag(kv[3][2]), asfrag(pB0), o[3]); o[3] = MFMA32(asfrag(kv[3][3]), asfrag(pB1), o[3]); pB0 = (u32x4){}; pB1 = (u32x4){};
#pragma unroll
        for (int q = 0; q < 4; ++q) { const float l = upper_bcast(o[q][12]); redo |= !(l < 1.2980742146337069e33f);
          const int e = max(__builtin_amdgcn_frexp_expf(l) + (int)REF_MARGIN, 0);
#pragma unroll
          for (int r = 0; r < 16; ++r) o[q][r] = __builtin_ldexpf(o[q][r], -e);
          mref[q] += (float)e; redo |= !(__builtin_fabsf(mref[q]) < 16777216.f); set_ref(qf[q][1], mref[q], hh);
          if (q == 0) {
#pragma unroll
            for (int r = 0; r < 16; ++r) sA[r] -= (float)e; } } }
    }
    cnt_add(lds, C_DONE);
    o[3] = MFMA32(asfrag(kv[3][2]), asfrag(pB0), o[3]); o[3] = MFMA32(asfrag(kv[3][3]), asfrag(pB1), o[3]);
    { float z[4] = {0.f, 0.f, 0.f, 0.f};
#pragma unroll
      for (int r = 0; r < 16; ++r)
#pragma unroll
        for (int q = 0; q < 4; ++q) z[q] = __builtin_fmaf(o[q][r], 0.f, z[q]);
      const float zz = (z[0] + z[1]) + (z[2] + z[3]); redo |= !(zz == 0.f); }
    if (__builtin_expect(__any(redo), 0)) {
#pragma unroll
      for (int q = 0; q < 4; ++q) { set_ref(qf[q][1], 0.f, hh); o[q] = zero; mref[q] = -3.0e38f; }
      for (int kt = 0; kt < 16; ++kt) {
        const u32x4 k0 = LD16(kvb + kt * 4096), k1 = LD16(kvb + kt * 4096 + 1024), v0 = LD16(kvb + kt * 4096 + 2048), v1 = LD16(kvb + kt * 4096 + 3072);
#pragma unroll
        for (int q = 0; q < 4; ++q) {
          f32x16 s = MFMA32(asfrag(k0), qf[q][0], zero); s = MFMA32(asfrag(k1), qf[q][1], s);
          const float mn = __builtin_fmaxf(mref[q], tile_max(s)), al = __builtin_amdgcn_exp2f(mref[q] - mn); mref[q] = mn;
#pragma unroll
          for (int r = 0; r < 16; ++r) { o[q][r] *= al; s[r] = __builtin_amdgcn_exp2f(s[r] - mn); }
          const u32x4 p0 = {cvtpk(s[0], s[1]), cvtpk(s[2], s[3]), cvtpk(s[4], s[5]), cvtpk(s[6], s[7])}, p1 = {cvtpk(s[8], s[9]), cvtpk(s[10], s[11]), cvtpk(s[12], s[13]), cvtpk(s[14], s[15])};
          o[q] = MFMA32(asfrag(v0), asfrag(p0), o[q]); o[q] = MFMA32(asfrag(v1), asfrag(p1), o[q]);
        }
      }
    }
    STAMP(7);
    VSTAMP(28, nresc + (__any(redo) ? 1000 : 0));
    cnt_wait(lds, C_QR, 4u); cnt_wait(lds, C_H, 8u);
    auto area = [&](int dst, int e) -> lptr { return lds + (dst * 3 + e) * 4096 + lane * 16; };
#pragma unroll
    for (int q = 1; q < 4; ++q) {
      const lptr a = area((q + t) & 3, q - 1);
      o[q][13] = hh ? mref[q] : o[q][13];
#pragma unroll
      for (int i = 0; i < 4; ++i) *(LAS f32x4*)(a + i * 1024) = (f32x4){o[q][4 * i], o[q][4 * i + 1], o[q][4 * i + 2], o[q][4 * i + 3]};
    }
    f32x16 of = o[0]; const float mown = mref[0];
    cnt_add(lds, C_CMB); STAMP(9); cnt_wait(lds, C_CMB, 4u);
    { f32x4 pp[3][4]; float ms[3];
#pragma unroll
      for (int e = 0; e < 3; ++e) { const lptr a = area(t, e);
#pragma unroll
        for (int i = 0; i < 4; ++i) pp[e][i] = ldsf4(a + i * 1024);
        ms[e] = upper_bcast(pp[e][3][1]); }
      const float mx = __builtin_fmaxf(__builtin_fmaxf(mown, ms[0]), __builtin_fmaxf(ms[1], ms[2]));
      const float f0 = __builtin_amdgcn_exp2f(mown - mx - 2.f), f1 = __builtin_amdgcn_exp2f(ms[0] - mx - 2.f), f2 = __builtin_amdgcn_exp2f(ms[1] - mx - 2.f), f3 = __builtin_amdgcn_exp2f(ms[2] - mx - 2.f);
#pragma unroll
      for (int r = 0; r < 16; ++r) of[r] = of[r] * f0 + pp[0][r >> 2][r & 3] * f1 + pp[1][r >> 2][r & 3] * f2 + pp[2][r >> 2][r & 3] * f3;
      const float inv = __builtin_amdgcn_rcpf(upper_bcast(of[12]));
      u32x4 e0, e1;
#pragma unroll
      for (int p = 0; p < 4; ++p) { e0[p] = cvtpk(of[2 * p] * inv, of[2 * p + 1] * inv); e1[p] = cvtpk(of[8 + 2 * p] * inv, of[9 + 2 * p] * inv); }
      if (hh) { e1[2] = 0u; e1[3] = 0u; }
      const unsigned a = WS_O + rt * 32768 + j * 2048 + lane * 16;
      ST16(e0, a); ST16(e1, a + 1024); }
    STAMP(10);
    WAIT_VM(0);
    if (cnt_add(lds, C_O) == 3u && lane == 0) __hip_atomic_store((GAS u64*)(ws + WS_FO + fme), tag, RLX_AGENT);
    STAMP(8); CSTAMP(31); STAMP_OUT();
  } else {
    Ld k;
    { const int row = 16 * t + (lane >> 3); const unsigned piece = ((lane & 7) ^ (((row >> 1) & 1) << 2)) << 4;
      k.xsrc = x + rb * 128 * 896; k.w0rs = j < 14 ? 2688u : 3584u; k.w0src = j < 14 ? wqkv + 896 + j * 128 : w1 + (j - 14) * 128;
      k.fasrc = w1 + (2 + j) * 128; k.fbsrc = w1 + (12 + j) * 128; k.dst = lds0 + t * 2048;
      k.w0vo = row * k.w0rs + piece; k.wfvo = row * 3584 + piece;
      k.wqsrc = wqkv + j * 56; k.wqvo = (32 * t + (lane >> 2)) * 2688 + (lane & 3) * 16; k.wq3 = t < 2 ? 384 * 2688 : 0;
      k.xvo0 = row * 896 + (((lane & 7) ^ (lane >> 4)) << 4); k.xvo1 = k.xvo0 ^ 64u; }
    u64 tagraw;
    {
      ld_items<0, PRO>(k);
      tagraw = __hip_atomic_load((GAS u64*)(ws + WS_EPOCH), RLX_AGENT);
      COMMON_SETUP();
      f32x4 s1 = {}, s2 = {};
      p1_load<0>(k, xa, s1, s2, xrd, xwrap STA);
      STAMP(1);
      { const float q = (lane & 2) ? ((lane & 1) ? s2[3] : s2[2]) : ((lane & 1) ? s2[1] : s2[0]);
        const float a = swap_add(s1[0]), bb = swap_add(q); if (hh == 0) *(LAS f32x2*)(lds + L_STAT + (t * 32 + r32) * 8) = (f32x2){a, bb}; }
      asm volatile("s_waitcnt vmcnt(%0) lgkmcnt(0)\n\ts_barrier" :: "i"(2 * (n_issued(6) - 28)) : "memory");
      row_norm(lds, t, r32, rs, nmb);
#pragma unroll
      for (int s = 0; s < 4; ++s) { wr0[s] += L_FA; wr1[s] += L_FA; PIN(wr0[s]); PIN(wr1[s]); }
      bf16x8 f0[2], f1[2]; f32x16 a0 = {}, a1 = {}; f32x4 c0 = {}, c1 = {}; float cr[16]; u32x4 o[2];
      f0[0] = trfrag(wr0[0], 512); f1[0] = trfrag(wr1[0], 512);
      f_steps<0, 0>(k, lds, xa, f0, f1, a0, a1, c0, c1, sel, wr0, wr1);
      cs_put(lds, L_CSA, 0, t, lane, c0); cs_put(lds, L_CSA, 1, t, lane, c1);
      WAIT_VM(0); cnt_add(lds, C_CSA); cnt_wait(lds, C_CSA, 4u);
      STAMP(2);
      { const unsigned ad = WS_H + rt * 114688 + (2 + j) * 4096 + lane * 16;
        lane_to_regs(priv, r32, hh, cs_get(lds, L_CSA, 0, r32), cr); epi_T<true>(a0, cr, rs, nmb, o); ST16(o[0], ad); ST16(o[1], ad + 1024);
        lane_to_regs(priv + 128, r32, hh, cs_get(lds, L_CSA, 1, r32), cr); epi_T<true>(a1, cr, rs, nmb, o); ST16(o[0], ad + 2048); ST16(o[1], ad + 3072); }
      STAMP(3);
      if (j >= 6) {
#pragma unroll
        for (int s = 0; s < 4; ++s) { wr0[s] += L_FB - L_FA; wr1[s] += L_FB - L_FA; PIN(wr0[s]); PIN(wr1[s]); }
        a0 = (f32x16){}; a1 = (f32x16){}; c0 = (f32x4){}; c1 = (f32x4){};
        f0[0] = trfrag(wr0[0], 512); f1[0] = trfrag(wr1[0], 512);
        f_steps<1, 0>(k, lds, xa, f0, f1, a0, a1, c0, c1, sel, wr0, wr1);
        cs_put(lds, L_CSB, 0, t, lane, c0); cs_put(lds, L_CSB, 1, t, lane, c1);
        cnt_add(lds, C_CSB); cnt_wait(lds, C_CSB, 4u);
        const unsigned ad = WS_H + rt * 114688 + (12 + j) * 4096 + lane * 16;
        lane_to_regs(priv, r32, hh, cs_get(lds, L_CSB, 0, r32), cr); epi_T<true>(a0, cr, rs, nmb, o); ST16(o[0], ad); ST16(o[1], ad + 1024);
        lane_to_regs(priv + 128, r32, hh, cs_get(lds, L_CSB, 1, r32), cr); epi_T<true>(a1, cr, rs, nmb, o); ST16(o[0], ad + 2048); ST16(o[1], ad + 3072);
      }
    }
    GET_TAG();
    WAIT_VM(0);
    if (cnt_add(lds, C_H) == 7u && lane == 0) __hip_atomic_store((GAS u64*)(ws + WS_FH + fme), tag, RLX_AGENT);
    STAMP(4);
    const int cg = bi >> 2;
    if (cg < 7) {
      const int rt0 = 8 * xg + 2 * (bi & 3), rb3 = rt0 >> 2;
      const int ring = L_RING + 3 * t * SLOT;
      const int ct = t >> 1, rtl = t & 1;
      G2 g; g.lds = lds; g.t = t; g.ringdst = lds0 + ring; g.lv = lane * 16;
      g.w2vo = (lane >> 3) * 896 + (((lane & 7) ^ (((lane >> 4) & 1) << 2)) << 4);
      g.w2s = w2 + t * 448 * 896 + cg * 128; g.wos = wo + t * 112 * 896 + cg * 128;
      g.hb0 = ws + WS_H + (rt0 + rtl) * 114688 + t * 28672; g.hb1 = ws + WS_H + (rt0 + (rtl ^ 1)) * 114688 + t * 28672;
      g.br0 = wrd0 + ring + ((ct ^ (q4 >> 1)) << 6); g.br1 = wrd0 + ring + ((ct ^ 1 ^ (q4 >> 1)) << 6);
      g2_ws<0, 12>(g);
      const int fb3 = rb3 * 16 + (lane & 15);
      poll_flags(ws + WS_FH + fb3 * 128, tag);
      WAIT_VM(0);
      STAMP(5);
      u32x4 hf[14][2];
#pragma unroll
      for (int s = 0; s < 14; ++s) { ldg_sc1(hf[s][0], g.hb0 + s * 1024, g.lv); ldg_sc1(hf[s][1], g.hb1 + s * 1024, g.lv); }
      f32x16 a00 = {}, a01 = {}, a10 = {}, a11 = {};
      PIN(g.br0); PIN(g.br1);
      { bf16x8 f0[2], f1[2]; f0[0] = trfrag(g.br0, 512); f1[0] = trfrag(g.br1, 512);
        g2_steps<0>(g, hf, f0, f1, a00, a01, a10, a11); }
      STAMP(6);
      WAIT_VM(0);
      bf16x8 wf[8][2];
      { const lptr o0 = lds + ring + SLOT + (4 * hh + q4) * 128 + pi_std * 8;
#pragma unroll
        for (int e = 0; e < 8; ++e)
#pragma unroll
          for (int cc = 0; cc < 2; ++cc) wf[e][cc] = trfrag(o0 + ((cc ^ ct ^ (q4 >> 1)) << 6) + ((e >> 1) * 28 + (e & 1) * 16) * 128, 1024); }
      const size_t oo = (size_t)((rt0 + rtl) * 32 + r32) * 896 + cg * 128 + ct * 64 + hh * 16;
      const u32x4 xr0 = *(const GAS u32x4*)(x + oo), xr1 = *(const GAS u32x4*)(x + oo + 32);
      poll_flags(ws + WS_FO + fb3 * 128, tag);
      STAMP(7);
      { const unsigned ob0 = WS_O + (rt0 + rtl) * 32768 + t * 8192 + lane * 16, ob1 = ob0 ^ 32768u;
        u32x4 of[8][2];
#pragma unroll
        for (int e = 0; e < 8; ++e) { of[e][0] = LD16(ob0 + e * 1024); of[e][1] = LD16(ob1 + e * 1024); }
        SBAR();
#pragma unroll
        for (int e = 0; e < 8; ++e) { const bf16x8 h0 = asfrag(of[e][0]), h1 = asfrag(of[e][1]);
          a11 = MFMA32(wf[e][1], h1, a11); a10 = MFMA32(wf[e][1], h0, a10); a01 = MFMA32(wf[e][0], h1, a01); a00 = MFMA32(wf[e][0], h0, a00);
          PIN(a00); PIN(a01); PIN(a10); PIN(a11); } }
      STAMP(9);
      if (b == 0 && tid == 256) __hip_atomic_store((GAS u64*)(ws + WS_EPOCH), tag, RLX_AGENT);
      { const lptr mine = lds + ring + lane * 16;
        auto put = [&](const f32x16& a, int e) {
#pragma unroll
          for (int i = 0; i < 4; ++i) *(LAS f32x4*)(mine + e * 4096 + i * 1024) = (f32x4){a[4 * i], a[4 * i + 1], a[4 * i + 2], a[4 * i + 3]}; };
        put(a11, 3); put(a10, 2); put(a01, 1); }
      f32x16 fin = a00;
      cnt_add(lds, C_RED); STAMP(10); cnt_wait(lds, C_RED, 4u);
#pragma unroll
      for (int e = 1; e < 4; ++e) { const lptr a = lds + L_RING + 3 * (t ^ e) * SLOT + e * 4096 + lane * 16;
#pragma unroll
        for (int i = 0; i < 4; ++i) { const f32x4 v = ldsf4(a + i * 1024); fin[4 * i] += v[0]; fin[4 * i + 1] += v[1]; fin[4 * i + 2] += v[2]; fin[4 * i + 3] += v[3]; } }
      u32x4 r0, r1;
#pragma unroll
      for (int p = 0; p < 4; ++p) { r0[p] = cvtpk(fin[2 * p] + bflo(xr0[p]), fin[2 * p + 1] + bfhi(xr0[p])); r1[p] = cvtpk(fin[8 + 2 * p] + bflo(xr1[p]), fin[9 + 2 * p] + bfhi(xr1[p])); }
      *(GAS u32x4*)(out + oo) = r0; *(GAS u32x4*)(out + oo + 32) = r1;
      STAMP(8);
    }
    STAMP_OUT();
  }
}

extern "C" void kernel_launch(void* const* d_in, const int* in_sizes, int n_in, void* d_out, int out_size, void* d_ws, size_t ws_size, hipStream_t stream) {
  fused_block<<<256, 512, 0, stream>>>((const char*)d_in[0], (const char*)d_in[1], (const char*)d_in[2], (const char*)d_in[4], (const char*)d_in[5], (char*)d_out, (char*)d_ws + WS_BASE);
}
```

```cpp
#include <hip/hip_runtime.h>
#include <stdint.h>

typedef short bf16x8 __attribute__((ext_vector_type(8)));
typedef short s16x4 __attribute__((ext_vector_type(4)));
typedef float f32x16 __attribute__((ext_vector_type(16)));
typedef float f32x4 __attribute__((ext_vector_type(4)));
typedef float f32x2 __attribute__((ext_vector_type(2)));
typedef __bf16 bf16x2v __attribute__((ext_vector_type(2)));
typedef unsigned u32x4 __attribute__((ext_vector_type(4)));
typedef unsigned long long u64;
#define LAS __attribute__((address_space(3)))
#define GAS __attribute__((address_space(1)))
#define DI __device__ __forceinline__
#define MFMA32(a, b, c) __builtin_amdgcn_mfma_f32_32x32x16_bf16(a, b, c, 0, 0, 0)
#define MFMA16(a, b, c) __builtin_amdgcn_mfma_f32_16x16x32_bf16(a, b, c, 0, 0, 0)
#define SBAR() __builtin_amdgcn_sched_barrier(0)
#define PIN(x) asm volatile("" : "+v"(x))
#define RLX_AGENT __ATOMIC_RELAXED, __HIP_MEMORY_SCOPE_AGENT
#define WAIT_BAR(N) asm volatile("s_waitcnt vmcnt(" #N ") lgkmcnt(0)\n\ts_barrier" ::: "memory")
#define WAIT_VM(N) asm volatile("s_waitcnt vmcnt(" #N ")" ::: "memory")
#define WAIT_LGKM0() asm volatile("s_waitcnt lgkmcnt(0)" ::: "memory")

constexpr unsigned WS_EPOCH = 0;
constexpr unsigned WS_FKV = 4096, WS_FH = WS_FKV + 32768, WS_FO = WS_FH + 32768;
constexpr unsigned WS_KV = 1u << 20;
constexpr unsigned WS_H = 5u << 20;
constexpr unsigned WS_O = 13u << 20;
constexpr unsigned WS_BYTES = 15u << 20;
constexpr int SLOT = 8192, NSLOT = 19;
constexpr int L_MISC = NSLOT * SLOT;
constexpr int L_STAT = L_MISC;
constexpr int L_CS0 = L_MISC + 2048, L_CSA = L_CS0 + 1024, L_CSB = L_CSA + 1024;
constexpr int L_PRIV = L_MISC + 5120;
constexpr int L_CNT = L_MISC + 7168;
constexpr int LDS_BYTES = 163840;
constexpr int L_WQ = 2 * SLOT, L_FA = 6 * SLOT, L_FB = 13 * SLOT, L_QX = SLOT, L_RING = 6 * SLOT;
constexpr int p1_slot(int item) { return (item % NSLOT) * SLOT; }
enum { C_CSA, C_CSB, C_Q, C_QR, C_KV, C_H, C_F, C_DONE, C_CMB, C_O, C_RED };

#define WS_BASE 0
#define TOUCH 0
#define MAPA 8
#define G2_SPLIT 16
#define CODE_BYTES 49152
constexpr float QSCALE = 0.18898223650461362f * 1.4426950408889634f;
constexpr float REF_MARGIN = 40.f;
constexpr unsigned SPIN_CAP = 1u << 16;

DI unsigned cvtpk(float lo, float hi) { f32x2 v = {lo, hi}; return __builtin_bit_cast(unsigned, __builtin_convertvector(v, bf16x2v)); }
DI float bf16r(float x) { return __uint_as_float(cvtpk(x, 0.f) << 16); }
DI float bflo(unsigned v) { return __uint_as_float(v << 16); }
DI float bfhi(unsigned v) { return __uint_as_float(v & 0xffff0000u); }
DI float swap_add(float v) { auto r = __builtin_amdgcn_permlane32_swap(__float_as_uint(v), __float_as_uint(v), false, false); return __uint_as_float(r[0]) + __uint_as_float(r[1]); }
DI float swap_max(float v) { auto r = __builtin_amdgcn_permlane32_swap(__float_as_uint(v), __float_as_uint(v), false, false); return __builtin_fmaxf(__uint_as_float(r[0]), __uint_as_float(r[1])); }
DI float upper_bcast(float v) { auto r = __builtin_amdgcn_permlane32_swap(__float_as_uint(v), __float_as_uint(v), false, false); return __uint_as_float(r[1]); }
DI void glds16(const void* sbase, unsigned voff, unsigned ldsdst) {
  unsigned keep;
  asm volatile("s_mov_b32 %0, m0\n\ts_mov_b32 m0, %3\n\ts_nop 0\n\tglobal_load_lds_dwordx4 %1, %2\n\ts_mov_b32 m0, %0"
               : "=&s"(keep) : "v"(voff), "s"(sbase), "s"(ldsdst) : "memory");
}
typedef LAS char* lptr;
typedef short v4i16_t __attribute__((ext_vector_type(4)));
DI s16x4 trd(lptr p) { return __builtin_bit_cast(s16x4, __builtin_amdgcn_ds_read_tr16_b64_v4i16((LAS v4i16_t*)p)); }
DI bf16x8 trfrag(lptr p, int second) { s16x4 lo = trd(p), hi = trd(p + second); return __builtin_shufflevector(lo, hi, 0, 1, 2, 3, 4, 5, 6, 7); }
DI bf16x8 ldsb128(lptr p) { return *(LAS bf16x8*)p; }
DI f32x4 ldsf4(lptr p) { return *(LAS f32x4*)p; }
DI bf16x8 asfrag(u32x4 v) { return __builtin_bit_cast(bf16x8, v); }
DI unsigned cnt_add(lptr lds, int c) { unsigned r = 0; WAIT_LGKM0(); if ((threadIdx.x & 63) == 0) r = __hip_atomic_fetch_add((LAS unsigned*)(lds + L_CNT + 4 * c), 1u, __ATOMIC_RELAXED, __HIP_MEMORY_SCOPE_WORKGROUP);
  return __builtin_amdgcn_readfirstlane(r); }
DI void cnt_wait(lptr lds, int c, unsigned n) {
  while (__builtin_amdgcn_readfirstlane(__hip_atomic_load((LAS unsigned*)(lds + L_CNT + 4 * c), __ATOMIC_RELAXED, __HIP_MEMORY_SCOPE_WORKGROUP)) < n) __builtin_amdgcn_s_sleep(1);
  asm volatile("" ::: "memory"); }
DI void poll_flags(const char* flag, u64 tag) {
#pragma nounroll
  for (unsigned s = 0; s < SPIN_CAP; ++s) {
    const u64 v = __hip_atomic_load((GAS u64*)flag, RLX_AGENT);
    if (__all(v == tag)) break;
#define POLLSLEEP 1
    __builtin_amdgcn_s_sleep(POLLSLEEP);
  }
  __builtin_amdgcn_fence(__ATOMIC_ACQUIRE, "wavefront");
  asm volatile("" ::: "memory");
}
#define STAMP(i) do {} while (0)
#define CSTAMP(i) do {} while (0)
#define VSTAMP(i, v) do {} while (0)
#define STAMP_OUT() do {} while (0)
#define NRESC
#define STV
#define STA
#define BAR() asm volatile("s_waitcnt lgkmcnt(0)\n\ts_barrier" ::: "memory")

#define PRO 9
#define RAMP 3
constexpr int n_issued(int c) { return c < 0 ? PRO : (PRO + RAMP * (c + 1) < 19 + 3 * c ? PRO + RAMP * (c + 1) : 19 + 3 * c); }
static_assert(n_issued(6) >= 29, "Wq and the head of F_a must be in flight at B1");
#define MFMA4(a, b, c) __builtin_amdgcn_mfma_f32_4x4x4bf16_1k(a, b, c, 0, 0, 0)
DI void rowstats(const bf16x8& f, const s16x4& ones, f32x4& s1, f32x4& s2) {
  const s16x4 lo = __builtin_shufflevector(f, f, 0, 1, 2, 3), hi = __builtin_shufflevector(f, f, 4, 5, 6, 7);
  s1 = MFMA4(ones, lo, s1); s2 = MFMA4(lo, lo, s2); s1 = MFMA4(ones, hi, s1); s2 = MFMA4(hi, hi, s2);
}
template <int C> DI void x_frags(bf16x8 (&xa)[28], const lptr (&xrd)[4], int xwrap) {
#pragma unroll
  for (int s = 0; s < 4; ++s) xa[4 * C + s] = ldsb128(xrd[s] + p1_slot(3 * C) + (C == 6 ? xwrap : 0));
}
template <bool VT, int C> DI void p1_compute(bf16x8 (&xa)[28], f32x16& a0, f32x16& a1, f32x4& c0, f32x4& c1, const bf16x8& sel,
                                             const lptr (&xrd)[4], int xwrap, const lptr (&w0)[4], const lptr (&w1)[4] STV) {
  if constexpr (C < 7) {
    BAR();
    if (C == 0) STAMP(12);
    bf16x8 f0[4], f1[4];
#pragma unroll
    for (int s = 0; s < 4; ++s) {
      xa[4 * C + s] = ldsb128(xrd[s] + p1_slot(3 * C) + (C == 6 ? xwrap : 0));
      f0[s] = trfrag(w0[s] + p1_slot(3 * C + 2), 512); f1[s] = trfrag(w1[s] + p1_slot(3 * C + 2), 512);
    }
#pragma unroll
    for (int s = 0; s < 4; ++s) {
      if constexpr (VT) { a0 = MFMA32(xa[4 * C + s], f0[s], a0); a1 = MFMA32(xa[4 * C + s], f1[s], a1); }
      else { a0 = MFMA32(f0[s], xa[4 * C + s], a0); a1 = MFMA32(f1[s], xa[4 * C + s], a1); }
      if (s == 0) { c0 = MFMA16(sel, f0[0], c0); c1 = MFMA16(sel, f1[0], c1); }
    }
    PIN(a0); PIN(a1); PIN(c0); PIN(c1);
    p1_compute<VT, C + 1>(xa, a0, a1, c0, c1, sel, xrd, xwrap, w0, w1 STA);
  }
}
struct Ld { const char *xsrc, *w0src, *wqsrc, *fasrc, *fbsrc; unsigned xvo0, xvo1, w0vo, wfvo, wqvo, wq3, w0rs, dst; };
template <int N, int E> DI void ld_piece(const Ld& k) {
  const unsigned dst = k.dst + p1_slot(N) + E * 1024;
  if constexpr (N < 21) {
    constexpr int c = N / 3, q = N % 3;
    if constexpr (q < 2) glds16(k.xsrc + (q * 64 + E * 8) * 896 + c * 128, E ? k.xvo1 : k.xvo0, dst);
    else glds16(k.w0src + (size_t)(c * 64 + E * 8) * k.w0rs, k.w0vo, dst);
  } else if constexpr (N < 24) glds16(k.wqsrc + ((N - 21) * 128 + E * 16) * 2688, k.wqvo, dst);
  else if constexpr (N == 24) glds16(k.wqsrc + k.wq3 + E * 16 * 2688, k.wqvo, dst);
  else if constexpr (N < 32) glds16(k.fasrc + ((N - 25) * 64 + E * 8) * 3584, k.wfvo, dst);
  else glds16(k.fbsrc + ((N - 32) * 64 + E * 8) * 3584, k.wfvo, dst);
}
template <int A, int B> DI void ld_items(const Ld& k) { if constexpr (A < B) { ld_piece<A, 0>(k); ld_piece<A, 1>(k); ld_items<A + 1, B>(k); } }
template <int C> DI void p1_load(const Ld& k, bf16x8 (&xa)[28], f32x4& s1, f32x4& s2, const lptr (&xrd)[4], int xwrap STV) {
  if constexpr (C < 7) {
    asm volatile("s_waitcnt vmcnt(%0) lgkmcnt(0)\n\ts_barrier" :: "i"(2 * (n_issued(C - 1) - 3 * (C + 1))) : "memory");
    if (C == 0) STAMP(12);
    x_frags<C>(xa, xrd, xwrap);
    SBAR(); ld_items<n_issued(C - 1), n_issued(C)>(k); SBAR();
    const s16x4 ones = {0x3f80, 0x3f80, 0x3f80, 0x3f80};
#pragma unroll
    for (int s = 0; s < 4; ++s) rowstats(xa[4 * C + s], ones, s1, s2);
    PIN(s1); PIN(s2);
    p1_load<C + 1>(k, xa, s1, s2, xrd, xwrap STA);
  }
}
DI void cs_put(lptr lds, int area, int c, int t, int lane, const f32x4& cs) {
  if (lane < 16) { *(LAS float*)(lds + area + c * 512 + lane * 16 + t * 4) = cs[0]; *(LAS float*)(lds + area + c * 512 + (lane + 16) * 16 + t * 4) = cs[1]; }
}
DI float cs_get(lptr lds, int area, int c, int r32) { const f32x4 p = ldsf4(lds + area + c * 512 + r32 * 16); return (p[0] + p[1]) + (p[2] + p[3]); }
DI void lane_to_regs(lptr priv, int r32, int hh, float v, float (&o)[16]) {
  *(LAS float*)(priv + r32 * 4) = v;
#pragma unroll
  for (int i = 0; i < 4; ++i) { const f32x4 q = ldsf4(priv + (8 * i + 4 * hh) * 4); o[4 * i] = q[0]; o[4 * i + 1] = q[1]; o[4 * i + 2] = q[2]; o[4 * i + 3] = q[3]; }
}
template <bool RELU> DI void epi_T(const f32x16& acc, const float (&cs)[16], float sc, float nmb, u32x4 (&o)[2]) {
#pragma unroll
  for (int p = 0; p < 8; ++p) {
    float a = sc * __builtin_fmaf(nmb, cs[2 * p], acc[2 * p]), b = sc * __builtin_fmaf(nmb, cs[2 * p + 1], acc[2 * p + 1]);
    if (RELU) { a = __builtin_fmaxf(a, 0.f); b = __builtin_fmaxf(b, 0.f); }
    o[p >> 2][p & 3] = cvtpk(a, b);
  }
}
#define ST16(v, off) __builtin_amdgcn_raw_buffer_store_b128(v, rsw, off, 0, 16)
#define ABL_NOKV 0
#define LDAUX 16
#define LD16(off) __builtin_amdgcn_raw_buffer_load_b128(rsw, off, 0, LDAUX)

DI void set_ref(bf16x8& q1, float M, int hh) {
  const float n = -M; const float h = __uint_as_float(__float_as_uint(n) & 0xffff0000u), r = n - h;
  const float m = __uint_as_float(__float_as_uint(r) & 0xffff0000u), l = r - m;
  u32x4 v = __builtin_bit_cast(u32x4, q1);
  const unsigned w2 = (__float_as_uint(h) >> 16) | (__float_as_uint(m) & 0xffff0000u), w3 = __float_as_uint(l) >> 16;
  v[2] = hh ? w2 : v[2]; v[3] = hh ? w3 : v[3];
  q1 = __builtin_bit_cast(bf16x8, v);
}
DI float tile_max(const f32x16& s) {
  float a = __builtin_fmaxf(__builtin_fmaxf(s[0], s[1]), s[2]), b = __builtin_fmaxf(__builtin_fmaxf(s[3], s[4]), s[5]);
#pragma unroll
  for (int r = 6; r < 14; r += 4) { a = __builtin_fmaxf(__builtin_fmaxf(a, s[r]), s[r + 1]); b = __builtin_fmaxf(__builtin_fmaxf(b, s[r + 2]), s[r + 3]); }
  a = __builtin_fmaxf(__builtin_fmaxf(a, s[14]), s[15]);
  return swap_max(__builtin_fmaxf(a, b));
}

DI void row_norm(lptr lds, int t, int r32, float& rs, float& nmb) {
  const f32x2 p = *(LAS f32x2*)(lds + L_STAT + (t * 32 + r32) * 8);
  const float mean = p[0] * (1.f / 448.f), var = __builtin_fmaxf(p[1] * (1.f / 448.f) - mean * mean, 0.f);
  const float sd = bf16r(__builtin_amdgcn_sqrtf(bf16r(bf16r(var) + 1e-5f))); rs = __builtin_amdgcn_rcpf(sd); nmb = -bf16r(mean);
}
template <int P, int ST> DI void f_steps(const Ld& k, lptr lds, const bf16x8 (&xa)[28], bf16x8 (&f0)[2], bf16x8 (&f1)[2], f32x16& a0, f32x16& a1, f32x4& c0, f32x4& c1,
                                         const bf16x8& sel, const lptr (&w0)[4], const lptr (&w1)[4]) {
  if constexpr (ST < 28) {
    if constexpr (P == 0 && ST == 6) { constexpr int n = 2 * n_issued(6) + 6 < 78 ? 2 * n_issued(6) + 6 : 78; asm volatile("s_waitcnt vmcnt(%0)" :: "i"(n - 64) : "memory"); cnt_add(lds, C_F); }
    if constexpr (P == 0 && ST == 11) cnt_wait(lds, C_F, 4u);
    if constexpr (ST + 1 < 28) { constexpr int off = p1_slot((P ? 32 : 25) + ((ST + 1) >> 2)) - (P ? L_FB : L_FA);
      f0[(ST + 1) & 1] = trfrag(w0[(ST + 1) & 3] + off, 512); f1[(ST + 1) & 1] = trfrag(w1[(ST + 1) & 3] + off, 512); }
    SBAR();
    a0 = MFMA32(f0[ST & 1], xa[ST], a0); a1 = MFMA32(f1[ST & 1], xa[ST], a1);
    if constexpr ((ST & 3) == 0) { c0 = MFMA16(sel, f0[ST & 1], c0); c1 = MFMA16(sel, f1[ST & 1], c1); }
    PIN(a0); PIN(a1); PIN(c0); PIN(c1); SBAR();
    if constexpr (P == 0 && n_issued(6) + (ST >> 1) < 39) ld_piece<n_issued(6) + (ST >> 1), ST & 1>(k);
    f_steps<P, ST + 1>(k, lds, xa, f0, f1, a0, a1, c0, c1, sel, w0, w1);
  }
}

constexpr int g2_issued(int st) { int n = 28; for (int s = 0; s < st; ++s) { if (s < 14) n += 2; if (s < 24) n += 2; } return n; }
constexpr int g2_pos_h(int s) { return s < 14 ? 2 * s + 1 : g2_issued(s - 14) + 1; }
constexpr int g2_pos_w(int s) { return s < 12 ? -1 : g2_issued(s - 12) + (s - 12 < 14 ? 2 : 0) + 1; }
constexpr int g2_wait(int st) { const int a = g2_pos_h(st), b = st + 1 < 28 ? g2_pos_w(st + 1) : -1; const int n = g2_issued(st) - 1 - (a > b ? a : b); return n > 63 ? 63 : n; }
DI void ldg_sc1(u32x4& d, const char* sbase, unsigned voff) { asm volatile("global_load_dwordx4 %0, %1, %2 sc1" : "=v"(d) : "v"(voff), "s"(sbase) : "memory"); }
struct G2 { const char *hb0, *hb1, *w2s, *wos; unsigned lv, w2vo, ringdst; int t; lptr br0, br1, lds; };
template <int ST> DI void g2_w(const G2& g) {
#pragma unroll
  for (int e = 0; e < 2; ++e) {
    const unsigned dst = g.ringdst + (ST % 12) * 2048 + e * 1024;
    if constexpr (ST < 28) glds16(g.w2s + (ST * 16 + e * 8) * 896, g.w2vo, dst);
    else if constexpr (ST < 35) glds16(g.wos + ((ST - 28) * 16 + e * 8) * 896, g.w2vo, dst);
    else glds16(g.wos + (g.t == 3 ? 104 : 112 + e * 8) * 896, g.w2vo, dst);
  }
}
template <int A, int B> DI void g2_ws(const G2& g) { if constexpr (A < B) { g2_w<A>(g); g2_ws<A + 1, B>(g); } }
template <int ST> DI void g2_steps(const G2& g, u32x4 (&hf)[14][2], bf16x8 (&f0)[2], bf16x8 (&f1)[2], f32x16& a00, f32x16& a01, f32x16& a10, f32x16& a11) {
  if constexpr (ST < 28) {
    if constexpr (ST == G2_SPLIT) cnt_wait(g.lds, C_DONE, 4u);
    asm volatile("s_waitcnt vmcnt(%2)" : "+v"(hf[ST % 14][0]), "+v"(hf[ST % 14][1]) : "i"(g2_wait(ST)) : "memory");
    if constexpr (ST + 1 < 28) { f0[(ST + 1) & 1] = trfrag(g.br0 + ((ST + 1) % 12) * 2048, 512); f1[(ST + 1) & 1] = trfrag(g.br1 + ((ST + 1) % 12) * 2048, 512); }
    SBAR();
    const bf16x8 h0 = asfrag(hf[ST % 14][0]), h1 = asfrag(hf[ST % 14][1]);
    a00 = MFMA32(f0[ST & 1], h0, a00); a01 = MFMA32(f0[ST & 1], h1, a01); a10 = MFMA32(f1[ST & 1], h0, a10); a11 = MFMA32(f1[ST & 1], h1, a11);
    PIN(a00); PIN(a01); PIN(a10); PIN(a11); SBAR();
    if constexpr (ST < 14) { ldg_sc1(hf[ST][0], g.hb0 + (ST + 14) * 1024, g.lv); ldg_sc1(hf[ST][1], g.hb1 + (ST + 14) * 1024, g.lv); }
    if constexpr (ST < 24) g2_w<ST + 12>(g);
    g2_steps<ST + 1>(g, hf, f0, f1, a00, a01, a10, a11);
  }
}

__global__ void __launch_bounds__(512, 2) fused_block(const char* __restrict__ x, const char* __restrict__ wqkv, const char* __restrict__ wo,
                                                      const char* __restrict__ w1, const char* __restrict__ w2, char* __restrict__ out, char* ws) {
  __shared__ __attribute__((aligned(16))) char smem[LDS_BYTES];
  u64 pc; asm volatile("s_getpc_b64 %0" : "=s"(pc));
  const lptr lds = (lptr)smem; const unsigned lds0 = (unsigned)(uintptr_t)lds;
  const int tid = threadIdx.x, lane = tid & 63, r32 = lane & 31, hh = lane >> 5;
  const int w = __builtin_amdgcn_readfirstlane(tid >> 6), t = w & 3, c = w >> 2;
  const int b = blockIdx.x, xg = b & 7, bi = b >> 3;
  const int j = (xg & 3) * 4 + (bi >> 3), rb = (xg >> 2) * 8 + (bi & 7);
  const int fme = (rb * 16 + j) * 128, rt = rb * 4 + t;
  STAMP(0); CSTAMP(30); VSTAMP(29, (unsigned)__builtin_amdgcn_s_getreg(20 | (3 << 11)) & 0xfu);
  if (tid == 256) __hip_atomic_store((GAS u64*)(ws + 0x40000 + blockIdx.x * 8), (u64)__builtin_amdgcn_s_memrealtime(), RLX_AGENT);
  const int rot = t;
  const int q4 = (lane & 15) >> 2, p4 = lane & 3, g16 = (lane >> 4) & 1;
  const int pi_std = 4 * g16 + 2 * (p4 & 1) + (p4 >> 1);
  const lptr wrd0 = lds + (8 * hh + q4) * 128 + pi_std * 8;
  const lptr priv = lds + L_PRIV + w * 256;
  const __amdgpu_buffer_rsrc_t rsw = __builtin_amdgcn_make_buffer_rsrc(ws, 0, WS_BYTES, 0x00020000);
#define COMMON_SETUP() \
  lptr xrd[4], wr0[4], wr1[4]; \
  _Pragma("unroll") for (int s = 0; s < 4; ++s) { xrd[s] = lds + (t * 32 + r32) * 128 + (((2 * (s ^ rot) + hh) ^ ((r32 >> 1) & 7)) << 4); \
    wr0[s] = wrd0 + ((q4 >> 1) << 6) + (s ^ rot) * 2048; wr1[s] = wrd0 + ((1 ^ (q4 >> 1)) << 6) + (s ^ rot) * 2048; } \
  const int xwrap = (t >> 1) ? -NSLOT * SLOT : 0; \
  bf16x8 sel; { const short one = ((lane & 15) == ((lane >> 4) & 1)) ? (short)0x3f80 : (short)0; sel = (bf16x8){one, one, one, one, one, one, one, one}; } \
  bf16x8 xa[28]; float rs, nmb
#define GET_TAG() PIN(tagraw); const u64 tag = (((u64)(unsigned)__builtin_amdgcn_readfirstlane((unsigned)(tagraw >> 32)) << 32) | (unsigned)__builtin_amdgcn_readfirstlane((unsigned)tagraw)) + 1

  if (c == 0) {
    unsigned cpf0, cpf1;
    { asm volatile("global_load_dword %0, %1, %2" : "=v"(cpf0) : "v"(min((unsigned)tid * 128u, (unsigned)CODE_BYTES - 128u)), "s"(pc) : "memory");
      asm volatile("global_load_dword %0, %1, %2" : "=v"(cpf1) : "v"(min((unsigned)tid * 128u + 32768u, (unsigned)CODE_BYTES - 128u)), "s"(pc) : "memory"); }
    unsigned tch = 0;
    u64 tagraw = __hip_atomic_load((GAS u64*)(ws + WS_EPOCH), RLX_AGENT);
    if (tid < 16) *(LAS unsigned*)(lds + L_CNT + 4 * tid) = 0u;
    const bool isK = j < 7, isV = j >= 7 && j < 14;
    bf16x8 qf[4][2];
    {
      COMMON_SETUP();
      f32x16 a0 = {}, a1 = {}; f32x4 c0 = {}, c1 = {};
      if (isV) p1_compute<true, 0>(xa, a0, a1, c0, c1, sel, xrd, xwrap, wr0, wr1 STA); else p1_compute<false, 0>(xa, a0, a1, c0, c1, sel, xrd, xwrap, wr0, wr1 STA);
      STAMP(1);
      cs_put(lds, L_CS0, 0, t, lane, c0); cs_put(lds, L_CS0, 1, t, lane, c1);
      asm volatile("s_waitcnt vmcnt(0) lgkmcnt(0)\n\ts_barrier" : "+v"(cpf0), "+v"(cpf1), "+v"(tch) :: "memory");
      const float cl0 = cs_get(lds, L_CS0, 0, r32), cl1 = cs_get(lds, L_CS0, 1, r32);
      if (isV) {
        row_norm(lds, t, r32, rs, nmb);
        float rr[16], mm[16]; lane_to_regs(priv, r32, hh, rs, rr); lane_to_regs(priv + 128, r32, hh, nmb, mm);
        const int rho = r32 >> 2, col0 = (j - 7) * 64 + 4 * ((rho & 4) + 2 * (rho & 1) + ((rho >> 1) & 1)) + (r32 & 3);
#pragma unroll
        for (int cc = 0; cc < 2; ++cc) {
          const f32x16& a = cc ? a1 : a0; const float cl = cc ? cl1 : cl0; u32x4 o[2];
#pragma unroll
          for (int p = 0; p < 8; ++p) o[p >> 2][p & 3] = cvtpk(rr[2 * p] * __builtin_fmaf(mm[2 * p], cl, a[2 * p]), rr[2 * p + 1] * __builtin_fmaf(mm[2 * p + 1], cl, a[2 * p + 1]));
          const int col = col0 + cc * 32, hd = col / 28, d = col - hd * 28;
          const unsigned ad = WS_KV + hd * 262144 + rt * 4096 + 2048 + (hh * 32 + d) * 16;
          ST16(o[0], ad); ST16(o[1], ad + 1024);
          if (d >= 24) { const unsigned e = d == 24 ? 0x3f803f80u : 0u; const u32x4 pad = {e, e, e, e}; ST16(pad, ad + 64); ST16(pad, ad + 1024 + 64); }
        }
      } else {
        float cr0[16], cr1[16]; lane_to_regs(priv, r32, hh, cl0, cr0); lane_to_regs(priv + 128, r32, hh, cl1, cr1);
        row_norm(lds, t, r32, rs, nmb);
        u32x4 o[4];
        if (isK) {
          epi_T<false>(a0, cr0, rs, nmb, *(u32x4(*)[2])&o[0]); epi_T<false>(a1, cr1, rs, nmb, *(u32x4(*)[2])&o[2]);
#pragma unroll
          for (int s = 0; s < 4; ++s) {
            const int P = j * 8 + 2 * s + hh, hp = P / 7, rr = P - hp * 7;
            const int hd = 2 * hp + (rr > 3), sg = rr > 3 ? rr - 4 : rr;
            const unsigned ad = WS_KV + hd * 262144 + rt * 4096 + sg * 512 + r32 * 16;
            u32x4 v = o[s]; if (rr == 3) { v[2] = 0x3f803f80u; v[3] = 0x00003f80u; }
            ST16(v, ad);
            if (rr == 3) { const u32x4 e = {o[s][2], o[s][3], 0x3f803f80u, 0x00003f80u}; ST16(e, ad + 262144); }
          }
        } else {
          epi_T<true>(a0, cr0, rs, nmb, *(u32x4(*)[2])&o[0]); epi_T<true>(a1, cr1, rs, nmb, *(u32x4(*)[2])&o[2]);
          const unsigned ad = WS_H + rt * 114688 + (j - 14) * 4096 + lane * 16;
#pragma unroll
          for (int s = 0; s < 4; ++s) ST16(o[s], ad + s * 1024);
        }
      }
      STAMP(2);
      const int rho = 4 * g16 + p4, sg = 2 * (rho >> 2) + (rho & 1), half = (rho >> 1) & 1;
      const int g = (j & 1) ? (sg < 3 ? 2 * sg + 1 + half : (half ? 7 : 0)) : 2 * sg + half;
      lptr qb[4];
#pragma unroll
      for (int s = 0; s < 4; ++s) qb[s] = lds + L_WQ + (8 * hh + q4) * 64 + g * 8 + (s ^ rot) * 1024;
      a0 = (f32x16){}; c0 = (f32x4){};
      bf16x8 wf[3];
      wf[0] = trfrag(qb[0], 256); wf[1] = trfrag(qb[1], 256);
#pragma unroll
      for (int st = 0; st < 28; ++st) {
        if (st + 2 < 28) wf[(st + 2) % 3] = trfrag(qb[(st + 2) & 3] + ((st + 2) >> 2) * 4096, 256);
        SBAR();
        a0 = MFMA32(wf[st % 3], xa[st], a0);
        c0 = MFMA16(sel, wf[st % 3], c0);
        PIN(a0); PIN(c0); SBAR();
      }
      if (lane < 16) { *(LAS float*)(priv + lane * 4) = c0[0]; *(LAS float*)(priv + 64 + lane * 4) = c0[1]; }
      float cr[16];
#pragma unroll
      for (int i = 0; i < 4; ++i) { const f32x4 q = ldsf4(priv + (8 * i + 4 * hh) * 4); cr[4 * i] = q[0]; cr[4 * i + 1] = q[1]; cr[4 * i + 2] = q[2]; cr[4 * i + 3] = q[3]; }
      u32x4 o[2]; epi_T<false>(a0, cr, rs * QSCALE, nmb, o);
      if (hh) { o[1][2] = 0u; o[1][3] = 0u; }
      *(LAS u32x4*)(lds + L_QX + t * 2048 + lane * 16) = o[0]; *(LAS u32x4*)(lds + L_QX + t * 2048 + 1024 + lane * 16) = o[1];
    }
    cnt_add(lds, C_Q);
    STAMP(3);
    GET_TAG();
    WAIT_VM(0);
    if (cnt_add(lds, C_KV) == 3u && lane == 0) __hip_atomic_store((GAS u64*)(ws + WS_FKV + fme), tag, RLX_AGENT);
    if (cnt_add(lds, C_H) == 7u && lane == 0) __hip_atomic_store((GAS u64*)(ws + WS_FH + fme), tag, RLX_AGENT);
    STAMP(4);
    { const int c0 = (28 * j) >> 6, c1 = (28 * j + 27) >> 6, sel4 = lane >> 4;
      int jj = sel4 == 0 ? c0 : sel4 == 1 ? c1 : sel4 == 2 ? 7 + c0 : 7 + c1;
      if (j == 0 && (sel4 & 1)) jj = 14 + (sel4 >> 1);
      poll_flags(ws + WS_FKV + ((lane & 15) * 16 + jj) * 128, tag); }
    STAMP(5);
    const unsigned kvb = WS_KV + j * 262144 + t * 65536 + lane * 16;
    u32x4 kv[4][4];
#pragma unroll
    for (int u = 0; u < 3; ++u)
#pragma unroll
      for (int e = 0; e < 4; ++e) kv[u][e] = LD16(kvb + u * 4096 + e * 1024);
    cnt_wait(lds, C_Q, 4u);
#pragma unroll
    for (int q = 0; q < 4; ++q) { const lptr a = lds + L_QX + ((q + t) & 3) * 2048 + lane * 16; qf[q][0] = ldsb128(a); qf[q][1] = ldsb128(a + 1024); }
    cnt_add(lds, C_QR);
    const f32x16 zero = {};
    f32x16 o[4]; float mref[4]; bool redo = false;
#pragma unroll
    for (int q = 0; q < 4; ++q) {
      f32x16 s = MFMA32(asfrag(kv[0][0]), qf[q][0], zero); s = MFMA32(asfrag(kv[0][1]), qf[q][1], s);
      mref[q] = __builtin_ceilf(tile_max(s)) + REF_MARGIN; redo |= !(__builtin_fabsf(mref[q]) < 16777216.f);
      set_ref(qf[q][1], mref[q], hh); o[q] = zero;
    }
    STAMP(6);
    __builtin_amdgcn_s_setprio(2);
    f32x16 sA, sB; u32x4 pA0 = {}, pA1 = {}, pB0 = {}, pB1 = {};
    sA = MFMA32(asfrag(kv[0][0]), qf[0][0], zero); sA = MFMA32(asfrag(kv[0][1]), qf[0][1], sA);
    kv[3][2] = (u32x4){}; kv[3][3] = (u32x4){}; if (ABL_NOKV) { kv[3][0] = kv[0][0]; kv[3][1] = kv[0][1]; }
#define EX4(S, i) do { S[i] = __builtin_amdgcn_exp2f(S[i]); S[i + 1] = __builtin_amdgcn_exp2f(S[i + 1]); S[i + 2] = __builtin_amdgcn_exp2f(S[i + 2]); S[i + 3] = __builtin_amdgcn_exp2f(S[i + 3]); } while (0)
#define UNIT(SC, SN, PC0, PC1, PP0, PP1, KN0, KN1, QN, QP, VV0, VV1) do { \
      SBAR(); SN = MFMA32(asfrag(KN0), qf[QN][0], zero); PIN(SN); SBAR(); EX4(SC, 0); PIN(SC); SBAR(); \
      SN = MFMA32(asfrag(KN1), qf[QN][1], SN); PIN(SN); SBAR(); EX4(SC, 4); PIN(SC); SBAR(); \
      o[QP] = MFMA32(asfrag(VV0), asfrag(PP0), o[QP]); PIN(o[QP]); SBAR(); EX4(SC, 8); PIN(SC); SBAR(); \
      o[QP] = MFMA32(asfrag(VV1), asfrag(PP1), o[QP]); PIN(o[QP]); SBAR(); EX4(SC, 12); SBAR(); \
      PC0 = (u32x4){cvtpk(SC[0], SC[1]), cvtpk(SC[2], SC[3]), cvtpk(SC[4], SC[5]), cvtpk(SC[6], SC[7])}; \
      PC1 = (u32x4){cvtpk(SC[8], SC[9]), cvtpk(SC[10], SC[11]), cvtpk(SC[12], SC[13]), cvtpk(SC[14], SC[15])}; PIN(PC0); PIN(PC1); SBAR(); } while (0)
#define KEYTILE(U, UN, UL) do { \
      UNIT(sA, sB, pA0, pA1, pB0, pB1, kv[U][0], kv[U][1], 1, 3, kv[UL][2], kv[UL][3]); \
      if (!ABL_NOKV) { const int nt = min(kt + (U) + 3, 15) * 4096; _Pragma("unroll") for (int e = 0; e < 4; ++e) kv[UL][e] = __builtin_amdgcn_raw_buffer_load_b128(rsw, kvb + e * 1024, nt, LDAUX); } \
      UNIT(sB, sA, pB0, pB1, pA0, pA1, kv[U][0], kv[U][1], 2, 0, kv[U][2], kv[U][3]); \
      UNIT(sA, sB, pA0, pA1, pB0, pB1, kv[U][0], kv[U][1], 3, 1, kv[U][2], kv[U][3]); \
      UNIT(sB, sA, pB0, pB1, pA0, pA1, kv[UN][0], kv[UN][1], 0, 2, kv[U][2], kv[U][3]); \
    } while (0)
    for (int kt = 0; kt < 16; kt += 4) {
      KEYTILE(0, 1, 3); KEYTILE(1, 2, 0); KEYTILE(2, 3, 1); KEYTILE(3, 0, 2);
      const float lm = __builtin_fmaxf(__builtin_fmaxf(o[0][12], o[1][12]), __builtin_fmaxf(o[2][12], o[3][12]));
      if (__builtin_expect((__ballot(lm > 16777216.f) >> 32) != 0, 0)) {
        NRESC;
        o[3] = MFMA32(asfrag(kv[3][2]), asfrag(pB0), o[3]); o[3] = MFMA32(asfrag(kv[3][3]), asfrag(pB1), o[3]); pB0 = (u32x4){}; pB1 = (u32x4){};
#pragma unroll
        for (int q = 0; q < 4; ++q) { const float l = upper_bcast(o[q][12]); redo |= !(l < 1.2980742146337069e33f);
          const int e = max(__builtin_amdgcn_frexp_expf(l) + (int)REF_MARGIN, 0);
#pragma unroll
          for (int r = 0; r < 16; ++r) o[q][r] = __builtin_ldexpf(o[q][r], -e);
          mref[q] += (float)e; redo |= !(__builtin_fabsf(mref[q]) < 16777216.f); set_ref(qf[q][1], mref[q], hh);
          if (q == 0) {
#pragma unroll
            for (int r = 0; r < 16; ++r) sA[r] -= (float)e; } } }
    }
    cnt_add(lds, C_DONE);
    o[3] = MFMA32(asfrag(kv[3][2]), asfrag(pB0), o[3]); o[3] = MFMA32(asfrag(kv[3][3]), asfrag(pB1), o[3]);
    { float z[4] = {0.f, 0.f, 0.f, 0.f};
#pragma unroll
      for (int r = 0; r < 16; ++r)
#pragma unroll
        for (int q = 0; q < 4; ++q) z[q] = __builtin_fmaf(o[q][r], 0.f, z[q]);
      const float zz = (z[0] + z[1]) + (z[2] + z[3]); redo |= !(zz == 0.f); }
    if (__builtin_expect(__any(redo), 0)) {
#pragma unroll
      for (int q = 0; q < 4; ++q) { set_ref(qf[q][1], 0.f, hh); o[q] = zero; mref[q] = -3.0e38f; }
      for (int kt = 0; kt < 16; ++kt) {
        const u32x4 k0 = LD16(kvb + kt * 4096), k1 = LD16(kvb + kt * 4096 + 1024), v0 = LD16(kvb + kt * 4096 + 2048), v1 = LD16(kvb + kt * 4096 + 3072);
#pragma unroll
        for (int q = 0; q < 4; ++q) {
          f32x16 s = MFMA32(asfrag(k0), qf[q][0], zero); s = MFMA32(asfrag(k1), qf[q][1], s);
          const float mn = __builtin_fmaxf(mref[q], tile_max(s)), al = __builtin_amdgcn_exp2f(mref[q] - mn); mref[q] = mn;
#pragma unroll
          for (int r = 0; r < 16; ++r) { o[q][r] *= al; s[r] = __builtin_amdgcn_exp2f(s[r] - mn); }
          const u32x4 p0 = {cvtpk(s[0], s[1]), cvtpk(s[2], s[3]), cvtpk(s[4], s[5]), cvtpk(s[6], s[7])}, p1 = {cvtpk(s[8], s[9]), cvtpk(s[10], s[11]), cvtpk(s[12], s[13]), cvtpk(s[14], s[15])};
          o[q] = MFMA32(asfrag(v0), asfrag(p0), o[q]); o[q] = MFMA32(asfrag(v1), asfrag(p1), o[q]);
        }
      }
    }
    STAMP(7);
    VSTAMP(28, nresc + (__any(redo) ? 1000 : 0));
    cnt_wait(lds, C_QR, 4u); cnt_wait(lds, C_H, 8u);
    auto area = [&](int dst, int e) -> lptr { return lds + (dst * 3 + e) * 4096 + lane * 16; };
#pragma unroll
    for (int q = 1; q < 4; ++q) {
      const lptr a = area((q + t) & 3, q - 1);
      o[q][13] = hh ? mref[q] : o[q][13];
#pragma unroll
      for (int i = 0; i < 4; ++i) *(LAS f32x4*)(a + i * 1024) = (f32x4){o[q][4 * i], o[q][4 * i + 1], o[q][4 * i + 2], o[q][4 * i + 3]};
    }
    f32x16 of = o[0]; const float mown = mref[0];
    cnt_add(lds, C_CMB); STAMP(9); cnt_wait(lds, C_CMB, 4u);
    { f32x4 pp[3][4]; float ms[3];
#pragma unroll
      for (int e = 0; e < 3; ++e) { const lptr a = area(t, e);
#pragma unroll
        for (int i = 0; i < 4; ++i) pp[e][i] = ldsf4(a + i * 1024);
        ms[e] = upper_bcast(pp[e][3][1]); }
      const float mx = __builtin_fmaxf(__builtin_fmaxf(mown, ms[0]), __builtin_fmaxf(ms[1], ms[2]));
      const float f0 = __builtin_amdgcn_exp2f(mown - mx - 2.f), f1 = __builtin_amdgcn_exp2f(ms[0] - mx - 2.f), f2 = __builtin_amdgcn_exp2f(ms[1] - mx - 2.f), f3 = __builtin_amdgcn_exp2f(ms[2] - mx - 2.f);
#pragma unroll
      for (int r = 0; r < 16; ++r) of[r] = of[r] * f0 + pp[0][r >> 2][r & 3] * f1 + pp[1][r >> 2][r & 3] * f2 + pp[2][r >> 2][r & 3] * f3;
      const float inv = __builtin_amdgcn_rcpf(upper_bcast(of[12]));
      u32x4 e0, e1;
#pragma unroll
      for (int p = 0; p < 4; ++p) { e0[p] = cvtpk(of[2 * p] * inv, of[2 * p + 1] * inv); e1[p] = cvtpk(of[8 + 2 * p] * inv, of[9 + 2 * p] * inv); }
      if (hh) { e1[2] = 0u; e1[3] = 0u; }
      const unsigned a = WS_O + rt * 32768 + j * 2048 + lane * 16;
      ST16(e0, a); ST16(e1, a + 1024); }
    STAMP(10);
    WAIT_VM(0);
    if (cnt_add(lds, C_O) == 3u && lane == 0) __hip_atomic_store((GAS u64*)(ws + WS_FO + fme), tag, RLX_AGENT);
    STAMP(8); CSTAMP(31); STAMP_OUT();
  } else {
    Ld k;
    { const int row = 16 * t + (lane >> 3); const unsigned piece = ((lane & 7) ^ (((row >> 1) & 1) << 2)) << 4;
      k.xsrc = x + rb * 128 * 896; k.w0rs = j < 14 ? 2688u : 3584u; k.w0src = j < 14 ? wqkv + 896 + j * 128 : w1 + (j - 14) * 128;
      k.fasrc = w1 + (2 + j) * 128; k.fbsrc = w1 + (12 + j) * 128; k.dst = lds0 + t * 2048;
      k.w0vo = row * k.w0rs + piece; k.wfvo = row * 3584 + piece;
      k.wqsrc = wqkv + j * 56; k.wqvo = (32 * t + (lane >> 2)) * 2688 + (lane & 3) * 16; k.wq3 = t < 2 ? 384 * 2688 : 0;
      k.xvo0 = row * 896 + (((lane & 7) ^ (lane >> 4)) << 4); k.xvo1 = k.xvo0 ^ 64u; }
    u64 tagraw;
    {
      ld_items<0, PRO>(k);
      tagraw = __hip_atomic_load((GAS u64*)(ws + WS_EPOCH), RLX_AGENT);
      COMMON_SETUP();
      f32x4 s1 = {}, s2 = {};
      p1_load<0>(k, xa, s1, s2, xrd, xwrap STA);
      STAMP(1);
      { const float q = (lane & 2) ? ((lane & 1) ? s2[3] : s2[2]) : ((lane & 1) ? s2[1] : s2[0]);
        const float a = swap_add(s1[0]), bb = swap_add(q); if (hh == 0) *(LAS f32x2*)(lds + L_STAT + (t * 32 + r32) * 8) = (f32x2){a, bb}; }
      asm volatile("s_waitcnt vmcnt(%0) lgkmcnt(0)\n\ts_barrier" :: "i"(2 * (n_issued(6) - 28)) : "memory");
      row_norm(lds, t, r32, rs, nmb);
#pragma unroll
      for (int s = 0; s < 4; ++s) { wr0[s] += L_FA; wr1[s] += L_FA; PIN(wr0[s]); PIN(wr1[s]); }
      bf16x8 f0[2], f1[2]; f32x16 a0 = {}, a1 = {}; f32x4 c0 = {}, c1 = {}; float cr[16]; u32x4 o[2];
      f0[0] = trfrag(wr0[0], 512); f1[0] = trfrag(wr1[0], 512);
      f_steps<0, 0>(k, lds, xa, f0, f1, a0, a1, c0, c1, sel, wr0, wr1);
      cs_put(lds, L_CSA, 0, t, lane, c0); cs_put(lds, L_CSA, 1, t, lane, c1);
      WAIT_VM(0); cnt_add(lds, C_CSA); cnt_wait(lds, C_CSA, 4u);
      STAMP(2);
      { const unsigned ad = WS_H + rt * 114688 + (2 + j) * 4096 + lane * 16;
        lane_to_regs(priv, r32, hh, cs_get(lds, L_CSA, 0, r32), cr); epi_T<true>(a0, cr, rs, nmb, o); ST16(o[0], ad); ST16(o[1], ad + 1024);
        lane_to_regs(priv + 128, r32, hh, cs_get(lds, L_CSA, 1, r32), cr); epi_T<true>(a1, cr, rs, nmb, o); ST16(o[0], ad + 2048); ST16(o[1], ad + 3072); }
      STAMP(3);
      if (j >= 6) {
#pragma unroll
        for (int s = 0; s < 4; ++s) { wr0[s] += L_FB - L_FA; wr1[s] += L_FB - L_FA; PIN(wr0[s]); PIN(wr1[s]); }
        a0 = (f32x16){}; a1 = (f32x16){}; c0 = (f32x4){}; c1 = (f32x4){};
        f0[0] = trfrag(wr0[0], 512); f1[0] = trfrag(wr1[0], 512);
        f_steps<1, 0>(k, lds, xa, f0, f1, a0, a1, c0, c1, sel, wr0, wr1);
        cs_put(lds, L_CSB, 0, t, lane, c0); cs_put(lds, L_CSB, 1, t, lane, c1);
        cnt_add(lds, C_CSB); cnt_wait(lds, C_CSB, 4u);
        const unsigned ad = WS_H + rt * 114688 + (12 + j) * 4096 + lane * 16;
        lane_to_regs(priv, r32, hh, cs_get(lds, L_CSB, 0, r32), cr); epi_T<true>(a0, cr, rs, nmb, o); ST16(o[0], ad); ST16(o[1], ad + 1024);
        lane_to_regs(priv + 128, r32, hh, cs_get(lds, L_CSB, 1, r32), cr); epi_T<true>(a1, cr, rs, nmb, o); ST16(o[0], ad + 2048); ST16(o[1], ad + 3072);
      }
    }
    GET_TAG();
    WAIT_VM(0);
    if (cnt_add(lds, C_H) == 7u && lane == 0) __hip_atomic_store((GAS u64*)(ws + WS_FH + fme), tag, RLX_AGENT);
    STAMP(4);
    const int cg = bi >> 2;
    if (cg < 7) {
      const int rt0 = 8 * xg + 2 * (bi & 3), rb3 = rt0 >> 2;
      const int ring = L_RING + 3 * t * SLOT;
      const int ct = t >> 1, rtl = t & 1;
      G2 g; g.lds = lds; g.t = t; g.ringdst = lds0 + ring; g.lv = lane * 16;
      g.w2vo = (lane >> 3) * 896 + (((lane & 7) ^ (((lane >> 4) & 1) << 2)) << 4);
      g.w2s = w2 + t * 448 * 896 + cg * 128; g.wos = wo + t * 112 * 896 + cg * 128;
      g.hb0 = ws + WS_H + (rt0 + rtl) * 114688 + t * 28672; g.hb1 = ws + WS_H + (rt0 + (rtl ^ 1)) * 114688 + t * 28672;
      g.br0 = wrd0 + ring + ((ct ^ (q4 >> 1)) << 6); g.br1 = wrd0 + ring + ((ct ^ 1 ^ (q4 >> 1)) << 6);
      g2_ws<0, 12>(g);
      const int fb3 = rb3 * 16 + (lane & 15);
      poll_flags(ws + WS_FH + fb3 * 128, tag);
      WAIT_VM(0);
      STAMP(5);
      u32x4 hf[14][2];
#pragma unroll
      for (int s = 0; s < 14; ++s) { ldg_sc1(hf[s][0], g.hb0 + s * 1024, g.lv); ldg_sc1(hf[s][1], g.hb1 + s * 1024, g.lv); }
      f32x16 a00 = {}, a01 = {}, a10 = {}, a11 = {};
      PIN(g.br0); PIN(g.br1);
      { bf16x8 f0[2], f1[2]; f0[0] = trfrag(g.br0, 512); f1[0] = trfrag(g.br1, 512);
        g2_steps<0>(g, hf, f0, f1, a00, a01, a10, a11); }
      STAMP(6);
      WAIT_VM(0);
      bf16x8 wf[8][2];
      { const lptr o0 = lds + ring + SLOT + (4 * hh + q4) * 128 + pi_std * 8;
#pragma unroll
        for (int e = 0; e < 8; ++e)
#pragma unroll
          for (int cc = 0; cc < 2; ++cc) wf[e][cc] = trfrag(o0 + ((cc ^ ct ^ (q4 >> 1)) << 6) + ((e >> 1) * 28 + (e & 1) * 16) * 128, 1024); }
      const size_t oo = (size_t)((rt0 + rtl) * 32 + r32) * 896 + cg * 128 + ct * 64 + hh * 16;
      const u32x4 xr0 = *(const GAS u32x4*)(x + oo), xr1 = *(const GAS u32x4*)(x + oo + 32);
      poll_flags(ws + WS_FO + fb3 * 128, tag);
      STAMP(7);
      { const unsigned ob0 = WS_O + (rt0 + rtl) * 32768 + t * 8192 + lane * 16, ob1 = ob0 ^ 32768u;
        u32x4 of[8][2];
#pragma unroll
        for (int e = 0; e < 8; ++e) { of[e][0] = LD16(ob0 + e * 1024); of[e][1] = LD16(ob1 + e * 1024); }
        SBAR();
#pragma unroll
        for (int e = 0; e < 8; ++e) { const bf16x8 h0 = asfrag(of[e][0]), h1 = asfrag(of[e][1]);
          a11 = MFMA32(wf[e][1], h1, a11); a10 = MFMA32(wf[e][1], h0, a10); a01 = MFMA32(wf[e][0], h1, a01); a00 = MFMA32(wf[e][0], h0, a00);
          PIN(a00); PIN(a01); PIN(a10); PIN(a11); } }
      STAMP(9);
      if (b == 0 && tid == 256) __hip_atomic_store((GAS u64*)(ws + WS_EPOCH), tag, RLX_AGENT);
      { const lptr mine = lds + ring + lane * 16;
        auto put = [&](const f32x16& a, int e) {
#pragma unroll
          for (int i = 0; i < 4; ++i) *(LAS f32x4*)(mine + e * 4096 + i * 1024) = (f32x4){a[4 * i], a[4 * i + 1], a[4 * i + 2], a[4 * i + 3]}; };
        put(a11, 3); put(a10, 2); put(a01, 1); }
      f32x16 fin = a00;
      cnt_add(lds, C_RED); STAMP(10); cnt_wait(lds, C_RED, 4u);
#pragma unroll
      for (int e = 1; e < 4; ++e) { const lptr a = lds + L_RING + 3 * (t ^ e) * SLOT + e * 4096 + lane * 16;
#pragma unroll
        for (int i = 0; i < 4; ++i) { const f32x4 v = ldsf4(a + i * 1024); fin[4 * i] += v[0]; fin[4 * i + 1] += v[1]; fin[4 * i + 2] += v[2]; fin[4 * i + 3] += v[3]; } }
      u32x4 r0, r1;
#pragma unroll
      for (int p = 0; p < 4; ++p) { r0[p] = cvtpk(fin[2 * p] + bflo(xr0[p]), fin[2 * p + 1] + bfhi(xr0[p])); r1[p] = cvtpk(fin[8 + 2 * p] + bflo(xr1[p]), fin[9 + 2 * p] + bfhi(xr1[p])); }
      *(GAS u32x4*)(out + oo) = r0; *(GAS u32x4*)(out + oo + 32) = r1;
      if (b == 0 && w == 4) { u64 mn = ~0ull, mx = 0;
        for (int i = 0; i < 4; ++i) { const u64 v = __hip_atomic_load((GAS u64*)(ws + 0x40000 + (lane * 4 + i) * 8), RLX_AGENT); mn = v < mn ? v : mn; mx = v > mx ? v : mx; }
        for (int o = 32; o; o >>= 1) { const u64 a = __shfl_xor(mn, o), c2 = __shfl_xor(mx, o); mn = a < mn ? a : mn; mx = c2 > mx ? c2 : mx; }
        const u64 now = __builtin_amdgcn_s_memrealtime(); u64 X = 0; X = X > 5000 ? 5000 : X;
        while (__builtin_amdgcn_s_memrealtime() - now < X * 1) {} }
    }
    STAMP_OUT();
  }
}

extern "C" void kernel_launch(void* const* d_in, const int* in_sizes, int n_in, void* d_out, int out_size, void* d_ws, size_t ws_size, hipStream_t stream) {
  fused_block<<<256, 512, 0, stream>>>((const char*)d_in[0], (const char*)d_in[1], (const char*)d_in[2], (const char*)d_in[4], (const char*)d_in[5], (char*)d_out, (char*)d_ws + WS_BASE);
}
```

```cpp
#define METRIC 0
#include <hip/hip_runtime.h>
#include <stdint.h>

typedef short bf16x8 __attribute__((ext_vector_type(8)));
typedef short s16x4 __attribute__((ext_vector_type(4)));
typedef float f32x16 __attribute__((ext_vector_type(16)));
typedef float f32x4 __attribute__((ext_vector_type(4)));
typedef float f32x2 __attribute__((ext_vector_type(2)));
typedef __bf16 bf16x2v __attribute__((ext_vector_type(2)));
typedef unsigned u32x4 __attribute__((ext_vector_type(4)));
typedef unsigned long long u64;
#define LAS __attribute__((address_space(3)))
#define GAS __attribute__((address_space(1)))
#define DI __device__ __forceinline__
#define MFMA32(a, b, c) __builtin_amdgcn_mfma_f32_32x32x16_bf16(a, b, c, 0, 0, 0)
#define MFMA16(a, b, c) __builtin_amdgcn_mfma_f32_16x16x32_bf16(a, b, c, 0, 0, 0)
#define SBAR() __builtin_amdgcn_sched_barrier(0)
#define PIN(x) asm volatile("" : "+v"(x))
#define RLX_AGENT __ATOMIC_RELAXED, __HIP_MEMORY_SCOPE_AGENT
#define WAIT_BAR(N) asm volatile("s_waitcnt vmcnt(" #N ") lgkmcnt(0)\n\ts_barrier" ::: "memory")
#define WAIT_VM(N) asm volatile("s_waitcnt vmcnt(" #N ")" ::: "memory")
#define WAIT_LGKM0() asm volatile("s_waitcnt lgkmcnt(0)" ::: "memory")

constexpr unsigned WS_EPOCH = 0;
constexpr unsigned WS_FKV = 4096, WS_FH = WS_FKV + 32768, WS_FO = WS_FH + 32768;
constexpr unsigned WS_KV = 1u << 20;
constexpr unsigned WS_H = 5u << 20;
constexpr unsigned WS_O = 13u << 20;
constexpr unsigned WS_BYTES = 15u << 20;
constexpr int SLOT = 8192, NSLOT = 19;
constexpr int L_MISC = NSLOT * SLOT;
constexpr int L_STAT = L_MISC;
constexpr int L_CS0 = L_MISC + 2048, L_CSA = L_CS0 + 1024, L_CSB = L_CSA + 1024;
constexpr int L_PRIV = L_MISC + 5120;
constexpr int L_CNT = L_MISC + 7168;
constexpr int LDS_BYTES = 163840;
constexpr int L_WQ = 2 * SLOT, L_FA = 6 * SLOT, L_FB = 13 * SLOT, L_QX = SLOT, L_RING = 6 * SLOT;
constexpr int p1_slot(int item) { return (item % NSLOT) * SLOT; }
enum { C_CSA, C_CSB, C_Q, C_QR, C_KV, C_H, C_F, C_DONE, C_CMB, C_O, C_RED };

#define WS_BASE 0
#define TOUCH 0
#define MAPA 8
#define G2_SPLIT 16
#define CODE_BYTES 49152
constexpr float QSCALE = 0.18898223650461362f * 1.4426950408889634f;
constexpr float REF_MARGIN = 40.f;
constexpr unsigned SPIN_CAP = 1u << 16;

DI unsigned cvtpk(float lo, float hi) { f32x2 v = {lo, hi}; return __builtin_bit_cast(unsigned, __builtin_convertvector(v, bf16x2v)); }
DI float bf16r(float x) { return __uint_as_float(cvtpk(x, 0.f) << 16); }
DI float bflo(unsigned v) { return __uint_as_float(v << 16); }
DI float bfhi(unsigned v) { return __uint_as_float(v & 0xffff0000u); }
DI float swap_add(float v) { auto r = __builtin_amdgcn_permlane32_swap(__float_as_uint(v), __float_as_uint(v), false, false); return __uint_as_float(r[0]) + __uint_as_float(r[1]); }
DI float swap_max(float v) { auto r = __builtin_amdgcn_permlane32_swap(__float_as_uint(v), __float_as_uint(v), false, false); return __builtin_fmaxf(__uint_as_float(r[0]), __uint_as_float(r[1])); }
DI float upper_bcast(float v) { auto r = __builtin_amdgcn_permlane32_swap(__float_as_uint(v), __float_as_uint(v), false, false); return __uint_as_float(r[1]); }
DI void glds16(const void* sbase, unsigned voff, unsigned ldsdst) {
  unsigned keep;
  asm volatile("s_mov_b32 %0, m0\n\ts_mov_b32 m0, %3\n\ts_nop 0\n\tglobal_load_lds_dwordx4 %1, %2\n\ts_mov_b32 m0, %0"
               : "=&s"(keep) : "v"(voff), "s"(sbase), "s"(ldsdst) : "memory");
}
typedef LAS char* lptr;
typedef short v4i16_t __attribute__((ext_vector_type(4)));
DI s16x4 trd(lptr p) { return __builtin_bit_cast(s16x4, __builtin_amdgcn_ds_read_tr16_b64_v4i16((LAS v4i16_t*)p)); }
DI bf16x8 trfrag(lptr p, int second) { s16x4 lo = trd(p), hi = trd(p + second); return __builtin_shufflevector(lo, hi, 0, 1, 2, 3, 4, 5, 6, 7); }
DI bf16x8 ldsb128(lptr p) { return *(LAS bf16x8*)p; }
DI f32x4 ldsf4(lptr p) { return *(LAS f32x4*)p; }
DI bf16x8 asfrag(u32x4 v) { return __builtin_bit_cast(bf16x8, v); }
DI unsigned cnt_add(lptr lds, int c) { unsigned r = 0; WAIT_LGKM0(); if ((threadIdx.x & 63) == 0) r = __hip_atomic_fetch_add((LAS unsigned*)(lds + L_CNT + 4 * c), 1u, __ATOMIC_RELAXED, __HIP_MEMORY_SCOPE_WORKGROUP);
  return __builtin_amdgcn_readfirstlane(r); }
DI void cnt_wait(lptr lds, int c, unsigned n) {
  while (__builtin_amdgcn_readfirstlane(__hip_atomic_load((LAS unsigned*)(lds + L_CNT + 4 * c), __ATOMIC_RELAXED, __HIP_MEMORY_SCOPE_WORKGROUP)) < n) __builtin_amdgcn_s_sleep(1);
  asm volatile("" ::: "memory"); }
DI void poll_flags(const char* flag, u64 tag) {
#pragma nounroll
  for (unsigned s = 0; s < SPIN_CAP; ++s) {
    const u64 v = __hip_atomic_load((GAS u64*)flag, RLX_AGENT);
    if (__all(v == tag)) break;
#define POLLSLEEP 1
    __builtin_amdgcn_s_sleep(POLLSLEEP);
  }
  __builtin_amdgcn_fence(__ATOMIC_ACQUIRE, "wavefront");
  asm volatile("" ::: "memory");
}
#define STAMP(i) do {} while (0)
#define CSTAMP(i) do {} while (0)
#define VSTAMP(i, v) do {} while (0)
#define STAMP_OUT() do {} while (0)
#define NRESC
#define STV
#define STA
#define BAR() asm volatile("s_waitcnt lgkmcnt(0)\n\ts_barrier" ::: "memory")

#define PRO 9
#define RAMP 3
constexpr int n_issued(int c) { return c < 0 ? PRO : (PRO + RAMP * (c + 1) < 19 + 3 * c ? PRO + RAMP * (c + 1) : 19 + 3 * c); }
static_assert(n_issued(6) >= 29, "Wq and the head of F_a must be in flight at B1");
#define MFMA4(a, b, c) __builtin_amdgcn_mfma_f32_4x4x4bf16_1k(a, b, c, 0, 0, 0)
DI void rowstats(const bf16x8& f, const s16x4& ones, f32x4& s1, f32x4& s2) {
  const s16x4 lo = __builtin_shufflevector(f, f, 0, 1, 2, 3), hi = __builtin_shufflevector(f, f, 4, 5, 6, 7);
  s1 = MFMA4(ones, lo, s1); s2 = MFMA4(lo, lo, s2); s1 = MFMA4(ones, hi, s1); s2 = MFMA4(hi, hi, s2);
}
template <int C> DI void x_frags(bf16x8 (&xa)[28], const lptr (&xrd)[4], int xwrap) {
#pragma unroll
  for (int s = 0; s < 4; ++s) xa[4 * C + s] = ldsb128(xrd[s] + p1_slot(3 * C) + (C == 6 ? xwrap : 0));
}
template <bool VT, int C> DI void p1_compute(bf16x8 (&xa)[28], f32x16& a0, f32x16& a1, f32x4& c0, f32x4& c1, const bf16x8& sel,
                                             const lptr (&xrd)[4], int xwrap, const lptr (&w0)[4], const lptr (&w1)[4] STV) {
  if constexpr (C < 7) {
    BAR();
    if (C == 0) STAMP(12);
    bf16x8 f0[4], f1[4];
#pragma unroll
    for (int s = 0; s < 4; ++s) {
      xa[4 * C + s] = ldsb128(xrd[s] + p1_slot(3 * C) + (C == 6 ? xwrap : 0));
      f0[s] = trfrag(w0[s] + p1_slot(3 * C + 2), 512); f1[s] = trfrag(w1[s] + p1_slot(3 * C + 2), 512);
    }
#pragma unroll
    for (int s = 0; s < 4; ++s) {
      if constexpr (VT) { a0 = MFMA32(xa[4 * C + s], f0[s], a0); a1 = MFMA32(xa[4 * C + s], f1[s], a1); }
      else { a0 = MFMA32(f0[s], xa[4 * C + s], a0); a1 = MFMA32(f1[s], xa[4 * C + s], a1); }
      if (s == 0) { c0 = MFMA16(sel, f0[0], c0); c1 = MFMA16(sel, f1[0], c1); }
    }
    PIN(a0); PIN(a1); PIN(c0); PIN(c1);
    p1_compute<VT, C + 1>(xa, a0, a1, c0, c1, sel, xrd, xwrap, w0, w1 STA);
  }
}
struct Ld { const char *xsrc, *w0src, *wqsrc, *fasrc, *fbsrc; unsigned xvo0, xvo1, w0vo, wfvo, wqvo, wq3, w0rs, dst; };
template <int N, int E> DI void ld_piece(const Ld& k) {
  const unsigned dst = k.dst + p1_slot(N) + E * 1024;
  if constexpr (N < 21) {
    constexpr int c = N / 3, q = N % 3;
    if constexpr (q < 2) glds16(k.xsrc + (q * 64 + E * 8) * 896 + c * 128, E ? k.xvo1 : k.xvo0, dst);
    else glds16(k.w0src + (size_t)(c * 64 + E * 8) * k.w0rs, k.w0vo, dst);
  } else if constexpr (N < 24) glds16(k.wqsrc + ((N - 21) * 128 + E * 16) * 2688, k.wqvo, dst);
  else if constexpr (N == 24) glds16(k.wqsrc + k.wq3 + E * 16 * 2688, k.wqvo, dst);
  else if constexpr (N < 32) glds16(k.fasrc + ((N - 25) * 64 + E * 8) * 3584, k.wfvo, dst);
  else glds16(k.fbsrc + ((N - 32) * 64 + E * 8) * 3584, k.wfvo, dst);
}
template <int A, int B> DI void ld_items(const Ld& k) { if constexpr (A < B) { ld_piece<A, 0>(k); ld_piece<A, 1>(k); ld_items<A + 1, B>(k); } }
template <int C> DI void p1_load(const Ld& k, bf16x8 (&xa)[28], f32x4& s1, f32x4& s2, const lptr (&xrd)[4], int xwrap STV) {
  if constexpr (C < 7) {
    asm volatile("s_waitcnt vmcnt(%0) lgkmcnt(0)\n\ts_barrier" :: "i"(2 * (n_issued(C - 1) - 3 * (C + 1))) : "memory");
    if (C == 0) STAMP(12);
    x_frags<C>(xa, xrd, xwrap);
    SBAR(); ld_items<n_issued(C - 1), n_issued(C)>(k); SBAR();
    const s16x4 ones = {0x3f80, 0x3f80, 0x3f80, 0x3f80};
#pragma unroll
    for (int s = 0; s < 4; ++s) rowstats(xa[4 * C + s], ones, s1, s2);
    PIN(s1); PIN(s2);
    p1_load<C + 1>(k, xa, s1, s2, xrd, xwrap STA);
  }
}
DI void cs_put(lptr lds, int area, int c, int t, int lane, const f32x4& cs) {
  if (lane < 16) { *(LAS float*)(lds + area + c * 512 + lane * 16 + t * 4) = cs[0]; *(LAS float*)(lds + area + c * 512 + (lane + 16) * 16 + t * 4) = cs[1]; }
}
DI float cs_get(lptr lds, int area, int c, int r32) { const f32x4 p = ldsf4(lds + area + c * 512 + r32 * 16); return (p[0] + p[1]) + (p[2] + p[3]); }
DI void lane_to_regs(lptr priv, int r32, int hh, float v, float (&o)[16]) {
  *(LAS float*)(priv + r32 * 4) = v;
#pragma unroll
  for (int i = 0; i < 4; ++i) { const f32x4 q = ldsf4(priv + (8 * i + 4 * hh) * 4); o[4 * i] = q[0]; o[4 * i + 1] = q[1]; o[4 * i + 2] = q[2]; o[4 * i + 3] = q[3]; }
}
template <bool RELU> DI void epi_T(const f32x16& acc, const float (&cs)[16], float sc, float nmb, u32x4 (&o)[2]) {
#pragma unroll
  for (int p = 0; p < 8; ++p) {
    float a = sc * __builtin_fmaf(nmb, cs[2 * p], acc[2 * p]), b = sc * __builtin_fmaf(nmb, cs[2 * p + 1], acc[2 * p + 1]);
    if (RELU) { a = __builtin_fmaxf(a, 0.f); b = __builtin_fmaxf(b, 0.f); }
    o[p >> 2][p & 3] = cvtpk(a, b);
  }
}
#define ST16(v, off) __builtin_amdgcn_raw_buffer_store_b128(v, rsw, off, 0, 16)
#define ABL_NOKV 0
#define LDAUX 16
#define LD16(off) __builtin_amdgcn_raw_buffer_load_b128(rsw, off, 0, LDAUX)

DI void set_ref(bf16x8& q1, float M, int hh) {
  const float n = -M; const float h = __uint_as_float(__float_as_uint(n) & 0xffff0000u), r = n - h;
  const float m = __uint_as_float(__float_as_uint(r) & 0xffff0000u), l = r - m;
  u32x4 v = __builtin_bit_cast(u32x4, q1);
  const unsigned w2 = (__float_as_uint(h) >> 16) | (__float_as_uint(m) & 0xffff0000u), w3 = __float_as_uint(l) >> 16;
  v[2] = hh ? w2 : v[2]; v[3] = hh ? w3 : v[3];
  q1 = __builtin_bit_cast(bf16x8, v);
}
DI float tile_max(const f32x16& s) {
  float a = __builtin_fmaxf(__builtin_fmaxf(s[0], s[1]), s[2]), b = __builtin_fmaxf(__builtin_fmaxf(s[3], s[4]), s[5]);
#pragma unroll
  for (int r = 6; r < 14; r += 4) { a = __builtin_fmaxf(__builtin_fmaxf(a, s[r]), s[r + 1]); b = __builtin_fmaxf(__builtin_fmaxf(b, s[r + 2]), s[r + 3]); }
  a = __builtin_fmaxf(__builtin_fmaxf(a, s[14]), s[15]);
  return swap_max(__builtin_fmaxf(a, b));
}

DI void row_norm(lptr lds, int t, int r32, float& rs, float& nmb) {
  const f32x2 p = *(LAS f32x2*)(lds + L_STAT + (t * 32 + r32) * 8);
  const float mean = p[0] * (1.f / 448.f), var = __builtin_fmaxf(p[1] * (1.f / 448.f) - mean * mean, 0.f);
  const float sd = bf16r(__builtin_amdgcn_sqrtf(bf16r(bf16r(var) + 1e-5f))); rs = __builtin_amdgcn_rcpf(sd); nmb = -bf16r(mean);
}
template <int P, int ST> DI void f_steps(const Ld& k, lptr lds, const bf16x8 (&xa)[28], bf16x8 (&f0)[2], bf16x8 (&f1)[2], f32x16& a0, f32x16& a1, f32x4& c0, f32x4& c1,
                                         const bf16x8& sel, const lptr (&w0)[4], const lptr (&w1)[4]) {
  if constexpr (ST < 28) {
    if constexpr (P == 0 && ST == 6) { constexpr int n = 2 * n_issued(6) + 6 < 78 ? 2 * n_issued(6) + 6 : 78; asm volatile("s_waitcnt vmcnt(%0)" :: "i"(n - 64) : "memory"); cnt_add(lds, C_F); }
    if constexpr (P == 0 && ST == 11) cnt_wait(lds, C_F, 4u);
    if constexpr (ST + 1 < 28) { constexpr int off = p1_slot((P ? 32 : 25) + ((ST + 1) >> 2)) - (P ? L_FB : L_FA);
      f0[(ST + 1) & 1] = trfrag(w0[(ST + 1) & 3] + off, 512); f1[(ST + 1) & 1] = trfrag(w1[(ST + 1) & 3] + off, 512); }
    SBAR();
    a0 = MFMA32(f0[ST & 1], xa[ST], a0); a1 = MFMA32(f1[ST & 1], xa[ST], a1);
    if constexpr ((ST & 3) == 0) { c0 = MFMA16(sel, f0[ST & 1], c0); c1 = MFMA16(sel, f1[ST & 1], c1); }
    PIN(a0); PIN(a1); PIN(c0); PIN(c1); SBAR();
    if constexpr (P == 0 && n_issued(6) + (ST >> 1) < 39) ld_piece<n_issued(6) + (ST >> 1), ST & 1>(k);
    f_steps<P, ST + 1>(k, lds, xa, f0, f1, a0, a1, c0, c1, sel, w0, w1);
  }
}

constexpr int g2_issued(int st) { int n = 28; for (int s = 0; s < st; ++s) { if (s < 14) n += 2; if (s < 24) n += 2; } return n; }
constexpr int g2_pos_h(int s) { return s < 14 ? 2 * s + 1 : g2_issued(s - 14) + 1; }
constexpr int g2_pos_w(int s) { return s < 12 ? -1 : g2_issued(s - 12) + (s - 12 < 14 ? 2 : 0) + 1; }
constexpr int g2_wait(int st) { const int a = g2_pos_h(st), b = st + 1 < 28 ? g2_pos_w(st + 1) : -1; const int n = g2_issued(st) - 1 - (a > b ? a : b); return n > 63 ? 63 : n; }
DI void ldg_sc1(u32x4& d, const char* sbase, unsigned voff) { asm volatile("global_load_dwordx4 %0, %1, %2 sc1" : "=v"(d) : "v"(voff), "s"(sbase) : "memory"); }
struct G2 { const char *hb0, *hb1, *w2s, *wos; unsigned lv, w2vo, ringdst; int t; lptr br0, br1, lds; };
template <int ST> DI void g2_w(const G2& g) {
#pragma unroll
  for (int e = 0; e < 2; ++e) {
    const unsigned dst = g.ringdst + (ST % 12) * 2048 + e * 1024;
    if constexpr (ST < 28) glds16(g.w2s + (ST * 16 + e * 8) * 896, g.w2vo, dst);
    else if constexpr (ST < 35) glds16(g.wos + ((ST - 28) * 16 + e * 8) * 896, g.w2vo, dst);
    else glds16(g.wos + (g.t == 3 ? 104 : 112 + e * 8) * 896, g.w2vo, dst);
  }
}
template <int A, int B> DI void g2_ws(const G2& g) { if constexpr (A < B) { g2_w<A>(g); g2_ws<A + 1, B>(g); } }
template <int ST> DI void g2_steps(const G2& g, u32x4 (&hf)[14][2], bf16x8 (&f0)[2], bf16x8 (&f1)[2], f32x16& a00, f32x16& a01, f32x16& a10, f32x16& a11) {
  if constexpr (ST < 28) {
    if constexpr (ST == G2_SPLIT) cnt_wait(g.lds, C_DONE, 4u);
    asm volatile("s_waitcnt vmcnt(%2)" : "+v"(hf[ST % 14][0]), "+v"(hf[ST % 14][1]) : "i"(g2_wait(ST)) : "memory");
    if constexpr (ST + 1 < 28) { f0[(ST + 1) & 1] = trfrag(g.br0 + ((ST + 1) % 12) * 2048, 512); f1[(ST + 1) & 1] = trfrag(g.br1 + ((ST + 1) % 12) * 2048, 512); }
    SBAR();
    const bf16x8 h0 = asfrag(hf[ST % 14][0]), h1 = asfrag(hf[ST % 14][1]);
    a00 = MFMA32(f0[ST & 1], h0, a00); a01 = MFMA32(f0[ST & 1], h1, a01); a10 = MFMA32(f1[ST & 1], h0, a10); a11 = MFMA32(f1[ST & 1], h1, a11);
    PIN(a00); PIN(a01); PIN(a10); PIN(a11); SBAR();
    if constexpr (ST < 14) { ldg_sc1(hf[ST][0], g.hb0 + (ST + 14) * 1024, g.lv); ldg_sc1(hf[ST][1], g.hb1 + (ST + 14) * 1024, g.lv); }
    if constexpr (ST < 24) g2_w<ST + 12>(g);
    g2_steps<ST + 1>(g, hf, f0, f1, a00, a01, a10, a11);
  }
}

__global__ void __launch_bounds__(512, 2) fused_block(const char* __restrict__ x, const char* __restrict__ wqkv, const char* __restrict__ wo,
                                                      const char* __restrict__ w1, const char* __restrict__ w2, char* __restrict__ out, char* ws) {
  __shared__ __attribute__((aligned(16))) char smem[LDS_BYTES];
  u64 pc; asm volatile("s_getpc_b64 %0" : "=s"(pc));
  const lptr lds = (lptr)smem; const unsigned lds0 = (unsigned)(uintptr_t)lds;
  const int tid = threadIdx.x, lane = tid & 63, r32 = lane & 31, hh = lane >> 5;
  const int w = __builtin_amdgcn_readfirstlane(tid >> 6), t = w & 3, c = w >> 2;
  const int b = blockIdx.x, xg = b & 7, bi = b >> 3;
  const int j = (xg & 3) * 4 + (bi >> 3), rb = (xg >> 2) * 8 + (bi & 7);
  const int fme = (rb * 16 + j) * 128, rt = rb * 4 + t;
  STAMP(0); CSTAMP(30); VSTAMP(29, (unsigned)__builtin_amdgcn_s_getreg(20 | (3 << 11)) & 0xfu);
  if (tid == 0) __hip_atomic_store((GAS u64*)(ws + 0x40000 + 0 * 2048 + blockIdx.x * 8), (u64)__builtin_amdgcn_s_memrealtime(), RLX_AGENT);
  const int rot = t;
  const int q4 = (lane & 15) >> 2, p4 = lane & 3, g16 = (lane >> 4) & 1;
  const int pi_std = 4 * g16 + 2 * (p4 & 1) + (p4 >> 1);
  const lptr wrd0 = lds + (8 * hh + q4) * 128 + pi_std * 8;
  const lptr priv = lds + L_PRIV + w * 256;
  const __amdgpu_buffer_rsrc_t rsw = __builtin_amdgcn_make_buffer_rsrc(ws, 0, WS_BYTES, 0x00020000);
#define COMMON_SETUP() \
  lptr xrd[4], wr0[4], wr1[4]; \
  _Pragma("unroll") for (int s = 0; s < 4; ++s) { xrd[s] = lds + (t * 32 + r32) * 128 + (((2 * (s ^ rot) + hh) ^ ((r32 >> 1) & 7)) << 4); \
    wr0[s] = wrd0 + ((q4 >> 1) << 6) + (s ^ rot) * 2048; wr1[s] = wrd0 + ((1 ^ (q4 >> 1)) << 6) + (s ^ rot) * 2048; } \
  const int xwrap = (t >> 1) ? -NSLOT * SLOT : 0; \
  bf16x8 sel; { const short one = ((lane & 15) == ((lane >> 4) & 1)) ? (short)0x3f80 : (short)0; sel = (bf16x8){one, one, one, one, one, one, one, one}; } \
  bf16x8 xa[28]; float rs, nmb
#define GET_TAG() PIN(tagraw); const u64 tag = (((u64)(unsigned)__builtin_amdgcn_readfirstlane((unsigned)(tagraw >> 32)) << 32) | (unsigned)__builtin_amdgcn_readfirstlane((unsigned)tagraw)) + 1

  if (c == 0) {
    unsigned cpf0, cpf1;
    { asm volatile("global_load_dword %0, %1, %2" : "=v"(cpf0) : "v"(min((unsigned)tid * 128u, (unsigned)CODE_BYTES - 128u)), "s"(pc) : "memory");
      asm volatile("global_load_dword %0, %1, %2" : "=v"(cpf1) : "v"(min((unsigned)tid * 128u + 32768u, (unsigned)CODE_BYTES - 128u)), "s"(pc) : "memory"); }
    unsigned tch = 0;
    u64 tagraw = __hip_atomic_load((GAS u64*)(ws + WS_EPOCH), RLX_AGENT);
    if (tid < 16) *(LAS unsigned*)(lds + L_CNT + 4 * tid) = 0u;
    const bool isK = j < 7, isV = j >= 7 && j < 14;
    bf16x8 qf[4][2];
    {
      COMMON_SETUP();
      f32x16 a0 = {}, a1 = {}; f32x4 c0 = {}, c1 = {};
      if (isV) p1_compute<true, 0>(xa, a0, a1, c0, c1, sel, xrd, xwrap, wr0, wr1 STA); else p1_compute<false, 0>(xa, a0, a1, c0, c1, sel, xrd, xwrap, wr0, wr1 STA);
  if (tid == 0) __hip_atomic_store((GAS u64*)(ws + 0x40000 + 1 * 2048 + blockIdx.x * 8), (u64)__builtin_amdgcn_s_memrealtime(), RLX_AGENT);
      cs_put(lds, L_CS0, 0, t, lane, c0); cs_put(lds, L_CS0, 1, t, lane, c1);
      asm volatile("s_waitcnt vmcnt(0) lgkmcnt(0)\n\ts_barrier" : "+v"(cpf0), "+v"(cpf1), "+v"(tch) :: "memory");
      const float cl0 = cs_get(lds, L_CS0, 0, r32), cl1 = cs_get(lds, L_CS0, 1, r32);
      if (isV) {
        row_norm(lds, t, r32, rs, nmb);
        float rr[16], mm[16]; lane_to_regs(priv, r32, hh, rs, rr); lane_to_regs(priv + 128, r32, hh, nmb, mm);
        const int rho = r32 >> 2, col0 = (j - 7) * 64 + 4 * ((rho & 4) + 2 * (rho & 1) + ((rho >> 1) & 1)) + (r32 & 3);
#pragma unroll
        for (int cc = 0; cc < 2; ++cc) {
          const f32x16& a = cc ? a1 : a0; const float cl = cc ? cl1 : cl0; u32x4 o[2];
#pragma unroll
          for (int p = 0; p < 8; ++p) o[p >> 2][p & 3] = cvtpk(rr[2 * p] * __builtin_fmaf(mm[2 * p], cl, a[2 * p]), rr[2 * p + 1] * __builtin_fmaf(mm[2 * p + 1], cl, a[2 * p + 1]));
          const int col = col0 + cc * 32, hd = col / 28, d = col - hd * 28;
          const unsigned ad = WS_KV + hd * 262144 + rt * 4096 + 2048 + (hh * 32 + d) * 16;
          ST16(o[0], ad); ST16(o[1], ad + 1024);
          if (d >= 24) { const unsigned e = d == 24 ? 0x3f803f80u : 0u; const u32x4 pad = {e, e, e, e}; ST16(pad, ad + 64); ST16(pad, ad + 1024 + 64); }
        }
      } else {
        float cr0[16], cr1[16]; lane_to_regs(priv, r32, hh, cl0, cr0); lane_to_regs(priv + 128, r32, hh, cl1, cr1);
        row_norm(lds, t, r32, rs, nmb);
        u32x4 o[4];
        if (isK) {
          epi_T<false>(a0, cr0, rs, nmb, *(u32x4(*)[2])&o[0]); epi_T<false>(a1, cr1, rs, nmb, *(u32x4(*)[2])&o[2]);
#pragma unroll
          for (int s = 0; s < 4; ++s) {
            const int P = j * 8 + 2 * s + hh, hp = P / 7, rr = P - hp * 7;
            const int hd = 2 * hp + (rr > 3), sg = rr > 3 ? rr - 4 : rr;
            const unsigned ad = WS_KV + hd * 262144 + rt * 4096 + sg * 512 + r32 * 16;
            u32x4 v = o[s]; if (rr == 3) { v[2] = 0x3f803f80u; v[3] = 0x00003f80u; }
            ST16(v, ad);
            if (rr == 3) { const u32x4 e = {o[s][2], o[s][3], 0x3f803f80u, 0x00003f80u}; ST16(e, ad + 262144); }
          }
        } else {
          epi_T<true>(a0, cr0, rs, nmb, *(u32x4(*)[2])&o[0]); epi_T<true>(a1, cr1, rs, nmb, *(u32x4(*)[2])&o[2]);
          const unsigned ad = WS_H + rt * 114688 + (j - 14) * 4096 + lane * 16;
#pragma unroll
          for (int s = 0; s < 4; ++s) ST16(o[s], ad + s * 1024);
        }
      }
      STAMP(2);
      const int rho = 4 * g16 + p4, sg = 2 * (rho >> 2) + (rho & 1), half = (rho >> 1) & 1;
      const int g = (j & 1) ? (sg < 3 ? 2 * sg + 1 + half : (half ? 7 : 0)) : 2 * sg + half;
      lptr qb[4];
#pragma unroll
      for (int s = 0; s < 4; ++s) qb[s] = lds + L_WQ + (8 * hh + q4) * 64 + g * 8 + (s ^ rot) * 1024;
      a0 = (f32x16){}; c0 = (f32x4){};
      bf16x8 wf[3];
      wf[0] = trfrag(qb[0], 256); wf[1] = trfrag(qb[1], 256);
#pragma unroll
      for (int st = 0; st < 28; ++st) {
        if (st + 2 < 28) wf[(st + 2) % 3] = trfrag(qb[(st + 2) & 3] + ((st + 2) >> 2) * 4096, 256);
        SBAR();
        a0 = MFMA32(wf[st % 3], xa[st], a0);
        c0 = MFMA16(sel, wf[st % 3], c0);
        PIN(a0); PIN(c0); SBAR();
      }
      if (lane < 16) { *(LAS float*)(priv + lane * 4) = c0[0]; *(LAS float*)(priv + 64 + lane * 4) = c0[1]; }
      float cr[16];
#pragma unroll
      for (int i = 0; i < 4; ++i) { const f32x4 q = ldsf4(priv + (8 * i + 4 * hh) * 4); cr[4 * i] = q[0]; cr[4 * i + 1] = q[1]; cr[4 * i + 2] = q[2]; cr[4 * i + 3] = q[3]; }
      u32x4 o[2]; epi_T<false>(a0, cr, rs * QSCALE, nmb, o);
      if (hh) { o[1][2] = 0u; o[1][3] = 0u; }
      *(LAS u32x4*)(lds + L_QX + t * 2048 + lane * 16) = o[0]; *(LAS u32x4*)(lds + L_QX + t * 2048 + 1024 + lane * 16) = o[1];
    }
    cnt_add(lds, C_Q);
    STAMP(3);
    GET_TAG();
    WAIT_VM(0);
    if (cnt_add(lds, C_KV) == 3u && lane == 0) __hip_atomic_store((GAS u64*)(ws + WS_FKV + fme), tag, RLX_AGENT);
    if (cnt_add(lds, C_H) == 7u && lane == 0) __hip_atomic_store((GAS u64*)(ws + WS_FH + fme), tag, RLX_AGENT);
    STAMP(4);
    { const int c0 = (28 * j) >> 6, c1 = (28 * j + 27) >> 6, sel4 = lane >> 4;
      int jj = sel4 == 0 ? c0 : sel4 == 1 ? c1 : sel4 == 2 ? 7 + c0 : 7 + c1;
      if (j == 0 && (sel4 & 1)) jj = 14 + (sel4 >> 1);
      poll_flags(ws + WS_FKV + ((lane & 15) * 16 + jj) * 128, tag); }
  if (tid == 0) __hip_atomic_store((GAS u64*)(ws + 0x40000 + 2 * 2048 + blockIdx.x * 8), (u64)__builtin_amdgcn_s_memrealtime(), RLX_AGENT);
    const unsigned kvb = WS_KV + j * 262144 + t * 65536 + lane * 16;
    u32x4 kv[4][4];
#pragma unroll
    for (int u = 0; u < 3; ++u)
#pragma unroll
      for (int e = 0; e < 4; ++e) kv[u][e] = LD16(kvb + u * 4096 + e * 1024);
    cnt_wait(lds, C_Q, 4u);
#pragma unroll
    for (int q = 0; q < 4; ++q) { const lptr a = lds + L_QX + ((q + t) & 3) * 2048 + lane * 16; qf[q][0] = ldsb128(a); qf[q][1] = ldsb128(a + 1024); }
    cnt_add(lds, C_QR);
    const f32x16 zero = {};
    f32x16 o[4]; float mref[4]; bool redo = false;
#pragma unroll
    for (int q = 0; q < 4; ++q) {
      f32x16 s = MFMA32(asfrag(kv[0][0]), qf[q][0], zero); s = MFMA32(asfrag(kv[0][1]), qf[q][1], s);
      mref[q] = __builtin_ceilf(tile_max(s)) + REF_MARGIN; redo |= !(__builtin_fabsf(mref[q]) < 16777216.f);
      set_ref(qf[q][1], mref[q], hh); o[q] = zero;
    }
    STAMP(6);
    __builtin_amdgcn_s_setprio(2);
    f32x16 sA, sB; u32x4 pA0 = {}, pA1 = {}, pB0 = {}, pB1 = {};
    sA = MFMA32(asfrag(kv[0][0]), qf[0][0], zero); sA = MFMA32(asfrag(kv[0][1]), qf[0][1], sA);
    kv[3][2] = (u32x4){}; kv[3][3] = (u32x4){}; if (ABL_NOKV) { kv[3][0] = kv[0][0]; kv[3][1] = kv[0][1]; }
#define EX4(S, i) do { S[i] = __builtin_amdgcn_exp2f(S[i]); S[i + 1] = __builtin_amdgcn_exp2f(S[i + 1]); S[i + 2] = __builtin_amdgcn_exp2f(S[i + 2]); S[i + 3] = __builtin_amdgcn_exp2f(S[i + 3]); } while (0)
#define UNIT(SC, SN, PC0, PC1, PP0, PP1, KN0, KN1, QN, QP, VV0, VV1) do { \
      SBAR(); SN = MFMA32(asfrag(KN0), qf[QN][0], zero); PIN(SN); SBAR(); EX4(SC, 0); PIN(SC); SBAR(); \
      SN = MFMA32(asfrag(KN1), qf[QN][1], SN); PIN(SN); SBAR(); EX4(SC, 4); PIN(SC); SBAR(); \
      o[QP] = MFMA32(asfrag(VV0), asfrag(PP0), o[QP]); PIN(o[QP]); SBAR(); EX4(SC, 8); PIN(SC); SBAR(); \
      o[QP] = MFMA32(asfrag(VV1), asfrag(PP1), o[QP]); PIN(o[QP]); SBAR(); EX4(SC, 12); SBAR(); \
      PC0 = (u32x4){cvtpk(SC[0], SC[1]), cvtpk(SC[2], SC[3]), cvtpk(SC[4], SC[5]), cvtpk(SC[6], SC[7])}; \
      PC1 = (u32x4){cvtpk(SC[8], SC[9]), cvtpk(SC[10], SC[11]), cvtpk(SC[12], SC[13]), cvtpk(SC[14], SC[15])}; PIN(PC0); PIN(PC1); SBAR(); } while (0)
#define KEYTILE(U, UN, UL) do { \
      UNIT(sA, sB, pA0, pA1, pB0, pB1, kv[U][0], kv[U][1], 1, 3, kv[UL][2], kv[UL][3]); \
      if (!ABL_NOKV) { const int nt = min(kt + (U) + 3, 15) * 4096; _Pragma("unroll") for (int e = 0; e < 4; ++e) kv[UL][e] = __builtin_amdgcn_raw_buffer_load_b128(rsw, kvb + e * 1024, nt, LDAUX); } \
      UNIT(sB, sA, pB0, pB1, pA0, pA1, kv[U][0], kv[U][1], 2, 0, kv[U][2], kv[U][3]); \
      UNIT(sA, sB, pA0, pA1, pB0, pB1, kv[U][0], kv[U][1], 3, 1, kv[U][2], kv[U][3]); \
      UNIT(sB, sA, pB0, pB1, pA0, pA1, kv[UN][0], kv[UN][1], 0, 2, kv[U][2], kv[U][3]); \
    } while (0)
    for (int kt = 0; kt < 16; kt += 4) {
      KEYTILE(0, 1, 3); KEYTILE(1, 2, 0); KEYTILE(2, 3, 1); KEYTILE(3, 0, 2);
      const float lm = __builtin_fmaxf(__builtin_fmaxf(o[0][12], o[1][12]), __builtin_fmaxf(o[2][12], o[3][12]));
      if (__builtin_expect((__ballot(lm > 16777216.f) >> 32) != 0, 0)) {
        NRESC;
        o[3] = MFMA32(asfrag(kv[3][2]), asfrag(pB0), o[3]); o[3] = MFMA32(asfrag(kv[3][3]), asfrag(pB1), o[3]); pB0 = (u32x4){}; pB1 = (u32x4){};
#pragma unroll
        for (int q = 0; q < 4; ++q) { const float l = upper_bcast(o[q][12]); redo |= !(l < 1.2980742146337069e33f);
          const int e = max(__builtin_amdgcn_frexp_expf(l) + (int)REF_MARGIN, 0);
#pragma unroll
          for (int r = 0; r < 16; ++r) o[q][r] = __builtin_ldexpf(o[q][r], -e);
          mref[q] += (float)e; redo |= !(__builtin_fabsf(mref[q]) < 16777216.f); set_ref(qf[q][1], mref[q], hh);
          if (q == 0) {
#pragma unroll
            for (int r = 0; r < 16; ++r) sA[r] -= (float)e; } } }
    }
    cnt_add(lds, C_DONE);
    o[3] = MFMA32(asfrag(kv[3][2]), asfrag(pB0), o[3]); o[3] = MFMA32(asfrag(kv[3][3]), asfrag(pB1), o[3]);
    { float z[4] = {0.f, 0.f, 0.f, 0.f};
#pragma unroll
      for (int r = 0; r < 16; ++r)
#pragma unroll
        for (int q = 0; q < 4; ++q) z[q] = __builtin_fmaf(o[q][r], 0.f, z[q]);
      const float zz = (z[0] + z[1]) + (z[2] + z[3]); redo |= !(zz == 0.f); }
    if (__builtin_expect(__any(redo), 0)) {
#pragma unroll
      for (int q = 0; q < 4; ++q) { set_ref(qf[q][1], 0.f, hh); o[q] = zero; mref[q] = -3.0e38f; }
      for (int kt = 0; kt < 16; ++kt) {
        const u32x4 k0 = LD16(kvb + kt * 4096), k1 = LD16(kvb + kt * 4096 + 1024), v0 = LD16(kvb + kt * 4096 + 2048), v1 = LD16(kvb + kt * 4096 + 3072);
#pragma unroll
        for (int q = 0; q < 4; ++q) {
          f32x16 s = MFMA32(asfrag(k0), qf[q][0], zero); s = MFMA32(asfrag(k1), qf[q][1], s);
          const float mn = __builtin_fmaxf(mref[q], tile_max(s)), al = __builtin_amdgcn_exp2f(mref[q] - mn); mref[q] = mn;
#pragma unroll
          for (int r = 0; r < 16; ++r) { o[q][r] *= al; s[r] = __builtin_amdgcn_exp2f(s[r] - mn); }
          const u32x4 p0 = {cvtpk(s[0], s[1]), cvtpk(s[2], s[3]), cvtpk(s[4], s[5]), cvtpk(s[6], s[7])}, p1 = {cvtpk(s[8], s[9]), cvtpk(s[10], s[11]), cvtpk(s[12], s[13]), cvtpk(s[14], s[15])};
          o[q] = MFMA32(asfrag(v0), asfrag(p0), o[q]); o[q] = MFMA32(asfrag(v1), asfrag(p1), o[q]);
        }
      }
    }
  if (tid == 0) __hip_atomic_store((GAS u64*)(ws + 0x40000 + 3 * 2048 + blockIdx.x * 8), (u64)__builtin_amdgcn_s_memrealtime(), RLX_AGENT);
    VSTAMP(28, nresc + (__any(redo) ? 1000 : 0));
    cnt_wait(lds, C_QR, 4u); cnt_wait(lds, C_H, 8u);
    auto area = [&](int dst, int e) -> lptr { return lds + (dst * 3 + e) * 4096 + lane * 16; };
#pragma unroll
    for (int q = 1; q < 4; ++q) {
      const lptr a = area((q + t) & 3, q - 1);
      o[q][13] = hh ? mref[q] : o[q][13];
#pragma unroll
      for (int i = 0; i < 4; ++i) *(LAS f32x4*)(a + i * 1024) = (f32x4){o[q][4 * i], o[q][4 * i + 1], o[q][4 * i + 2], o[q][4 * i + 3]};
    }
    f32x16 of = o[0]; const float mown = mref[0];
    cnt_add(lds, C_CMB); STAMP(9); cnt_wait(lds, C_CMB, 4u);
    { f32x4 pp[3][4]; float ms[3];
#pragma unroll
      for (int e = 0; e < 3; ++e) { const lptr a = area(t, e);
#pragma unroll
        for (int i = 0; i < 4; ++i) pp[e][i] = ldsf4(a + i * 1024);
        ms[e] = upper_bcast(pp[e][3][1]); }
      const float mx = __builtin_fmaxf(__builtin_fmaxf(mown, ms[0]), __builtin_fmaxf(ms[1], ms[2]));
      const float f0 = __builtin_amdgcn_exp2f(mown - mx - 2.f), f1 = __builtin_amdgcn_exp2f(ms[0] - mx - 2.f), f2 = __builtin_amdgcn_exp2f(ms[1] - mx - 2.f), f3 = __builtin_amdgcn_exp2f(ms[2] - mx - 2.f);
#pragma unroll
      for (int r = 0; r < 16; ++r) of[r] = of[r] * f0 + pp[0][r >> 2][r & 3] * f1 + pp[1][r >> 2][r & 3] * f2 + pp[2][r >> 2][r & 3] * f3;
      const float inv = __builtin_amdgcn_rcpf(upper_bcast(of[12]));
      u32x4 e0, e1;
#pragma unroll
      for (int p = 0; p < 4; ++p) { e0[p] = cvtpk(of[2 * p] * inv, of[2 * p + 1] * inv); e1[p] = cvtpk(of[8 + 2 * p] * inv, of[9 + 2 * p] * inv); }
      if (hh) { e1[2] = 0u; e1[3] = 0u; }
      const unsigned a = WS_O + rt * 32768 + j * 2048 + lane * 16;
      ST16(e0, a); ST16(e1, a + 1024); }
    STAMP(10);
    WAIT_VM(0);
    if (cnt_add(lds, C_O) == 3u && lane == 0) __hip_atomic_store((GAS u64*)(ws + WS_FO + fme), tag, RLX_AGENT);
    STAMP(8); CSTAMP(31); STAMP_OUT();
  } else {
    Ld k;
    { const int row = 16 * t + (lane >> 3); const unsigned piece = ((lane & 7) ^ (((row >> 1) & 1) << 2)) << 4;
      k.xsrc = x + rb * 128 * 896; k.w0rs = j < 14 ? 2688u : 3584u; k.w0src = j < 14 ? wqkv + 896 + j * 128 : w1 + (j - 14) * 128;
      k.fasrc = w1 + (2 + j) * 128; k.fbsrc = w1 + (12 + j) * 128; k.dst = lds0 + t * 2048;
      k.w0vo = row * k.w0rs + piece; k.wfvo = row * 3584 + piece;
      k.wqsrc = wqkv + j * 56; k.wqvo = (32 * t + (lane >> 2)) * 2688 + (lane & 3) * 16; k.wq3 = t < 2 ? 384 * 2688 : 0;
      k.xvo0 = row * 896 + (((lane & 7) ^ (lane >> 4)) << 4); k.xvo1 = k.xvo0 ^ 64u; }
    u64 tagraw;
    {
      ld_items<0, PRO>(k);
      tagraw = __hip_atomic_load((GAS u64*)(ws + WS_EPOCH), RLX_AGENT);
      COMMON_SETUP();
      f32x4 s1 = {}, s2 = {};
      p1_load<0>(k, xa, s1, s2, xrd, xwrap STA);
      STAMP(1);
      { const float q = (lane & 2) ? ((lane & 1) ? s2[3] : s2[2]) : ((lane & 1) ? s2[1] : s2[0]);
        const float a = swap_add(s1[0]), bb = swap_add(q); if (hh == 0) *(LAS f32x2*)(lds + L_STAT + (t * 32 + r32) * 8) = (f32x2){a, bb}; }
      asm volatile("s_waitcnt vmcnt(%0) lgkmcnt(0)\n\ts_barrier" :: "i"(2 * (n_issued(6) - 28)) : "memory");
      row_norm(lds, t, r32, rs, nmb);
#pragma unroll
      for (int s = 0; s < 4; ++s) { wr0[s] += L_FA; wr1[s] += L_FA; PIN(wr0[s]); PIN(wr1[s]); }
      bf16x8 f0[2], f1[2]; f32x16 a0 = {}, a1 = {}; f32x4 c0 = {}, c1 = {}; float cr[16]; u32x4 o[2];
      f0[0] = trfrag(wr0[0], 512); f1[0] = trfrag(wr1[0], 512);
      f_steps<0, 0>(k, lds, xa, f0, f1, a0, a1, c0, c1, sel, wr0, wr1);
      cs_put(lds, L_CSA, 0, t, lane, c0); cs_put(lds, L_CSA, 1, t, lane, c1);
      WAIT_VM(0); cnt_add(lds, C_CSA); cnt_wait(lds, C_CSA, 4u);
      STAMP(2);
      { const unsigned ad = WS_H + rt * 114688 + (2 + j) * 4096 + lane * 16;
        lane_to_regs(priv, r32, hh, cs_get(lds, L_CSA, 0, r32), cr); epi_T<true>(a0, cr, rs, nmb, o); ST16(o[0], ad); ST16(o[1], ad + 1024);
        lane_to_regs(priv + 128, r32, hh, cs_get(lds, L_CSA, 1, r32), cr); epi_T<true>(a1, cr, rs, nmb, o); ST16(o[0], ad + 2048); ST16(o[1], ad + 3072); }
      STAMP(3);
      if (j >= 6) {
#pragma unroll
        for (int s = 0; s < 4; ++s) { wr0[s] += L_FB - L_FA; wr1[s] += L_FB - L_FA; PIN(wr0[s]); PIN(wr1[s]); }
        a0 = (f32x16){}; a1 = (f32x16){}; c0 = (f32x4){}; c1 = (f32x4){};
        f0[0] = trfrag(wr0[0], 512); f1[0] = trfrag(wr1[0], 512);
        f_steps<1, 0>(k, lds, xa, f0, f1, a0, a1, c0, c1, sel, wr0, wr1);
        cs_put(lds, L_CSB, 0, t, lane, c0); cs_put(lds, L_CSB, 1, t, lane, c1);
        cnt_add(lds, C_CSB); cnt_wait(lds, C_CSB, 4u);
        const unsigned ad = WS_H + rt * 114688 + (12 + j) * 4096 + lane * 16;
        lane_to_regs(priv, r32, hh, cs_get(lds, L_CSB, 0, r32), cr); epi_T<true>(a0, cr, rs, nmb, o); ST16(o[0], ad); ST16(o[1], ad + 1024);
        lane_to_regs(priv + 128, r32, hh, cs_get(lds, L_CSB, 1, r32), cr); epi_T<true>(a1, cr, rs, nmb, o); ST16(o[0], ad + 2048); ST16(o[1], ad + 3072);
      }
    }
    GET_TAG();
    WAIT_VM(0);
    if (cnt_add(lds, C_H) == 7u && lane == 0) __hip_atomic_store((GAS u64*)(ws + WS_FH + fme), tag, RLX_AGENT);
    STAMP(4);
    const int cg = bi >> 2;
    if (cg < 7) {
      const int rt0 = 8 * xg + 2 * (bi & 3), rb3 = rt0 >> 2;
      const int ring = L_RING + 3 * t * SLOT;
      const int ct = t >> 1, rtl = t & 1;
      G2 g; g.lds = lds; g.t = t; g.ringdst = lds0 + ring; g.lv = lane * 16;
      g.w2vo = (lane >> 3) * 896 + (((lane & 7) ^ (((lane >> 4) & 1) << 2)) << 4);
      g.w2s = w2 + t * 448 * 896 + cg * 128; g.wos = wo + t * 112 * 896 + cg * 128;
      g.hb0 = ws + WS_H + (rt0 + rtl) * 114688 + t * 28672; g.hb1 = ws + WS_H + (rt0 + (rtl ^ 1)) * 114688 + t * 28672;
      g.br0 = wrd0 + ring + ((ct ^ (q4 >> 1)) << 6); g.br1 = wrd0 + ring + ((ct ^ 1 ^ (q4 >> 1)) << 6);
      g2_ws<0, 12>(g);
      const int fb3 = rb3 * 16 + (lane & 15);
      poll_flags(ws + WS_FH + fb3 * 128, tag);
      WAIT_VM(0);
      STAMP(5);
      u32x4 hf[14][2];
#pragma unroll
      for (int s = 0; s < 14; ++s) { ldg_sc1(hf[s][0], g.hb0 + s * 1024, g.lv); ldg_sc1(hf[s][1], g.hb1 + s * 1024, g.lv); }
      f32x16 a00 = {}, a01 = {}, a10 = {}, a11 = {};
      PIN(g.br0); PIN(g.br1);
      { bf16x8 f0[2], f1[2]; f0[0] = trfrag(g.br0, 512); f1[0] = trfrag(g.br1, 512);
        g2_steps<0>(g, hf, f0, f1, a00, a01, a10, a11); }
      STAMP(6);
      WAIT_VM(0);
      bf16x8 wf[8][2];
      { const lptr o0 = lds + ring + SLOT + (4 * hh + q4) * 128 + pi_std * 8;
#pragma unroll
        for (int e = 0; e < 8; ++e)
#pragma unroll
          for (int cc = 0; cc < 2; ++cc) wf[e][cc] = trfrag(o0 + ((cc ^ ct ^ (q4 >> 1)) << 6) + ((e >> 1) * 28 + (e & 1) * 16) * 128, 1024); }
      const size_t oo = (size_t)((rt0 + rtl) * 32 + r32) * 896 + cg * 128 + ct * 64 + hh * 16;
      const u32x4 xr0 = *(const GAS u32x4*)(x + oo), xr1 = *(const GAS u32x4*)(x + oo + 32);
      poll_flags(ws + WS_FO + fb3 * 128, tag);
      STAMP(7);
      { const unsigned ob0 = WS_O + (rt0 + rtl) * 32768 + t * 8192 + lane * 16, ob1 = ob0 ^ 32768u;
        u32x4 of[8][2];
#pragma unroll
        for (int e = 0; e < 8; ++e) { of[e][0] = LD16(ob0 + e * 1024); of[e][1] = LD16(ob1 + e * 1024); }
        SBAR();
#pragma unroll
        for (int e = 0; e < 8; ++e) { const bf16x8 h0 = asfrag(of[e][0]), h1 = asfrag(of[e][1]);
          a11 = MFMA32(wf[e][1], h1, a11); a10 = MFMA32(wf[e][1], h0, a10); a01 = MFMA32(wf[e][0], h1, a01); a00 = MFMA32(wf[e][0], h0, a00);
          PIN(a00); PIN(a01); PIN(a10); PIN(a11); } }
      STAMP(9);
      if (b == 0 && tid == 256) __hip_atomic_store((GAS u64*)(ws + WS_EPOCH), tag, RLX_AGENT);
      { const lptr mine = lds + ring + lane * 16;
        auto put = [&](const f32x16& a, int e) {
#pragma unroll
          for (int i = 0; i < 4; ++i) *(LAS f32x4*)(mine + e * 4096 + i * 1024) = (f32x4){a[4 * i], a[4 * i + 1], a[4 * i + 2], a[4 * i + 3]}; };
        put(a11, 3); put(a10, 2); put(a01, 1); }
      f32x16 fin = a00;
      cnt_add(lds, C_RED); STAMP(10); cnt_wait(lds, C_RED, 4u);
#pragma unroll
      for (int e = 1; e < 4; ++e) { const lptr a = lds + L_RING + 3 * (t ^ e) * SLOT + e * 4096 + lane * 16;
#pragma unroll
        for (int i = 0; i < 4; ++i) { const f32x4 v = ldsf4(a + i * 1024); fin[4 * i] += v[0]; fin[4 * i + 1] += v[1]; fin[4 * i + 2] += v[2]; fin[4 * i + 3] += v[3]; } }
      u32x4 r0, r1;
#pragma unroll
      for (int p = 0; p < 4; ++p) { r0[p] = cvtpk(fin[2 * p] + bflo(xr0[p]), fin[2 * p + 1] + bfhi(xr0[p])); r1[p] = cvtpk(fin[8 + 2 * p] + bflo(xr1[p]), fin[9 + 2 * p] + bfhi(xr1[p])); }
      *(GAS u32x4*)(out + oo) = r0; *(GAS u32x4*)(out + oo + 32) = r1;
      STAMP(8);
    }
  if (tid == 256) __hip_atomic_store((GAS u64*)(ws + 0x40000 + 4 * 2048 + blockIdx.x * 8), (u64)__builtin_amdgcn_s_memrealtime(), RLX_AGENT);
    WAIT_VM(0);
    if (tid == 256) __hip_atomic_store((GAS u64*)(ws + 0x50000 + blockIdx.x * 128), tag, RLX_AGENT);
    if (b == 0 && w == 4) { u64 mn = ~0ull, mx = 0;
      for (int i = 0; i < 4; ++i) poll_flags(ws + 0x50000 + (lane * 4 + i) * 128, tag);
      for (int i = 0; i < 4; ++i) { const u64 v = __hip_atomic_load((GAS u64*)(ws + 0x40000 + (lane * 4 + i) * 8), RLX_AGENT); mn = v < mn ? v : mn;
        const u64 e = __hip_atomic_load((GAS u64*)(ws + 0x40000 + METRIC * 2048 + (lane * 4 + i) * 8), RLX_AGENT); mx = e > mx ? e : mx; }
      for (int o = 32; o; o >>= 1) { const u64 a = __shfl_xor(mn, o), c2 = __shfl_xor(mx, o); mn = a < mn ? a : mn; mx = c2 > mx ? c2 : mx; }
      const u64 now = __builtin_amdgcn_s_memrealtime(); u64 X = METRIC ? mx - mn : 0; X = X > 6000 ? 6000 : X;
      while (__builtin_amdgcn_s_memrealtime() - now < X) {} }
  }
}

extern "C" void kernel_launch(void* const* d_in, const int* in_sizes, int n_in, void* d_out, int out_size, void* d_ws, size_t ws_size, hipStream_t stream) {
  fused_block<<<256, 512, 0, stream>>>((const char*)d_in[0], (const char*)d_in[1], (const char*)d_in[2], (const char*)d_in[4], (const char*)d_in[5], (char*)d_out, (char*)d_ws + WS_BASE);
}
```

```cpp
#include <hip/hip_runtime.h>
#include <stdint.h>

typedef short bf16x8 __attribute__((ext_vector_type(8)));
typedef short s16x4 __attribute__((ext_vector_type(4)));
typedef float f32x16 __attribute__((ext_vector_type(16)));
typedef float f32x4 __attribute__((ext_vector_type(4)));
typedef float f32x2 __attribute__((ext_vector_type(2)));
typedef __bf16 bf16x2v __attribute__((ext_vector_type(2)));
typedef unsigned u32x4 __attribute__((ext_vector_type(4)));
typedef unsigned long long u64;
#define LAS __attribute__((address_space(3)))
#define GAS __attribute__((address_space(1)))
#define DI __device__ __forceinline__
#define MFMA32(a, b, c) __builtin_amdgcn_mfma_f32_32x32x16_bf16(a, b, c, 0, 0, 0)
#define MFMA16(a, b, c) __builtin_amdgcn_mfma_f32_16x16x32_bf16(a, b, c, 0, 0, 0)
#define SBAR() __builtin_amdgcn_sched_barrier(0)
#define PIN(x) asm volatile("" : "+v"(x))
#define RLX_AGENT __ATOMIC_RELAXED, __HIP_MEMORY_SCOPE_AGENT
#define WAIT_BAR(N) asm volatile("s_waitcnt vmcnt(" #N ") lgkmcnt(0)\n\ts_barrier" ::: "memory")
#define WAIT_VM(N) asm volatile("s_waitcnt vmcnt(" #N ")" ::: "memory")
#define WAIT_LGKM0() asm volatile("s_waitcnt lgkmcnt(0)" ::: "memory")

constexpr unsigned WS_EPOCH = 0;
constexpr unsigned WS_FKV = 4096, WS_FH = WS_FKV + 32768, WS_FO = WS_FH + 32768;
constexpr unsigned WS_KV = 1u << 20;
constexpr unsigned WS_H = 5u << 20;
constexpr unsigned WS_O = 13u << 20;
constexpr unsigned WS_BYTES = 15u << 20;
constexpr int SLOT = 8192, NSLOT = 19;
constexpr int L_MISC = NSLOT * SLOT;
constexpr int L_STAT = L_MISC;
constexpr int L_CS0 = L_MISC + 2048, L_CSA = L_CS0 + 1024, L_CSB = L_CSA + 1024;
constexpr int L_PRIV = L_MISC + 5120;
constexpr int L_CNT = L_MISC + 7168;
constexpr int LDS_BYTES = 163840;
constexpr int L_WQ = 2 * SLOT, L_FA = 6 * SLOT, L_FB = 13 * SLOT, L_QX = SLOT, L_RING = 6 * SLOT;
constexpr int p1_slot(int item) { return (item % NSLOT) * SLOT; }
enum { C_CSA, C_CSB, C_Q, C_QR, C_KV, C_H, C_F, C_DONE, C_CMB, C_O, C_RED };

#define WS_BASE 0
#define TOUCH 0
#define MAPA 8
#define G2_SPLIT 16
#define CODE_BYTES 49152
constexpr float QSCALE = 0.18898223650461362f * 1.4426950408889634f;
constexpr float REF_MARGIN = 40.f;
constexpr unsigned SPIN_CAP = 1u << 16;

DI unsigned cvtpk(float lo, float hi) { f32x2 v = {lo, hi}; return __builtin_bit_cast(unsigned, __builtin_convertvector(v, bf16x2v)); }
DI float bf16r(float x) { return __uint_as_float(cvtpk(x, 0.f) << 16); }
DI float bflo(unsigned v) { return __uint_as_float(v << 16); }
DI float bfhi(unsigned v) { return __uint_as_float(v & 0xffff0000u); }
DI float swap_add(float v) { auto r = __builtin_amdgcn_permlane32_swap(__float_as_uint(v), __float_as_uint(v), false, false); return __uint_as_float(r[0]) + __uint_as_float(r[1]); }
DI float swap_max(float v) { auto r = __builtin_amdgcn_permlane32_swap(__float_as_uint(v), __float_as_uint(v), false, false); return __builtin_fmaxf(__uint_as_float(r[0]), __uint_as_float(r[1])); }
DI float upper_bcast(float v) { auto r = __builtin_amdgcn_permlane32_swap(__float_as_uint(v), __float_as_uint(v), false, false); return __uint_as_float(r[1]); }
DI void glds16(const void* sbase, unsigned voff, unsigned ldsdst) {
  unsigned keep;
  asm volatile("s_mov_b32 %0, m0\n\ts_mov_b32 m0, %3\n\ts_nop 0\n\tglobal_load_lds_dwordx4 %1, %2\n\ts_mov_b32 m0, %0"
               : "=&s"(keep) : "v"(voff), "s"(sbase), "s"(ldsdst) : "memory");
}
typedef LAS char* lptr;
typedef short v4i16_t __attribute__((ext_vector_type(4)));
DI s16x4 trd(lptr p) { return __builtin_bit_cast(s16x4, __builtin_amdgcn_ds_read_tr16_b64_v4i16((LAS v4i16_t*)p)); }
DI bf16x8 trfrag(lptr p, int second) { s16x4 lo = trd(p), hi = trd(p + second); return __builtin_shufflevector(lo, hi, 0, 1, 2, 3, 4, 5, 6, 7); }
DI bf16x8 ldsb128(lptr p) { return *(LAS bf16x8*)p; }
DI f32x4 ldsf4(lptr p) { return *(LAS f32x4*)p; }
DI bf16x8 asfrag(u32x4 v) { return __builtin_bit_cast(bf16x8, v); }
DI unsigned cnt_add(lptr lds, int c) { unsigned r = 0; WAIT_LGKM0(); if ((threadIdx.x & 63) == 0) r = __hip_atomic_fetch_add((LAS unsigned*)(lds + L_CNT + 4 * c), 1u, __ATOMIC_RELAXED, __HIP_MEMORY_SCOPE_WORKGROUP);
  return __builtin_amdgcn_readfirstlane(r); }
DI void cnt_wait(lptr lds, int c, unsigned n) {
  while (__builtin_amdgcn_readfirstlane(__hip_atomic_load((LAS unsigned*)(lds + L_CNT + 4 * c), __ATOMIC_RELAXED, __HIP_MEMORY_SCOPE_WORKGROUP)) < n) __builtin_amdgcn_s_sleep(1);
  asm volatile("" ::: "memory"); }
DI void poll_flags(const char* flag, u64 tag) {
#pragma nounroll
  for (unsigned s = 0; s < SPIN_CAP; ++s) {
    const u64 v = __hip_atomic_load((GAS u64*)flag, RLX_AGENT);
    if (__all(v == tag)) break;
#define POLLSLEEP 1
    __builtin_amdgcn_s_sleep(POLLSLEEP);
  }
  __builtin_amdgcn_fence(__ATOMIC_ACQUIRE, "wavefront");
  asm volatile("" ::: "memory");
}
#define STAMP(i) do {} while (0)
#define CSTAMP(i) do {} while (0)
#define VSTAMP(i, v) do {} while (0)
#define STAMP_OUT() do {} while (0)
#define NRESC
#define STV
#define STA
#define BAR() asm volatile("s_waitcnt lgkmcnt(0)\n\ts_barrier" ::: "memory")

#define PRO 9
#define RAMP 3
constexpr int n_issued(int c) { return c < 0 ? PRO : (PRO + RAMP * (c + 1) < 19 + 3 * c ? PRO + RAMP * (c + 1) : 19 + 3 * c); }
static_assert(n_issued(6) >= 29, "Wq and the head of F_a must be in flight at B1");
#define MFMA4(a, b, c) __builtin_amdgcn_mfma_f32_4x4x4bf16_1k(a, b, c, 0, 0, 0)
DI void rowstats(const bf16x8& f, const s16x4& ones, f32x4& s1, f32x4& s2) {
  const s16x4 lo = __builtin_shufflevector(f, f, 0, 1, 2, 3), hi = __builtin_shufflevector(f, f, 4, 5, 6, 7);
  s1 = MFMA4(ones, lo, s1); s2 = MFMA4(lo, lo, s2); s1 = MFMA4(ones, hi, s1); s2 = MFMA4(hi, hi, s2);
}
template <int C> DI void x_frags(bf16x8 (&xa)[28], const lptr (&xrd)[4], int xwrap) {
#pragma unroll
  for (int s = 0; s < 4; ++s) xa[4 * C + s] = ldsb128(xrd[s] + p1_slot(3 * C) + (C == 6 ? xwrap : 0));
}
template <bool VT, int C> DI void p1_compute(bf16x8 (&xa)[28], f32x16& a0, f32x16& a1, f32x4& c0, f32x4& c1, const bf16x8& sel,
                                             const lptr (&xrd)[4], int xwrap, const lptr (&w0)[4], const lptr (&w1)[4] STV) {
  if constexpr (C < 7) {
    BAR();
    if (C == 0) STAMP(12);
    bf16x8 f0[4], f1[4];
#pragma unroll
    for (int s = 0; s < 4; ++s) {
      xa[4 * C + s] = ldsb128(xrd[s] + p1_slot(3 * C) + (C == 6 ? xwrap : 0));
      f0[s] = trfrag(w0[s] + p1_slot(3 * C + 2), 512); f1[s] = trfrag(w1[s] + p1_slot(3 * C + 2), 512);
    }
#pragma unroll
    for (int s = 0; s < 4; ++s) {
      if constexpr (VT) { a0 = MFMA32(xa[4 * C + s], f0[s], a0); a1 = MFMA32(xa[4 * C + s], f1[s], a1); }
      else { a0 = MFMA32(f0[s], xa[4 * C + s], a0); a1 = MFMA32(f1[s], xa[4 * C + s], a1); }
      if (s == 0) { c0 = MFMA16(sel, f0[0], c0); c1 = MFMA16(sel, f1[0], c1); }
    }
    PIN(a0); PIN(a1); PIN(c0); PIN(c1);
    p1_compute<VT, C + 1>(xa, a0, a1, c0, c1, sel, xrd, xwrap, w0, w1 STA);
  }
}
struct Ld { const char *xsrc, *w0src, *wqsrc, *fasrc, *fbsrc; unsigned xvo0, xvo1, w0vo, wfvo, wqvo, wq3, w0rs, dst; };
template <int N, int E> DI void ld_piece(const Ld& k) {
  const unsigned dst = k.dst + p1_slot(N) + E * 1024;
  if constexpr (N < 21) {
    constexpr int c = N / 3, q = N % 3;
    if constexpr (q < 2) glds16(k.xsrc + (q * 64 + E * 8) * 896 + c * 128, E ? k.xvo1 : k.xvo0, dst);
    else glds16(k.w0src + (size_t)(c * 64 + E * 8) * k.w0rs, k.w0vo, dst);
  } else if constexpr (N < 24) glds16(k.wqsrc + ((N - 21) * 128 + E * 16) * 2688, k.wqvo, dst);
  else if constexpr (N == 24) glds16(k.wqsrc + k.wq3 + E * 16 * 2688, k.wqvo, dst);
  else if constexpr (N < 32) glds16(k.fasrc + ((N - 25) * 64 + E * 8) * 3584, k.wfvo, dst);
  else glds16(k.fbsrc + ((N - 32) * 64 + E * 8) * 3584, k.wfvo, dst);
}
template <int A, int B> DI void ld_items(const Ld& k) { if constexpr (A < B) { ld_piece<A, 0>(k); ld_piece<A, 1>(k); ld_items<A + 1, B>(k); } }
template <int C> DI void p1_load(const Ld& k, bf16x8 (&xa)[28], f32x4& s1, f32x4& s2, const lptr (&xrd)[4], int xwrap STV) {
  if constexpr (C < 7) {
    asm volatile("s_waitcnt vmcnt(%0) lgkmcnt(0)\n\ts_barrier" :: "i"(2 * (n_issued(C - 1) - 3 * (C + 1))) : "memory");
    if (C == 0) STAMP(12);
    x_frags<C>(xa, xrd, xwrap);
    SBAR(); ld_items<n_issued(C - 1), n_issued(C)>(k); SBAR();
    const s16x4 ones = {0x3f80, 0x3f80, 0x3f80, 0x3f80};
#pragma unroll
    for (int s = 0; s < 4; ++s) rowstats(xa[4 * C + s], ones, s1, s2);
    PIN(s1); PIN(s2);
    p1_load<C + 1>(k, xa, s1, s2, xrd, xwrap STA);
  }
}
DI void cs_put(lptr lds, int area, int c, int t, int lane, const f32x4& cs) {
  if (lane < 16) { *(LAS float*)(lds + area + c * 512 + lane * 16 + t * 4) = cs[0]; *(LAS float*)(lds + area + c * 512 + (lane + 16) * 16 + t * 4) = cs[1]; }
}
DI float cs_get(lptr lds, int area, int c, int r32) { const f32x4 p = ldsf4(lds + area + c * 512 + r32 * 16); return (p[0] + p[1]) + (p[2] + p[3]); }
DI void lane_to_regs(lptr priv, int r32, int hh, float v, float (&o)[16]) {
  *(LAS float*)(priv + r32 * 4) = v;
#pragma unroll
  for (int i = 0; i < 4; ++i) { const f32x4 q = ldsf4(priv + (8 * i + 4 * hh) * 4); o[4 * i] = q[0]; o[4 * i + 1] = q[1]; o[4 * i + 2] = q[2]; o[4 * i + 3] = q[3]; }
}
template <bool RELU> DI void epi_T(const f32x16& acc, const float (&cs)[16], float sc, float nmb, u32x4 (&o)[2]) {
#pragma unroll
  for (int p = 0; p < 8; ++p) {
    float a = sc * __builtin_fmaf(nmb, cs[2 * p], acc[2 * p]), b = sc * __builtin_fmaf(nmb, cs[2 * p + 1], acc[2 * p + 1]);
    if (RELU) { a = __builtin_fmaxf(a, 0.f); b = __builtin_fmaxf(b, 0.f); }
    o[p >> 2][p & 3] = cvtpk(a, b);
  }
}
#define ST16(v, off) __builtin_amdgcn_raw_buffer_store_b128(v, rsw, off, 0, 16)
#define ABL_NOKV 0
#define LDAUX 16
#define LD16(off) __builtin_amdgcn_raw_buffer_load_b128(rsw, off, 0, LDAUX)

DI void set_ref(bf16x8& q1, float M, int hh) {
  const float n = -M; const float h = __uint_as_float(__float_as_uint(n) & 0xffff0000u), r = n - h;
  const float m = __uint_as_float(__float_as_uint(r) & 0xffff0000u), l = r - m;
  u32x4 v = __builtin_bit_cast(u32x4, q1);
  const unsigned w2 = (__float_as_uint(h) >> 16) | (__float_as_uint(m) & 0xffff0000u), w3 = __float_as_uint(l) >> 16;
  v[2] = hh ? w2 : v[2]; v[3] = hh ? w3 : v[3];
  q1 = __builtin_bit_cast(bf16x8, v);
}
DI float tile_max(const f32x16& s) {
  float a = __builtin_fmaxf(__builtin_fmaxf(s[0], s[1]), s[2]), b = __builtin_fmaxf(__builtin_fmaxf(s[3], s[4]), s[5]);
#pragma unroll
  for (int r = 6; r < 14; r += 4) { a = __builtin_fmaxf(__builtin_fmaxf(a, s[r]), s[r + 1]); b = __builtin_fmaxf(__builtin_fmaxf(b, s[r + 2]), s[r + 3]); }
  a = __builtin_fmaxf(__builtin_fmaxf(a, s[14]), s[15]);
  return swap_max(__builtin_fmaxf(a, b));
}

DI void row_norm(lptr lds, int t, int r32, float& rs, float& nmb) {
  const f32x2 p = *(LAS f32x2*)(lds + L_STAT + (t * 32 + r32) * 8);
  const float mean = p[0] * (1.f / 448.f), var = __builtin_fmaxf(p[1] * (1.f / 448.f) - mean * mean, 0.f);
  const float sd = bf16r(__builtin_amdgcn_sqrtf(bf16r(bf16r(var) + 1e-5f))); rs = __builtin_amdgcn_rcpf(sd); nmb = -bf16r(mean);
}
template <int P, int ST> DI void f_steps(const Ld& k, lptr lds, const bf16x8 (&xa)[28], bf16x8 (&f0)[2], bf16x8 (&f1)[2], f32x16& a0, f32x16& a1, f32x4& c0, f32x4& c1,
                                         const bf16x8& sel, const lptr (&w0)[4], const lptr (&w1)[4]) {
  if constexpr (ST < 28) {
    if constexpr (P == 0 && ST == 6) { constexpr int n = 2 * n_issued(6) + 6 < 78 ? 2 * n_issued(6) + 6 : 78; asm volatile("s_waitcnt vmcnt(%0)" :: "i"(n - 64) : "memory"); cnt_add(lds, C_F); }
    if constexpr (P == 0 && ST == 11) cnt_wait(lds, C_F, 4u);
    if constexpr (ST + 1 < 28) { constexpr int off = p1_slot((P ? 32 : 25) + ((ST + 1) >> 2)) - (P ? L_FB : L_FA);
      f0[(ST + 1) & 1] = trfrag(w0[(ST + 1) & 3] + off, 512); f1[(ST + 1) & 1] = trfrag(w1[(ST + 1) & 3] + off, 512); }
    SBAR();
    a0 = MFMA32(f0[ST & 1], xa[ST], a0); a1 = MFMA32(f1[ST & 1], xa[ST], a1);
    if constexpr ((ST & 3) == 0) { c0 = MFMA16(sel, f0[ST & 1], c0); c1 = MFMA16(sel, f1[ST & 1], c1); }
    PIN(a0); PIN(a1); PIN(c0); PIN(c1); SBAR();
    if constexpr (P == 0 && n_issued(6) + (ST >> 1) < 39) ld_piece<n_issued(6) + (ST >> 1), ST & 1>(k);
    f_steps<P, ST + 1>(k, lds, xa, f0, f1, a0, a1, c0, c1, sel, w0, w1);
  }
}

constexpr int g2_issued(int st) { int n = 28; for (int s = 0; s < st; ++s) { if (s < 14) n += 2; if (s < 24) n += 2; } return n; }
constexpr int g2_pos_h(int s) { return s < 14 ? 2 * s + 1 : g2_issued(s - 14) + 1; }
constexpr int g2_pos_w(int s) { return s < 12 ? -1 : g2_issued(s - 12) + (s - 12 < 14 ? 2 : 0) + 1; }
constexpr int g2_wait(int st) { const int a = g2_pos_h(st), b = st + 1 < 28 ? g2_pos_w(st + 1) : -1; const int n = g2_issued(st) - 1 - (a > b ? a : b); return n > 63 ? 63 : n; }
DI void ldg_sc1(u32x4& d, const char* sbase, unsigned voff) { asm volatile("global_load_dwordx4 %0, %1, %2 sc1" : "=v"(d) : "v"(voff), "s"(sbase) : "memory"); }
struct G2 { const char *hb0, *hb1, *w2s, *wos; unsigned lv, w2vo, ringdst; int t; lptr br0, br1, lds; };
template <int ST> DI void g2_w(const G2& g) {
#pragma unroll
  for (int e = 0; e < 2; ++e) {
    const unsigned dst = g.ringdst + (ST % 12) * 2048 + e * 1024;
    if constexpr (ST < 28) glds16(g.w2s + (ST * 16 + e * 8) * 896, g.w2vo, dst);
    else if constexpr (ST < 35) glds16(g.wos + ((ST - 28) * 16 + e * 8) * 896, g.w2vo, dst);
    else glds16(g.wos + (g.t == 3 ? 104 : 112 + e * 8) * 896, g.w2vo, dst);
  }
}
template <int A, int B> DI void g2_ws(const G2& g) { if constexpr (A < B) { g2_w<A>(g); g2_ws<A + 1, B>(g); } }
template <int ST> DI void g2_steps(const G2& g, u32x4 (&hf)[14][2], bf16x8 (&f0)[2], bf16x8 (&f1)[2], f32x16& a00, f32x16& a01, f32x16& a10, f32x16& a11) {
  if constexpr (ST < 28) {
    if constexpr (ST == G2_SPLIT) cnt_wait(g.lds, C_DONE, 4u);
    asm volatile("s_waitcnt vmcnt(%2)" : "+v"(hf[ST % 14][0]), "+v"(hf[ST % 14][1]) : "i"(g2_wait(ST)) : "memory");
    if constexpr (ST + 1 < 28) { f0[(ST + 1) & 1] = trfrag(g.br0 + ((ST + 1) % 12) * 2048, 512); f1[(ST + 1) & 1] = trfrag(g.br1 + ((ST + 1) % 12) * 2048, 512); }
    SBAR();
    const bf16x8 h0 = asfrag(hf[ST % 14][0]), h1 = asfrag(hf[ST % 14][1]);
    a00 = MFMA32(f0[ST & 1], h0, a00); a01 = MFMA32(f0[ST & 1], h1, a01); a10 = MFMA32(f1[ST & 1], h0, a10); a11 = MFMA32(f1[ST & 1], h1, a11);
    PIN(a00); PIN(a01); PIN(a10); PIN(a11); SBAR();
    if constexpr (ST < 14) { ldg_sc1(hf[ST][0], g.hb0 + (ST + 14) * 1024, g.lv); ldg_sc1(hf[ST][1], g.hb1 + (ST + 14) * 1024, g.lv); }
    if constexpr (ST < 24) g2_w<ST + 12>(g);
    g2_steps<ST + 1>(g, hf, f0, f1, a00, a01, a10, a11);
  }
}

__global__ void __launch_bounds__(512, 2) fused_block(const char* __restrict__ x, const char* __restrict__ wqkv, const char* __restrict__ wo,
                                                      const char* __restrict__ w1, const char* __restrict__ w2, char* __restrict__ out, char* ws) {
  __shared__ __attribute__((aligned(16))) char smem[LDS_BYTES];
  u64 pc; asm volatile("s_getpc_b64 %0" : "=s"(pc));
  const lptr lds = (lptr)smem; const unsigned lds0 = (unsigned)(uintptr_t)lds;
  const int tid = threadIdx.x, lane = tid & 63, r32 = lane & 31, hh = lane >> 5;
  const int w = __builtin_amdgcn_readfirstlane(tid >> 6), t = w & 3, c = w >> 2;
  const int b = blockIdx.x, xg = b & 7, bi = b >> 3;
  const int j = (xg & 3) * 4 + (bi >> 3), rb = (xg >> 2) * 8 + (bi & 7);
  const int fme = (rb * 16 + j) * 128, rt = rb * 4 + t;
  STAMP(0); CSTAMP(30); VSTAMP(29, (unsigned)__builtin_amdgcn_s_getreg(20 | (3 << 11)) & 0xfu);
  const int rot = t;
  const int q4 = (lane & 15) >> 2, p4 = lane & 3, g16 = (lane >> 4) & 1;
  const int pi_std = 4 * g16 + 2 * (p4 & 1) + (p4 >> 1);
  const lptr wrd0 = lds + (8 * hh + q4) * 128 + pi_std * 8;
  const lptr priv = lds + L_PRIV + w * 256;
  const __amdgpu_buffer_rsrc_t rsw = __builtin_amdgcn_make_buffer_rsrc(ws, 0, WS_BYTES, 0x00020000);
#define COMMON_SETUP() \
  lptr xrd[4], wr0[4], wr1[4]; \
  _Pragma("unroll") for (int s = 0; s < 4; ++s) { xrd[s] = lds + (t * 32 + r32) * 128 + (((2 * (s ^ rot) + hh) ^ ((r32 >> 1) & 7)) << 4); \
    wr0[s] = wrd0 + ((q4 >> 1) << 6) + (s ^ rot) * 2048; wr1[s] = wrd0 + ((1 ^ (q4 >> 1)) << 6) + (s ^ rot) * 2048; } \
  const int xwrap = (t >> 1) ? -NSLOT * SLOT : 0; \
  bf16x8 sel; { const short one = ((lane & 15) == ((lane >> 4) & 1)) ? (short)0x3f80 : (short)0; sel = (bf16x8){one, one, one, one, one, one, one, one}; } \
  bf16x8 xa[28]; float rs, nmb
#define GET_TAG() PIN(tagraw); const u64 tag = (((u64)(unsigned)__builtin_amdgcn_readfirstlane((unsigned)(tagraw >> 32)) << 32) | (unsigned)__builtin_amdgcn_readfirstlane((unsigned)tagraw)) + 1

  if (c == 0) {
    unsigned cpf0, cpf1;
    { asm volatile("global_load_dword %0, %1, %2" : "=v"(cpf0) : "v"(min((unsigned)tid * 128u, (unsigned)CODE_BYTES - 128u)), "s"(pc) : "memory");
      asm volatile("global_load_dword %0, %1, %2" : "=v"(cpf1) : "v"(min((unsigned)tid * 128u + 32768u, (unsigned)CODE_BYTES - 128u)), "s"(pc) : "memory"); }
    unsigned tch = 0;
    u64 tagraw = __hip_atomic_load((GAS u64*)(ws + WS_EPOCH), RLX_AGENT);
    if (tid < 16) *(LAS unsigned*)(lds + L_CNT + 4 * tid) = 0u;
    const bool isK = j < 7, isV = j >= 7 && j < 14;
    bf16x8 qf[4][2];
    {
      COMMON_SETUP();
      f32x16 a0 = {}, a1 = {}; f32x4 c0 = {}, c1 = {};
      if (isV) p1_compute<true, 0>(xa, a0, a1, c0, c1, sel, xrd, xwrap, wr0, wr1 STA); else p1_compute<false, 0>(xa, a0, a1, c0, c1, sel, xrd, xwrap, wr0, wr1 STA);
      STAMP(1);
      cs_put(lds, L_CS0, 0, t, lane, c0); cs_put(lds, L_CS0, 1, t, lane, c1);
      asm volatile("s_waitcnt vmcnt(0) lgkmcnt(0)\n\ts_barrier" : "+v"(cpf0), "+v"(cpf1), "+v"(tch) :: "memory");
      const float cl0 = cs_get(lds, L_CS0, 0, r32), cl1 = cs_get(lds, L_CS0, 1, r32);
      if (isV) {
        row_norm(lds, t, r32, rs, nmb);
        float rr[16], mm[16]; lane_to_regs(priv, r32, hh, rs, rr); lane_to_regs(priv + 128, r32, hh, nmb, mm);
        const int rho = r32 >> 2, col0 = (j - 7) * 64 + 4 * ((rho & 4) + 2 * (rho & 1) + ((rho >> 1) & 1)) + (r32 & 3);
#pragma unroll
        for (int cc = 0; cc < 2; ++cc) {
          const f32x16& a = cc ? a1 : a0; const float cl = cc ? cl1 : cl0; u32x4 o[2];
#pragma unroll
          for (int p = 0; p < 8; ++p) o[p >> 2][p & 3] = cvtpk(rr[2 * p] * __builtin_fmaf(mm[2 * p], cl, a[2 * p]), rr[2 * p + 1] * __builtin_fmaf(mm[2 * p + 1], cl, a[2 * p + 1]));
          const int col = col0 + cc * 32, hd = col / 28, d = col - hd * 28;
          const unsigned ad = WS_KV + hd * 262144 + rt * 4096 + 2048 + (hh * 32 + d) * 16;
          ST16(o[0], ad); ST16(o[1], ad + 1024);
          if (d >= 24) { const unsigned e = d == 24 ? 0x3f803f80u : 0u; const u32x4 pad = {e, e, e, e}; ST16(pad, ad + 64); ST16(pad, ad + 1024 + 64); }
        }
      } else {
        float cr0[16], cr1[16]; lane_to_regs(priv, r32, hh, cl0, cr0); lane_to_regs(priv + 128, r32, hh, cl1, cr1);
        row_norm(lds, t, r32, rs, nmb);
        u32x4 o[4];
        if (isK) {
          epi_T<false>(a0, cr0, rs, nmb, *(u32x4(*)[2])&o[0]); epi_T<false>(a1, cr1, rs, nmb, *(u32x4(*)[2])&o[2]);
#pragma unroll
          for (int s = 0; s < 4; ++s) {
            const int P = j * 8 + 2 * s + hh, hp = P / 7, rr = P - hp * 7;
            const int hd = 2 * hp + (rr > 3), sg = rr > 3 ? rr - 4 : rr;
            const unsigned ad = WS_KV + hd * 262144 + rt * 4096 + sg * 512 + r32 * 16;
            u32x4 v = o[s]; if (rr == 3) { v[2] = 0x3f803f80u; v[3] = 0x00003f80u; }
            ST16(v, ad);
            if (rr == 3) { const u32x4 e = {o[s][2], o[s][3], 0x3f803f80u, 0x00003f80u}; ST16(e, ad + 262144); }
          }
        } else {
          epi_T<true>(a0, cr0, rs, nmb, *(u32x4(*)[2])&o[0]); epi_T<true>(a1, cr1, rs, nmb, *(u32x4(*)[2])&o[2]);
          const unsigned ad = WS_H + rt * 114688 + (j - 14) * 4096 + lane * 16;
#pragma unroll
          for (int s = 0; s < 4; ++s) ST16(o[s], ad + s * 1024);
        }
      }
      STAMP(2);
      const int rho = 4 * g16 + p4, sg = 2 * (rho >> 2) + (rho & 1), half = (rho >> 1) & 1;
      const int g = (j & 1) ? (sg < 3 ? 2 * sg + 1 + half : (half ? 7 : 0)) : 2 * sg + half;
      lptr qb[4];
#pragma unroll
      for (int s = 0; s < 4; ++s) qb[s] = lds + L_WQ + (8 * hh + q4) * 64 + g * 8 + (s ^ rot) * 1024;
      a0 = (f32x16){}; c0 = (f32x4){};
      bf16x8 wf[3];
      wf[0] = trfrag(qb[0], 256); wf[1] = trfrag(qb[1], 256);
#pragma unroll
      for (int st = 0; st < 28; ++st) {
        if (st + 2 < 28) wf[(st + 2) % 3] = trfrag(qb[(st + 2) & 3] + ((st + 2) >> 2) * 4096, 256);
        SBAR();
        a0 = MFMA32(wf[st % 3], xa[st], a0);
        c0 = MFMA16(sel, wf[st % 3], c0);
        PIN(a0); PIN(c0); SBAR();
      }
      if (lane < 16) { *(LAS float*)(priv + lane * 4) = c0[0]; *(LAS float*)(priv + 64 + lane * 4) = c0[1]; }
      float cr[16];
#pragma unroll
      for (int i = 0; i < 4; ++i) { const f32x4 q = ldsf4(priv + (8 * i + 4 * hh) * 4); cr[4 * i] = q[0]; cr[4 * i + 1] = q[1]; cr[4 * i + 2] = q[2]; cr[4 * i + 3] = q[3]; }
      u32x4 o[2]; epi_T<false>(a0, cr, rs * QSCALE, nmb, o);
      if (hh) { o[1][2] = 0u; o[1][3] = 0u; }
      *(LAS u32x4*)(lds + L_QX + t * 2048 + lane * 16) = o[0]; *(LAS u32x4*)(lds + L_QX + t * 2048 + 1024 + lane * 16) = o[1];
    }
    cnt_add(lds, C_Q);
    STAMP(3);
    GET_TAG();
    WAIT_VM(0);
    if (cnt_add(lds, C_KV) == 3u && lane == 0) __hip_atomic_store((GAS u64*)(ws + WS_FKV + fme), tag, RLX_AGENT);
    if (cnt_add(lds, C_H) == 7u && lane == 0) __hip_atomic_store((GAS u64*)(ws + WS_FH + fme), tag, RLX_AGENT);
    STAMP(4);
    { const int c0 = (28 * j) >> 6, c1 = (28 * j + 27) >> 6, sel4 = lane >> 4;
      int jj = sel4 == 0 ? c0 : sel4 == 1 ? c1 : sel4 == 2 ? 7 + c0 : 7 + c1;
      if (j == 0 && (sel4 & 1)) jj = 14 + (sel4 >> 1);
      poll_flags(ws + WS_FKV + ((lane & 15) * 16 + jj) * 128, tag); }
    STAMP(5);
    const unsigned kvb = WS_KV + j * 262144 + t * 65536 + lane * 16;
    u32x4 kv[4][4];
#pragma unroll
    for (int u = 0; u < 3; ++u)
#pragma unroll
      for (int e = 0; e < 4; ++e) kv[u][e] = LD16(kvb + u * 4096 + e * 1024);
    cnt_wait(lds, C_Q, 4u);
#pragma unroll
    for (int q = 0; q < 4; ++q) { const lptr a = lds + L_QX + ((q + t) & 3) * 2048 + lane * 16; qf[q][0] = ldsb128(a); qf[q][1] = ldsb128(a + 1024); }
    cnt_add(lds, C_QR);
    const f32x16 zero = {};
    f32x16 o[4]; float mref[4]; bool redo = false;
#pragma unroll
    for (int q = 0; q < 4; ++q) {
      f32x16 s = MFMA32(asfrag(kv[0][0]), qf[q][0], zero); s = MFMA32(asfrag(kv[0][1]), qf[q][1], s);
      mref[q] = __builtin_ceilf(tile_max(s)) + REF_MARGIN; redo |= !(__builtin_fabsf(mref[q]) < 16777216.f);
      set_ref(qf[q][1], mref[q], hh); o[q] = zero;
    }
    STAMP(6);
    __builtin_amdgcn_s_setprio(2);
    f32x16 sA, sB; u32x4 pA0 = {}, pA1 = {}, pB0 = {}, pB1 = {};
    sA = MFMA32(asfrag(kv[0][0]), qf[0][0], zero); sA = MFMA32(asfrag(kv[0][1]), qf[0][1], sA);
    kv[3][2] = (u32x4){}; kv[3][3] = (u32x4){}; if (ABL_NOKV) { kv[3][0] = kv[0][0]; kv[3][1] = kv[0][1]; }
#define EX4(S, i) do { S[i] = __builtin_amdgcn_exp2f(S[i]); S[i + 1] = __builtin_amdgcn_exp2f(S[i + 1]); S[i + 2] = __builtin_amdgcn_exp2f(S[i + 2]); S[i + 3] = __builtin_amdgcn_exp2f(S[i + 3]); } while (0)
#define UNIT(SC, SN, PC0, PC1, PP0, PP1, KN0, KN1, QN, QP, VV0, VV1) do { \
      SBAR(); SN = MFMA32(asfrag(KN0), qf[QN][0], zero); PIN(SN); SBAR(); EX4(SC, 0); PIN(SC); SBAR(); \
      SN = MFMA32(asfrag(KN1), qf[QN][1], SN); PIN(SN); SBAR(); EX4(SC, 4); PIN(SC); SBAR(); \
      o[QP] = MFMA32(asfrag(VV0), asfrag(PP0), o[QP]); PIN(o[QP]); SBAR(); EX4(SC, 8); PIN(SC); SBAR(); \
      o[QP] = MFMA32(asfrag(VV1), asfrag(PP1), o[QP]); PIN(o[QP]); SBAR(); EX4(SC, 12); SBAR(); \
      PC0 = (u32x4){cvtpk(SC[0], SC[1]), cvtpk(SC[2], SC[3]), cvtpk(SC[4], SC[5]), cvtpk(SC[6], SC[7])}; \
      PC1 = (u32x4){cvtpk(SC[8], SC[9]), cvtpk(SC[10], SC[11]), cvtpk(SC[12], SC[13]), cvtpk(SC[14], SC[15])}; PIN(PC0); PIN(PC1); SBAR(); } while (0)
#define KEYTILE(U, UN, UL) do { \
      UNIT(sA, sB, pA0, pA1, pB0, pB1, kv[U][0], kv[U][1], 1, 3, kv[UL][2], kv[UL][3]); \
      if (!ABL_NOKV) { const int nt = min(kt + (U) + 3, 15) * 4096; _Pragma("unroll") for (int e = 0; e < 4; ++e) kv[UL][e] = __builtin_amdgcn_raw_buffer_load_b128(rsw, kvb + e * 1024, nt, LDAUX); } \
      UNIT(sB, sA, pB0, pB1, pA0, pA1, kv[U][0], kv[U][1], 2, 0, kv[U][2], kv[U][3]); \
      UNIT(sA, sB, pA0, pA1, pB0, pB1, kv[U][0], kv[U][1], 3, 1, kv[U][2], kv[U][3]); \
      UNIT(sB, sA, pB0, pB1, pA0, pA1, kv[UN][0], kv[UN][1], 0, 2, kv[U][2], kv[U][3]); \
    } while (0)
    for (int kt = 0; kt < 16; kt += 4) {
      KEYTILE(0, 1, 3); KEYTILE(1, 2, 0); KEYTILE(2, 3, 1); KEYTILE(3, 0, 2);
      const float lm = __builtin_fmaxf(__builtin_fmaxf(o[0][12], o[1][12]), __builtin_fmaxf(o[2][12], o[3][12]));
      if (__builtin_expect((__ballot(lm > 16777216.f) >> 32) != 0, 0)) {
        NRESC;
        o[3] = MFMA32(asfrag(kv[3][2]), asfrag(pB0), o[3]); o[3] = MFMA32(asfrag(kv[3][3]), asfrag(pB1), o[3]); pB0 = (u32x4){}; pB1 = (u32x4){};
#pragma unroll
        for (int q = 0; q < 4; ++q) { const float l = upper_bcast(o[q][12]); redo |= !(l < 1.2980742146337069e33f);
          const int e = max(__builtin_amdgcn_frexp_expf(l) + (int)REF_MARGIN, 0);
#pragma unroll
          for (int r = 0; r < 16; ++r) o[q][r] = __builtin_ldexpf(o[q][r], -e);
          mref[q] += (float)e; redo |= !(__builtin_fabsf(mref[q]) < 16777216.f); set_ref(qf[q][1], mref[q], hh);
          if (q == 0) {
#pragma unroll
            for (int r = 0; r < 16; ++r) sA[r] -= (float)e; } } }
    }
    cnt_add(lds, C_DONE);
    o[3] = MFMA32(asfrag(kv[3][2]), asfrag(pB0), o[3]); o[3] = MFMA32(asfrag(kv[3][3]), asfrag(pB1), o[3]);
    { float z[4] = {0.f, 0.f, 0.f, 0.f};
#pragma unroll
      for (int r = 0; r < 16; ++r)
#pragma unroll
        for (int q = 0; q < 4; ++q) z[q] = __builtin_fmaf(o[q][r], 0.f, z[q]);
      const float zz = (z[0] + z[1]) + (z[2] + z[3]); redo |= !(zz == 0.f); }
    if (__builtin_expect(__any(redo), 0)) {
#pragma unroll
      for (int q = 0; q < 4; ++q) { set_ref(qf[q][1], 0.f, hh); o[q] = zero; mref[q] = -3.0e38f; }
      for (int kt = 0; kt < 16; ++kt) {
        const u32x4 k0 = LD16(kvb + kt * 4096), k1 = LD16(kvb + kt * 4096 + 1024), v0 = LD16(kvb + kt * 4096 + 2048), v1 = LD16(kvb + kt * 4096 + 3072);
#pragma unroll
        for (int q = 0; q < 4; ++q) {
          f32x16 s = MFMA32(asfrag(k0), qf[q][0], zero); s = MFMA32(asfrag(k1), qf[q][1], s);
          const float mn = __builtin_fmaxf(mref[q], tile_max(s)), al = __builtin_amdgcn_exp2f(mref[q] - mn); mref[q] = mn;
#pragma unroll
          for (int r = 0; r < 16; ++r) { o[q][r] *= al; s[r] = __builtin_amdgcn_exp2f(s[r] - mn); }
          const u32x4 p0 = {cvtpk(s[0], s[1]), cvtpk(s[2], s[3]), cvtpk(s[4], s[5]), cvtpk(s[6], s[7])}, p1 = {cvtpk(s[8], s[9]), cvtpk(s[10], s[11]), cvtpk(s[12], s[13]), cvtpk(s[14], s[15])};
          o[q] = MFMA32(asfrag(v0), asfrag(p0), o[q]); o[q] = MFMA32(asfrag(v1), asfrag(p1), o[q]);
        }
      }
    }
    STAMP(7);
    VSTAMP(28, nresc + (__any(redo) ? 1000 : 0));
    cnt_wait(lds, C_QR, 4u); cnt_wait(lds, C_H, 8u);
    auto area = [&](int dst, int e) -> lptr { return lds + (dst * 3 + e) * 4096 + lane * 16; };
#pragma unroll
    for (int q = 1; q < 4; ++q) {
      const lptr a = area((q + t) & 3, q - 1);
      o[q][13] = hh ? mref[q] : o[q][13];
#pragma unroll
      for (int i = 0; i < 4; ++i) *(LAS f32x4*)(a + i * 1024) = (f32x4){o[q][4 * i], o[q][4 * i + 1], o[q][4 * i + 2], o[q][4 * i + 3]};
    }
    f32x16 of = o[0]; const float mown = mref[0];
    cnt_add(lds, C_CMB); STAMP(9); cnt_wait(lds, C_CMB, 4u);
    { f32x4 pp[3][4]; float ms[3];
#pragma unroll
      for (int e = 0; e < 3; ++e) { const lptr a = area(t, e);
#pragma unroll
        for (int i = 0; i < 4; ++i) pp[e][i] = ldsf4(a + i * 1024);
        ms[e] = upper_bcast(pp[e][3][1]); }
      const float mx = __builtin_fmaxf(__builtin_fmaxf(mown, ms[0]), __builtin_fmaxf(ms[1], ms[2]));
      const float f0 = __builtin_amdgcn_exp2f(mown - mx - 2.f), f1 = __builtin_amdgcn_exp2f(ms[0] - mx - 2.f), f2 = __builtin_amdgcn_exp2f(ms[1] - mx - 2.f), f3 = __builtin_amdgcn_exp2f(ms[2] - mx - 2.f);
#pragma unroll
      for (int r = 0; r < 16; ++r) of[r] = of[r] * f0 + pp[0][r >> 2][r & 3] * f1 + pp[1][r >> 2][r & 3] * f2 + pp[2][r >> 2][r & 3] * f3;
      const float inv = __builtin_amdgcn_rcpf(upper_bcast(of[12]));
      u32x4 e0, e1;
#pragma unroll
      for (int p = 0; p < 4; ++p) { e0[p] = cvtpk(of[2 * p] * inv, of[2 * p + 1] * inv); e1[p] = cvtpk(of[8 + 2 * p] * inv, of[9 + 2 * p] * inv); }
      if (hh) { e1[2] = 0u; e1[3] = 0u; }
      const unsigned a = WS_O + rt * 32768 + j * 2048 + lane * 16;
      ST16(e0, a); ST16(e1, a + 1024); }
    STAMP(10);
    WAIT_VM(0);
    if (cnt_add(lds, C_O) == 3u && lane == 0) __hip_atomic_store((GAS u64*)(ws + WS_FO + fme), tag, RLX_AGENT);
    STAMP(8); CSTAMP(31); STAMP_OUT();
  } else {
    Ld k;
    { const int row = 16 * t + (lane >> 3); const unsigned piece = ((lane & 7) ^ (((row >> 1) & 1) << 2)) << 4;
      k.xsrc = x + rb * 128 * 896; k.w0rs = j < 14 ? 2688u : 3584u; k.w0src = j < 14 ? wqkv + 896 + j * 128 : w1 + (j - 14) * 128;
      k.fasrc = w1 + (2 + j) * 128; k.fbsrc = w1 + (12 + j) * 128; k.dst = lds0 + t * 2048;
      k.w0vo = row * k.w0rs + piece; k.wfvo = row * 3584 + piece;
      k.wqsrc = wqkv + j * 56; k.wqvo = (32 * t + (lane >> 2)) * 2688 + (lane & 3) * 16; k.wq3 = t < 2 ? 384 * 2688 : 0;
      k.xvo0 = row * 896 + (((lane & 7) ^ (lane >> 4)) << 4); k.xvo1 = k.xvo0 ^ 64u; }
    u64 tagraw;
    {
      ld_items<0, PRO>(k);
      tagraw = __hip_atomic_load((GAS u64*)(ws + WS_EPOCH), RLX_AGENT);
      COMMON_SETUP();
      f32x4 s1 = {}, s2 = {};
      p1_load<0>(k, xa, s1, s2, xrd, xwrap STA);
      STAMP(1);
      { const float q = (lane & 2) ? ((lane & 1) ? s2[3] : s2[2]) : ((lane & 1) ? s2[1] : s2[0]);
        const float a = swap_add(s1[0]), bb = swap_add(q); if (hh == 0) *(LAS f32x2*)(lds + L_STAT + (t * 32 + r32) * 8) = (f32x2){a, bb}; }
      asm volatile("s_waitcnt vmcnt(%0) lgkmcnt(0)\n\ts_barrier" :: "i"(2 * (n_issued(6) - 28)) : "memory");
      row_norm(lds, t, r32, rs, nmb);
#pragma unroll
      for (int s = 0; s < 4; ++s) { wr0[s] += L_FA; wr1[s] += L_FA; PIN(wr0[s]); PIN(wr1[s]); }
      bf16x8 f0[2], f1[2]; f32x16 a0 = {}, a1 = {}; f32x4 c0 = {}, c1 = {}; float cr[16]; u32x4 o[2];
      f0[0] = trfrag(wr0[0], 512); f1[0] = trfrag(wr1[0], 512);
      f_steps<0, 0>(k, lds, xa, f0, f1, a0, a1, c0, c1, sel, wr0, wr1);
      cs_put(lds, L_CSA, 0, t, lane, c0); cs_put(lds, L_CSA, 1, t, lane, c1);
      WAIT_VM(0); cnt_add(lds, C_CSA); cnt_wait(lds, C_CSA, 4u);
      STAMP(2);
      { const unsigned ad = WS_H + rt * 114688 + (2 + j) * 4096 + lane * 16;
        lane_to_regs(priv, r32, hh, cs_get(lds, L_CSA, 0, r32), cr); epi_T<true>(a0, cr, rs, nmb, o); ST16(o[0], ad); ST16(o[1], ad + 1024);
        lane_to_regs(priv + 128, r32, hh, cs_get(lds, L_CSA, 1, r32), cr); epi_T<true>(a1, cr, rs, nmb, o); ST16(o[0], ad + 2048); ST16(o[1], ad + 3072); }
      STAMP(3);
      if (j >= 6) {
#pragma unroll
        for (int s = 0; s < 4; ++s) { wr0[s] += L_FB - L_FA; wr1[s] += L_FB - L_FA; PIN(wr0[s]); PIN(wr1[s]); }
        a0 = (f32x16){}; a1 = (f32x16){}; c0 = (f32x4){}; c1 = (f32x4){};
        f0[0] = trfrag(wr0[0], 512); f1[0] = trfrag(wr1[0], 512);
        f_steps<1, 0>(k, lds, xa, f0, f1, a0, a1, c0, c1, sel, wr0, wr1);
        cs_put(lds, L_CSB, 0, t, lane, c0); cs_put(lds, L_CSB, 1, t, lane, c1);
        cnt_add(lds, C_CSB); cnt_wait(lds, C_CSB, 4u);
        const unsigned ad = WS_H + rt * 114688 + (12 + j) * 4096 + lane * 16;
        lane_to_regs(priv, r32, hh, cs_get(lds, L_CSB, 0, r32), cr); epi_T<true>(a0, cr, rs, nmb, o); ST16(o[0], ad); ST16(o[1], ad + 1024);
        lane_to_regs(priv + 128, r32, hh, cs_get(lds, L_CSB, 1, r32), cr); epi_T<true>(a1, cr, rs, nmb, o); ST16(o[0], ad + 2048); ST16(o[1], ad + 3072);
      }
    }
    GET_TAG();
    WAIT_VM(0);
    if (cnt_add(lds, C_H) == 7u && lane == 0) __hip_atomic_store((GAS u64*)(ws + WS_FH + fme), tag, RLX_AGENT);
    STAMP(4);
    const int cg = bi >> 2;
    if (cg < 7) {
      const int rt0 = 8 * xg + 2 * (bi & 3), rb3 = rt0 >> 2;
      const int ring = L_RING + 3 * t * SLOT;
      const int ct = t >> 1, rtl = t & 1;
      G2 g; g.lds = lds; g.t = t; g.ringdst = lds0 + ring; g.lv = lane * 16;
      g.w2vo = (lane >> 3) * 896 + (((lane & 7) ^ (((lane >> 4) & 1) << 2)) << 4);
      g.w2s = w2 + t * 448 * 896 + cg * 128; g.wos = wo + t * 112 * 896 + cg * 128;
      g.hb0 = ws + WS_H + (rt0 + rtl) * 114688 + t * 28672; g.hb1 = ws + WS_H + (rt0 + (rtl ^ 1)) * 114688 + t * 28672;
      g.br0 = wrd0 + ring + ((ct ^ (q4 >> 1)) << 6); g.br1 = wrd0 + ring + ((ct ^ 1 ^ (q4 >> 1)) << 6);
      g2_ws<0, 12>(g);
      const int fb3 = rb3 * 16 + (lane & 15);
      poll_flags(ws + WS_FH + fb3 * 128, tag);
      WAIT_VM(0);
      STAMP(5);
      u32x4 hf[14][2];
#pragma unroll
      for (int s = 0; s < 14; ++s) { ldg_sc1(hf[s][0], g.hb0 + s * 1024, g.lv); ldg_sc1(hf[s][1], g.hb1 + s * 1024, g.lv); }
      f32x16 a00 = {}, a01 = {}, a10 = {}, a11 = {};
      PIN(g.br0); PIN(g.br1);
      { bf16x8 f0[2], f1[2]; f0[0] = trfrag(g.br0, 512); f1[0] = trfrag(g.br1, 512);
        g2_steps<0>(g, hf, f0, f1, a00, a01, a10, a11); }
      STAMP(6);
      WAIT_VM(0);
      bf16x8 wf[8][2];
      { const lptr o0 = lds + ring + SLOT + (4 * hh + q4) * 128 + pi_std * 8;
#pragma unroll
        for (int e = 0; e < 8; ++e)
#pragma unroll
          for (int cc = 0; cc < 2; ++cc) wf[e][cc] = trfrag(o0 + ((cc ^ ct ^ (q4 >> 1)) << 6) + ((e >> 1) * 28 + (e & 1) * 16) * 128, 1024); }
      const size_t oo = (size_t)((rt0 + rtl) * 32 + r32) * 896 + cg * 128 + ct * 64 + hh * 16;
      const u32x4 xr0 = *(const GAS u32x4*)(x + oo), xr1 = *(const GAS u32x4*)(x + oo + 32);
      poll_flags(ws + WS_FO + fb3 * 128, tag);
      STAMP(7);
      { const unsigned ob0 = WS_O + (rt0 + rtl) * 32768 + t * 8192 + lane * 16, ob1 = ob0 ^ 32768u;
        u32x4 of[8][2];
#pragma unroll
        for (int e = 0; e < 8; ++e) { of[e][0] = LD16(ob0 + e * 1024); of[e][1] = LD16(ob1 + e * 1024); }
        SBAR();
#pragma unroll
        for (int e = 0; e < 8; ++e) { const bf16x8 h0 = asfrag(of[e][0]), h1 = asfrag(of[e][1]);
          a11 = MFMA32(wf[e][1], h1, a11); a10 = MFMA32(wf[e][1], h0, a10); a01 = MFMA32(wf[e][0], h1, a01); a00 = MFMA32(wf[e][0], h0, a00);
          PIN(a00); PIN(a01); PIN(a10); PIN(a11); } }
      STAMP(9);
      if (b == 0 && tid == 256) __hip_atomic_store((GAS u64*)(ws + WS_EPOCH), tag, RLX_AGENT);
      { const lptr mine = lds + ring + lane * 16;
        auto put = [&](const f32x16& a, int e) {
#pragma unroll
          for (int i = 0; i < 4; ++i) *(LAS f32x4*)(mine + e * 4096 + i * 1024) = (f32x4){a[4 * i], a[4 * i + 1], a[4 * i + 2], a[4 * i + 3]}; };
        put(a11, 3); put(a10, 2); put(a01, 1); }
      f32x16 fin = a00;
      cnt_add(lds, C_RED); STAMP(10); cnt_wait(lds, C_RED, 4u);
#pragma unroll
      for (int e = 1; e < 4; ++e) { const lptr a = lds + L_RING + 3 * (t ^ e) * SLOT + e * 4096 + lane * 16;
#pragma unroll
        for (int i = 0; i < 4; ++i) { const f32x4 v = ldsf4(a + i * 1024); fin[4 * i] += v[0]; fin[4 * i + 1] += v[1]; fin[4 * i + 2] += v[2]; fin[4 * i + 3] += v[3]; } }
      u32x4 r0, r1;
#pragma unroll
      for (int p = 0; p < 4; ++p) { r0[p] = cvtpk(fin[2 * p] + bflo(xr0[p]), fin[2 * p + 1] + bfhi(xr0[p])); r1[p] = cvtpk(fin[8 + 2 * p] + bflo(xr1[p]), fin[9 + 2 * p] + bfhi(xr1[p])); }
      *(GAS u32x4*)(out + oo) = r0; *(GAS u32x4*)(out + oo + 32) = r1;
      STAMP(8);
    }
    STAMP_OUT();
  }
}

__global__ void __launch_bounds__(512, 2) pre_k(char* ws) { __shared__ char sm[163840]; sm[threadIdx.x] = 1; __syncthreads(); if (ws == nullptr) ws[0] = sm[threadIdx.x ^ 1]; }
extern "C" void kernel_launch(void* const* d_in, const int* in_sizes, int n_in, void* d_out, int out_size, void* d_ws, size_t ws_size, hipStream_t stream) {
  fused_block<<<256, 512, 0, stream>>>((const char*)d_in[0], (const char*)d_in[1], (const char*)d_in[2], (const char*)d_in[4], (const char*)d_in[5], (char*)d_out, (char*)d_ws + WS_BASE);
  pre_k<<<256, 512, 0, stream>>>((char*)d_ws);
}
```

```cpp
#include <hip/hip_runtime.h>
#include <stdint.h>

typedef short bf16x8 __attribute__((ext_vector_type(8)));
typedef short s16x4 __attribute__((ext_vector_type(4)));
typedef float f32x16 __attribute__((ext_vector_type(16)));
typedef float f32x4 __attribute__((ext_vector_type(4)));
typedef float f32x2 __attribute__((ext_vector_type(2)));
typedef __bf16 bf16x2v __attribute__((ext_vector_type(2)));
typedef unsigned u32x4 __attribute__((ext_vector_type(4)));
typedef unsigned long long u64;
#define LAS __attribute__((address_space(3)))
#define GAS __attribute__((address_space(1)))
#define DI __device__ __forceinline__
#define MFMA32(a, b, c) __builtin_amdgcn_mfma_f32_32x32x16_bf16(a, b, c, 0, 0, 0)
#define MFMA16(a, b, c) __builtin_amdgcn_mfma_f32_16x16x32_bf16(a, b, c, 0, 0, 0)
#define SBAR() __builtin_amdgcn_sched_barrier(0)
#define PIN(x) asm volatile("" : "+v"(x))
#define RLX_AGENT __ATOMIC_RELAXED, __HIP_MEMORY_SCOPE_AGENT
#define WAIT_BAR(N) asm volatile("s_waitcnt vmcnt(" #N ") lgkmcnt(0)\n\ts_barrier" ::: "memory")
#define WAIT_VM(N) asm volatile("s_waitcnt vmcnt(" #N ")" ::: "memory")
#define WAIT_LGKM0() asm volatile("s_waitcnt lgkmcnt(0)" ::: "memory")

constexpr unsigned WS_EPOCH = 0;
constexpr unsigned WS_FKV = 4096, WS_FH = WS_FKV + 32768, WS_FO = WS_FH + 32768;
constexpr unsigned WS_KV = 1u << 20;
constexpr unsigned WS_H = 5u << 20;
constexpr unsigned WS_O = 13u << 20;
constexpr unsigned WS_BYTES = 15u << 20;
constexpr int SLOT = 8192, NSLOT = 19;
constexpr int L_MISC = NSLOT * SLOT;
constexpr int L_STAT = L_MISC;
constexpr int L_CS0 = L_MISC + 2048, L_CSA = L_CS0 + 1024, L_CSB = L_CSA + 1024;
constexpr int L_PRIV = L_MISC + 5120;
constexpr int L_CNT = L_MISC + 7168;
constexpr int L_TAG = L_MISC + 7424;
constexpr int LDS_BYTES = 163840;
constexpr int L_FA = 2 * SLOT, L_FB = 9 * SLOT, L_WQ = 16 * SLOT, L_QX = SLOT, L_RING = 2 * SLOT;
constexpr int p1_slot(int item) { return (item % NSLOT) * SLOT; }
enum { C_KV, C_H, C_O, C_O2, C_BAD,
       A_RD0, A_CS0 = A_RD0 + 6, A_ST, A_QW, A_DONE, A_CMB, A_CMB2,
       L_LD0, L_FAL = L_LD0 + 7, L_WQL, L_CSAP, L_CSBP, L_RED, N_EV };

#define WS_BASE 0
#define TOUCH 0
#define ROT7 0
#define MAPA 8
#define G2_SPLIT 8
#define CODE_BYTES 128
constexpr float QSCALE = 0.18898223650461362f * 1.4426950408889634f;
constexpr float REF_MARGIN = 64.f;
constexpr float REF_LIMIT = 4096.f;
constexpr unsigned SPIN_CAP = 1u << 16;

DI unsigned cvtpk(float lo, float hi) { f32x2 v = {lo, hi}; return __builtin_bit_cast(unsigned, __builtin_convertvector(v, bf16x2v)); }
DI float sc1(float x) { asm("" : "+v"(x)); return x; }
DI float bf16r(float x) { return __uint_as_float(cvtpk(x, 0.f) << 16); }
DI float bflo(unsigned v) { return __uint_as_float(v << 16); }
DI float bfhi(unsigned v) { return __uint_as_float(v & 0xffff0000u); }
DI float swap_add(float v) { auto r = __builtin_amdgcn_permlane32_swap(__float_as_uint(v), __float_as_uint(v), false, false); return __uint_as_float(r[0]) + __uint_as_float(r[1]); }
DI float swap_max(float v) { auto r = __builtin_amdgcn_permlane32_swap(__float_as_uint(v), __float_as_uint(v), false, false); return __builtin_fmaxf(__uint_as_float(r[0]), __uint_as_float(r[1])); }
DI float upper_bcast(float v) { auto r = __builtin_amdgcn_permlane32_swap(__float_as_uint(v), __float_as_uint(v), false, false); return __uint_as_float(r[1]); }
DI void glds16(const void* sbase, unsigned voff, unsigned ldsdst) {
  unsigned keep;
  asm volatile("s_mov_b32 %0, m0\n\ts_mov_b32 m0, %3\n\ts_nop 0\n\tglobal_load_lds_dwordx4 %1, %2\n\ts_mov_b32 m0, %0"
               : "=&s"(keep) : "v"(voff), "s"(sbase), "s"(ldsdst) : "memory");
}
typedef LAS char* lptr;
typedef short v4i16_t __attribute__((ext_vector_type(4)));
DI s16x4 trd(lptr p) { return __builtin_bit_cast(s16x4, __builtin_amdgcn_ds_read_tr16_b64_v4i16((LAS v4i16_t*)p)); }
DI bf16x8 trfrag(lptr p, int second) { s16x4 lo = trd(p), hi = trd(p + second); return __builtin_shufflevector(lo, hi, 0, 1, 2, 3, 4, 5, 6, 7); }
DI bf16x8 ldsb128(lptr p) { return *(LAS bf16x8*)p; }
DI f32x4 ldsf4(lptr p) { return *(LAS f32x4*)p; }
DI bf16x8 asfrag(u32x4 v) { return __builtin_bit_cast(bf16x8, v); }
struct Ev { unsigned base, one; };
template <int E> DI void post(const Ev& e) {
  asm volatile("s_mov_b64 exec, 1\n\tds_add_u32 %0, %1 offset:%2\n\ts_mov_b64 exec, -1" :: "v"(e.base), "v"(e.one), "i"(4 * E) : "memory"); }
template <int E> DI void pwait(const Ev& e) {
  unsigned a;
  asm volatile("1:\n\tds_read_b32 %0, %1 offset:%2\n\ts_waitcnt lgkmcnt(0)\n\tv_cmp_gt_u32 vcc, 4, %0\n\ts_cbranch_vccnz 1b"
               : "=&v"(a) : "v"(e.base), "i"(4 * E) : "vcc", "memory"); }
template <int E> DI void pwait_long(const Ev& e) {
  unsigned a;
  asm volatile("1:\n\ts_sleep 8\n\tds_read_b32 %0, %1 offset:%2\n\ts_waitcnt lgkmcnt(0)\n\tv_cmp_gt_u32 vcc, 4, %0\n\ts_cbranch_vccnz 1b"
               : "=&v"(a) : "v"(e.base), "i"(4 * E) : "vcc", "memory"); }
DI unsigned cnt_add(lptr lds, int c) { unsigned r = 0; if ((threadIdx.x & 63) == 0) r = __hip_atomic_fetch_add((LAS unsigned*)(lds + L_CNT + 4 * c), 1u, __ATOMIC_RELAXED, __HIP_MEMORY_SCOPE_WORKGROUP);
  return __builtin_amdgcn_readfirstlane(r); }
template <int SLEEP> DI void poll_flags(const char* flag, u64 tag) {
#pragma nounroll
  for (unsigned s = 0; s < SPIN_CAP; ++s) {
    const u64 v = __hip_atomic_load((GAS u64*)flag, RLX_AGENT);
    if (__all(v == tag)) break;
    __builtin_amdgcn_s_sleep(SLEEP);
  }
  __builtin_amdgcn_fence(__ATOMIC_ACQUIRE, "wavefront");
  asm volatile("" ::: "memory");
}
#define STAMP(i) do {} while (0)
#define CSTAMP(i) do {} while (0)
#define VSTAMP(i, v) do {} while (0)
#define STAMP_OUT() do {} while (0)
#define NRESC
#define STV
#define STA
#define FSTAMP(i) do {} while (0)
#define LSTAMP(i) do {} while (0)
#define BAR() asm volatile("s_waitcnt lgkmcnt(0)\n\ts_barrier" ::: "memory")

#define QPRIO 3
#define LPRIO 2
#define GPRIO 1
#define G2PRIO 0
#define NOCHECK 0
#define WQ_POST 8
#define QSPLIT 20
#define LOOK 4
DI float pick(u64 m, float hi, float lo) { float r; asm("v_cndmask_b32 %0, %1, %2, %3" : "=v"(r) : "v"(lo), "v"(hi), "s"(m)); return r; }
DI float gram_diag(const f32x16& g) {
  const u64 m1 = 0xaaaaaaaaaaaaaaaaull, m2 = 0xccccccccccccccccull, m4 = 0xf0f0f0f0f0f0f0f0ull, m8 = 0xff00ff00ff00ff00ull, m16 = 0xffff0000ffff0000ull;
  const float b0 = pick(m2, pick(m1, g[3], g[2]), pick(m1, g[1], g[0])), b1 = pick(m2, pick(m1, g[7], g[6]), pick(m1, g[5], g[4]));
  const float b2 = pick(m2, pick(m1, g[11], g[10]), pick(m1, g[9], g[8])), b3 = pick(m2, pick(m1, g[15], g[14]), pick(m1, g[13], g[12]));
  float d = pick(m16, pick(m8, b3, b2), pick(m8, b1, b0));
  asm volatile("s_nop 1" : "+v"(d));
  auto r = __builtin_amdgcn_permlane32_swap(__float_as_uint(d), __float_as_uint(d), false, false);
  return pick(m4, __uint_as_float(r[1]), __uint_as_float(r[0]));
}
template <int C> DI void x_frags(bf16x8 (&xa)[28], const lptr (&xrd)[4], int xwrap) {
#pragma unroll
  for (int s = 0; s < 4; ++s) xa[4 * C + s] = ldsb128(xrd[s] + p1_slot(3 * C) + (C == 6 ? xwrap : 0));
}
DI void row_norm(float s1, float s2, float& rs, float& nmb) {
  const float mean = sc1(s1 * (1.f / 448.f)), var = __builtin_fmaxf(sc1(s2 * (1.f / 448.f)) - mean * mean, 0.f);
  const float sd = bf16r(__builtin_amdgcn_sqrtf(bf16r(bf16r(var) + 1e-5f))); rs = __builtin_amdgcn_rcpf(sd); nmb = -bf16r(mean);
}
struct P1A { lptr x[4], w0[4], w1[4]; int xwrap; Ev ev; };
struct WF { s16x4 l0, h0, l1, h1; };
DI bf16x8 cat(const s16x4& lo, const s16x4& hi) { return __builtin_shufflevector(lo, hi, 0, 1, 2, 3, 4, 5, 6, 7); }
struct P1R { f32x16 a0, a1; f32x4 c0, c1, s1; f32x16 s2; };
#define MM(VT, acc, xf, wf) do { if constexpr (VT) acc = MFMA32(xf, wf, acc); else acc = MFMA32(wf, xf, acc); PIN(acc); SBAR(); } while (0)
template <bool VT, int ST, int C, int S> DI void p1_group(bf16x8 (&xa)[28], WF (&f)[4], P1R& r, const bf16x8& sel, const P1A& p) {
  constexpr int xo = C < 6 ? p1_slot(3 * C) - (C >> 1) * 6 * SLOT : 6 * SLOT, wo = C < 6 ? p1_slot(3 * C + 2) - (C >> 1) * 6 * SLOT : SLOT;
  if constexpr (ST >= 0) MM(VT, r.a0, xa[ST], cat(f[ST & 3].l0, f[ST & 3].h0));
  xa[4 * C + S] = ldsb128(p.x[S] + xo); f[S].l0 = trd(p.w0[S] + wo); SBAR();
  if constexpr (ST >= 0) MM(VT, r.a1, xa[ST], cat(f[ST & 3].l1, f[ST & 3].h1));
  f[S].h0 = trd(p.w0[S] + wo + 512); f[S].l1 = trd(p.w1[S] + wo); SBAR();
  if constexpr (ST >= 0) { r.s2 = MFMA32(xa[ST], xa[ST], r.s2); PIN(r.s2); SBAR(); }
  f[S].h1 = trd(p.w1[S] + wo + 512); SBAR();
  if constexpr (ST >= 0) { r.s1 = MFMA16(sel, xa[ST], r.s1); PIN(r.s1);
    if constexpr ((ST & 3) == 0) { r.c0 = MFMA16(sel, cat(f[0].l0, f[0].h0), r.c0); r.c1 = MFMA16(sel, cat(f[0].l1, f[0].h1), r.c1); PIN(r.c0); PIN(r.c1); }
    SBAR(); }
}
template <bool VT, int C> DI void p1_compute(bf16x8 (&xa)[28], WF (&f)[4], P1R& r, const bf16x8& sel, P1A& p STV) {
  if constexpr (C == 0) pwait<L_LD0>(p.ev);
  if (C == 0) STAMP(12);
  FSTAMP(15 + C);
  p1_group<VT, 4 * C - 2, C, 0>(xa, f, r, sel, p); p1_group<VT, 4 * C - 1, C, 1>(xa, f, r, sel, p); p1_group<VT, 4 * C, C, 2>(xa, f, r, sel, p);
  if constexpr (C < 6) {
    const unsigned nxt = *(volatile LAS unsigned*)(uintptr_t)(p.ev.base + 4 * (L_LD0 + C + 1));
    p1_group<VT, 4 * C + 1, C, 3>(xa, f, r, sel, p);
    post<A_RD0 + C>(p.ev);
    if constexpr (C == 1 || C == 3) {
#pragma unroll
      for (int s = 0; s < 4; ++s) { p.x[s] += 6 * SLOT; p.w0[s] += 6 * SLOT; p.w1[s] += 6 * SLOT; PIN(p.x[s]); PIN(p.w0[s]); PIN(p.w1[s]); }
    }
    if constexpr (C == 5) {
#pragma unroll
      for (int s = 0; s < 4; ++s) { p.x[s] += p.xwrap; p.w0[s] -= 12 * SLOT; p.w1[s] -= 12 * SLOT; PIN(p.x[s]); PIN(p.w0[s]); PIN(p.w1[s]); }
    }
    SBAR();
    if (__builtin_expect(__builtin_amdgcn_readfirstlane(nxt) < 4u, 0)) pwait<L_LD0 + C + 1>(p.ev);
    p1_compute<VT, C + 1>(xa, f, r, sel, p STA);
  }
}
struct Ld { const char *xsrc, *w0src, *wqsrc, *fasrc, *fbsrc; unsigned xvo0, xvo1, w0vo, wfvo, wqvo, w0rs, dst, xdst, xdst6, qdst; Ev ev; int ck[7]; };
template <int N, int E> DI void ld_piece(const Ld& k) {
  const unsigned dst = k.dst + p1_slot(N) + E * 1024;
  if constexpr (N < 21) {
    constexpr int c = N / 3, q = N % 3;
    if constexpr (q < 2) glds16(k.xsrc + (q * 16 + E * 8) * 896 + k.ck[c] * 128, E ? k.xvo1 : k.xvo0, (c < 6 ? k.xdst + p1_slot(3 * c) : k.xdst6) + (2 * q + E) * 1024);
    else glds16(k.w0src + (k.ck[c] * 64 + E * 8) * k.w0rs, k.w0vo, dst);
  } else if constexpr (N < 28) glds16(k.fasrc + (k.ck[N - 21] * 64 + E * 8) * 3584, k.wfvo, dst);
  else glds16(k.fbsrc + (k.ck[N - 28] * 64 + E * 8) * 3584, k.wfvo, dst);
}
template <int N> DI void p1_stream(const Ld& k, bf16x8 (&xa)[28], const lptr (&xrd)[4], int xwrap STV) {
  if constexpr (N < 35) {
    if constexpr (N == 18 || (N > 19 && (N - 19) % 3 == 0)) { pwait<A_RD0 + (N - 18) / 3>(k.ev); pwait<L_LD0 + (N - 18) / 3 + 1>(k.ev); }
    ld_piece<N, 0>(k); ld_piece<N, 1>(k);
    if constexpr (N == 2) asm volatile("s_barrier" ::: "memory");
    constexpr int M = N - LOOK;
    if constexpr (M >= 2 && M <= 20 && M % 3 == 2) {
      constexpr int C = M / 3;
      asm volatile("s_waitcnt vmcnt(%0)" :: "i"(2 * LOOK) : "memory");
      post<L_LD0 + C>(k.ev);
      if (C == 0) STAMP(12);
      x_frags<C>(xa, xrd, xwrap); SBAR();
      if (C == 6) STAMP(1);
    }
    p1_stream<N + 1>(k, xa, xrd, xwrap STA);
  }
}
DI void cs_put(lptr lds, int area, int c, int t, int lane, const f32x4& cs) {
  if (lane < 16) { *(LAS float*)(lds + area + c * 512 + lane * 16 + t * 4) = cs[0]; *(LAS float*)(lds + area + c * 512 + (lane + 16) * 16 + t * 4) = cs[1]; }
}
DI float cs_get(lptr lds, int area, int c, int r32) { const f32x4 p = ldsf4(lds + area + c * 512 + r32 * 16); return sc1(sc1(p[0] + p[1]) + sc1(p[2] + p[3])); }
DI void lane_to_regs(lptr priv, int r32, int hh, float v, float (&o)[16]) {
  *(LAS float*)(priv + r32 * 4) = v;
#pragma unroll
  for (int i = 0; i < 4; ++i) { const f32x4 q = ldsf4(priv + (8 * i + 4 * hh) * 4); o[4 * i] = q[0]; o[4 * i + 1] = q[1]; o[4 * i + 2] = q[2]; o[4 * i + 3] = q[3]; }
}
typedef short s16x2 __attribute__((ext_vector_type(2)));
DI void epi_T(const f32x16& acc, const float (&cs)[16], float sc, float nmb, short lo, u32x4& o0, u32x4& o1) {
#pragma unroll
  for (int p = 0; p < 8; ++p) {
    const float v0 = sc1(sc1(__builtin_fmaf(cs[2 * p], nmb, acc[2 * p])) * sc), v1 = sc1(sc1(__builtin_fmaf(cs[2 * p + 1], nmb, acc[2 * p + 1])) * sc);
    const unsigned w = __builtin_bit_cast(unsigned, __builtin_elementwise_max(__builtin_bit_cast(s16x2, cvtpk(v0, v1)), (s16x2){lo, lo}));
    if (p < 4) o0[p] = w; else o1[p - 4] = w;
  }
}
#define ST16(v, off) __builtin_amdgcn_raw_buffer_store_b128(v, rsw, off, 0, 16)
#define ABL_NOKV 0
#define LDAUX 16
#define LD16(off) __builtin_amdgcn_raw_buffer_load_b128(rsw, off, 0, LDAUX)

DI float set_ref(bf16x8& q1, float m, int hh) {
  const unsigned w = cvtpk(-__builtin_rintf(m), 0.f);
  u32x4 v = __builtin_bit_cast(u32x4, q1); v[2] = hh ? w : v[2]; q1 = __builtin_bit_cast(bf16x8, v);
  return -__uint_as_float(w << 16);
}
DI float tile_max(const f32x16& s) {
  float a = __builtin_fmaxf(__builtin_fmaxf(s[0], s[1]), s[2]), b = __builtin_fmaxf(__builtin_fmaxf(s[3], s[4]), s[5]);
#pragma unroll
  for (int r = 6; r < 14; r += 4) { a = __builtin_fmaxf(__builtin_fmaxf(a, s[r]), s[r + 1]); b = __builtin_fmaxf(__builtin_fmaxf(b, s[r + 2]), s[r + 3]); }
  a = __builtin_fmaxf(__builtin_fmaxf(a, s[14]), s[15]);
  return swap_max(__builtin_fmaxf(a, b));
}

template <int P, int ST> DI void f_steps(const Ld& k, const bf16x8 (&xa)[28], WF (&f)[3], f32x16& a0, f32x16& a1, f32x4& c0, f32x4& c1, const bf16x8& sel, const lptr (&w0)[4], const lptr (&w1)[4]) {
  if constexpr (ST < 28) {
    constexpr int N = ST + 2, off = (N >> 2) * SLOT;
    SBAR(); MM(false, a0, xa[ST], cat(f[ST % 3].l0, f[ST % 3].h0));
    if constexpr (N < 28) { f[N % 3].l0 = trd(w0[N & 3] + off); f[N % 3].h0 = trd(w0[N & 3] + off + 512); SBAR(); }
    MM(false, a1, xa[ST], cat(f[ST % 3].l1, f[ST % 3].h1));
    if constexpr (N < 28) { f[N % 3].l1 = trd(w1[N & 3] + off); f[N % 3].h1 = trd(w1[N & 3] + off + 512); SBAR(); }
    if constexpr ((ST & 3) == 0) { c0 = MFMA16(sel, cat(f[ST % 3].l0, f[ST % 3].h0), c0); c1 = MFMA16(sel, cat(f[ST % 3].l1, f[ST % 3].h1), c1); PIN(c0); PIN(c1); }
    if constexpr (P == 0 && ST == WQ_POST) { WAIT_VM(0); post<L_WQL>(k.ev); }
    f_steps<P, ST + 1>(k, xa, f, a0, a1, c0, c1, sel, w0, w1);
  }
}
DI void wf_read(WF& f, lptr p0, lptr p1) { f.l0 = trd(p0); f.h0 = trd(p0 + 512); f.l1 = trd(p1); f.h1 = trd(p1 + 512); }

constexpr int g2_issued(int st) { int n = 28; for (int s = 0; s < st; ++s) { if (s < 14) n += 2; if (s < 24) n += 2; } return n; }
constexpr int g2_pos_h(int s) { return s < 14 ? 2 * s + 1 : g2_issued(s - 14) + 1; }
constexpr int g2_pos_w(int s) { return s < 12 ? -1 : g2_issued(s - 12) + (s - 12 < 14 ? 2 : 0) + 1; }
constexpr int g2_wait(int st) { const int a = g2_pos_h(st), b = st + 2 < 28 ? g2_pos_w(st + 2) : -1; const int n = g2_issued(st) - 1 - (a > b ? a : b); return n > 63 ? 63 : n; }
DI void ldg_sc1(u32x4& d, const char* sbase, unsigned voff) { asm volatile("global_load_dwordx4 %0, %1, %2 sc1" : "=v"(d) : "v"(voff), "s"(sbase) : "memory"); }
struct G2 { const char *hb0, *hb1, *w2s, *wos; unsigned lv, w2vo, ringdst; Ev ev; int t; lptr br0, br1; };
template <int ST, int E> DI void g2_w(const G2& g) {
  const unsigned dst = g.ringdst + (ST % 12) * 2048 + E * 1024;
  if constexpr (ST < 28) glds16(g.w2s + (ST * 16 + E * 8) * 896, g.w2vo, dst);
  else if constexpr (ST < 35) glds16(g.wos + ((ST - 28) * 16 + E * 8) * 896, g.w2vo, dst);
  else glds16(g.wos + (g.t == 3 ? 104 : 112 + E * 8) * 896, g.w2vo, dst);
}
template <int A, int B> DI void g2_ws(const G2& g) { if constexpr (A < B) { g2_w<A, 0>(g); g2_w<A, 1>(g); g2_ws<A + 1, B>(g); } }
template <int ST> DI void g2_steps(const G2& g, u32x4 (&hf)[14][2], WF (&f)[3], f32x16& a00, f32x16& a01, f32x16& a10, f32x16& a11) {
  if constexpr (ST < 28) {
    if constexpr (ST == G2_SPLIT) { __builtin_amdgcn_s_setprio(0); pwait_long<A_DONE>(g.ev); __builtin_amdgcn_s_setprio(GPRIO); }
    asm volatile("s_waitcnt vmcnt(%2)" : "+v"(hf[ST % 14][0]), "+v"(hf[ST % 14][1]) : "i"(g2_wait(ST)) : "memory");
    constexpr int N = ST + 2, off = (N % 12) * 2048;
    const bf16x8 h0 = asfrag(hf[ST % 14][0]), h1 = asfrag(hf[ST % 14][1]), w0 = cat(f[ST % 3].l0, f[ST % 3].h0), w1 = cat(f[ST % 3].l1, f[ST % 3].h1);
    SBAR(); MM(false, a00, h0, w0);
    if constexpr (N < 28) { f[N % 3].l0 = trd(g.br0 + off); f[N % 3].h0 = trd(g.br0 + off + 512); SBAR(); }
    MM(false, a01, h1, w0);
    if constexpr (N < 28) { f[N % 3].l1 = trd(g.br1 + off); f[N % 3].h1 = trd(g.br1 + off + 512); SBAR(); }
    MM(false, a10, h0, w1);
    MM(false, a11, h1, w1);
    if constexpr (ST < 14) { ldg_sc1(hf[ST][0], g.hb0 + (ST + 14) * 1024, g.lv); ldg_sc1(hf[ST][1], g.hb1 + (ST + 14) * 1024, g.lv); }
    if constexpr (ST < 24) { g2_w<ST + 12, 0>(g); g2_w<ST + 12, 1>(g); }
    g2_steps<ST + 1>(g, hf, f, a00, a01, a10, a11);
  }
}

__global__ void __launch_bounds__(512, 2) fused_block(const char* x, const char* wqkv, const char* wo, const char* w1, const char* w2, char* out, char* ws) {
  asm volatile("" : "+s"(x), "+s"(wqkv), "+s"(wo), "+s"(w1), "+s"(w2), "+s"(out), "+s"(ws));
  __shared__ __attribute__((aligned(16))) char smem[LDS_BYTES];
  u64 pc; asm volatile("s_getpc_b64 %0" : "=s"(pc));
  const lptr lds = (lptr)smem; const unsigned lds0 = (unsigned)(uintptr_t)lds;
  const int tid = threadIdx.x, lane = tid & 63, r32 = lane & 31, hh = lane >> 5;
  const int w = __builtin_amdgcn_readfirstlane(tid >> 6), t = w & 3, c = w >> 2;
  const int b = blockIdx.x, xg = b & 7, bi = b >> 3;
  const int j = (xg & 3) * 4 + (bi >> 3), rb = (xg >> 2) * 8 + (bi & 7);
  const int fme = (rb * 16 + j) * 128, rt = rb * 4 + t;
  STAMP(0); CSTAMP(30); VSTAMP(29, (unsigned)__builtin_amdgcn_s_getreg(20 | (3 << 11)) & 0xfu);
  const int rot = t, r7 = ROT7 ? (rb + 2 * j) % 7 : 0;
  const int q4 = (lane & 15) >> 2, p4 = lane & 3, g16 = (lane >> 4) & 1;
  const int pi_std = 4 * g16 + 2 * (p4 & 1) + (p4 >> 1);
  const lptr wrd0 = lds + (8 * hh + q4) * 128 + pi_std * 8;
  const lptr priv = lds + L_PRIV + w * 256;
  Ev ev = {lds0 + L_CNT, 1u}; PIN(ev.base); PIN(ev.one);
  const __amdgpu_buffer_rsrc_t rsw = __builtin_amdgcn_make_buffer_rsrc(ws, 0, WS_BYTES, 0x00020000);
#define COMMON_SETUP() \
  lptr xrd[4], wr0[4], wr1[4]; \
  _Pragma("unroll") for (int s = 0; s < 4; ++s) { xrd[s] = lds + (t * 32 + r32) * 128 + (((2 * (s ^ rot) + hh) ^ ((r32 >> 1) & 7)) << 4); \
    wr0[s] = wrd0 + ((q4 >> 1) << 6) + (s ^ rot) * 2048; wr1[s] = wrd0 + ((1 ^ (q4 >> 1)) << 6) + (s ^ rot) * 2048; } \
  const int xwrap = (t >> 1) ? -NSLOT * SLOT : 0; \
  bf16x8 sel; { const short one = ((lane & 15) == ((lane >> 4) & 1)) ? (short)0x3f80 : (short)0; sel = (bf16x8){one, one, one, one, one, one, one, one}; } \
  bf16x8 xa[28]; float rs, nmb
#define GET_TAG() PIN(tagraw); const u64 tag = (((u64)(unsigned)__builtin_amdgcn_readfirstlane((unsigned)(tagraw >> 32)) << 32) | (unsigned)__builtin_amdgcn_readfirstlane((unsigned)tagraw)) + 1

  if (c == 0) {
    unsigned cpf0;
    asm volatile("global_load_dword %0, %1, %2" : "=v"(cpf0) : "v"(min((unsigned)(bi * 16 + (tid & 15)) * 128u, (unsigned)CODE_BYTES - 128u)), "s"(pc) : "memory");
    unsigned tch = 0;
    u64 tagraw = __hip_atomic_load((GAS u64*)(ws + WS_EPOCH), RLX_AGENT);
    if (tid < N_EV) *(LAS unsigned*)(lds + L_CNT + 4 * tid) = 0u;
    BAR();
    const bool isK = j < 7, isV = j >= 7 && j < 14;
    bf16x8 qf[4][2]; u64 tag;
    {
      COMMON_SETUP();
      unsigned ad[4]; bool strad[4] = {false, false, false, false};
      if (isV) { const int rho = r32 >> 2, col0 = (j - 7) * 64 + 4 * ((rho & 4) + 2 * (rho & 1) + ((rho >> 1) & 1)) + (r32 & 3);
#pragma unroll
        for (int cc = 0; cc < 2; ++cc) { const int col = col0 + cc * 32, hd = col / 28, d = col - hd * 28;
          ad[cc] = WS_KV + hd * 262144 + rt * 4096 + 2048 + (hh * 32 + d) * 16; ad[2 + cc] = d >= 24 ? ad[cc] + 64 : 0x80000000u; strad[cc] = d == 24; }
      } else if (isK) {
#pragma unroll
        for (int s = 0; s < 4; ++s) { const int P = j * 8 + 2 * s + hh, hp = P / 7, rr = P - hp * 7, hd = 2 * hp + (rr > 3), sg = rr > 3 ? rr - 4 : rr;
          ad[s] = WS_KV + hd * 262144 + rt * 4096 + sg * 512 + r32 * 16; strad[s] = rr == 3; }
      } else {
#pragma unroll
        for (int s = 0; s < 4; ++s) ad[s] = WS_H + rt * 114688 + (j - 14) * 4096 + s * 1024 + lane * 16;
      }
      PIN(ad[0]); PIN(ad[1]); PIN(ad[2]); PIN(ad[3]);
      P1R r = {}; float cr0[16], cr1[16], cl0, cl1;
      { P1A p; p.xwrap = xwrap; p.ev = ev; WF f[4];
#pragma unroll
        for (int s = 0; s < 4; ++s) { p.x[s] = xrd[s]; p.w0[s] = wr0[s]; p.w1[s] = wr1[s]; }
        const lptr csw = lds + L_CS0 + lane * 16 + t * 4;
#define P1_TAIL(VT) do { \
        p1_compute<VT, 0>(xa, f, r, sel, p STA); \
        __builtin_amdgcn_s_setprio(3); \
        p1_group<VT, 25, 6, 3>(xa, f, r, sel, p); \
        r.s2 = MFMA32(xa[26], xa[26], r.s2); PIN(r.s2); SBAR(); \
        if (lane < 16) { *(LAS float*)csw = r.c0[0]; *(LAS float*)(csw + 256) = r.c0[1]; *(LAS float*)(csw + 512) = r.c1[0]; *(LAS float*)(csw + 768) = r.c1[1]; } \
        post<A_CS0>(ev); SBAR(); \
        r.s1 = MFMA16(sel, xa[26], r.s1); r.s1 = MFMA16(sel, xa[27], r.s1); r.s2 = MFMA32(xa[27], xa[27], r.s2); PIN(r.s1); PIN(r.s2); SBAR(); \
        MM(VT, r.a0, xa[26], cat(f[2].l0, f[2].h0)); \
        pwait<A_CS0>(ev); \
        const f32x4 p0_ = ldsf4(lds + L_CS0 + r32 * 16), p1_ = ldsf4(lds + L_CS0 + 512 + r32 * 16); \
        if (lane < 16) { *(LAS float*)(priv + lane * 4) = r.s1[0]; *(LAS float*)(priv + 64 + lane * 4) = r.s1[1]; } \
        const float sx_ = *(LAS float*)(priv + r32 * 4); SBAR(); \
        MM(VT, r.a1, xa[26], cat(f[2].l1, f[2].h1)); \
        const float sq_ = gram_diag(r.s2); \
        cl0 = sc1(sc1(p0_[0] + p0_[1]) + sc1(p0_[2] + p0_[3])); cl1 = sc1(sc1(p1_[0] + p1_[1]) + sc1(p1_[2] + p1_[3])); \
        if constexpr (!VT) { lane_to_regs(priv, r32, hh, cl0, cr0); lane_to_regs(priv + 128, r32, hh, cl1, cr1); } SBAR(); \
        MM(VT, r.a0, xa[27], cat(f[3].l0, f[3].h0)); \
        row_norm(sx_, sq_, rs, nmb); \
        *(LAS float*)(lds + L_STAT + t * 256 + lane * 4) = hh ? nmb : rs; post<A_ST>(ev); SBAR();        \
        MM(VT, r.a1, xa[27], cat(f[3].l1, f[3].h1)); } while (0)
        if (isV) P1_TAIL(true); else P1_TAIL(false); }
      STAMP(1);
      u32x4 o[4];
      if (isV) {
#pragma unroll
        for (int i = 0; i < 4; ++i) { const f32x4 rr = ldsf4(lds + L_STAT + t * 256 + (8 * i + 4 * hh) * 4), mm = ldsf4(lds + L_STAT + t * 256 + 128 + (8 * i + 4 * hh) * 4);
#pragma unroll
          for (int e = 0; e < 2; ++e) {
            const int n = 4 * i + 2 * e;
            o[i >> 1][2 * (i & 1) + e] = cvtpk(sc1(sc1(__builtin_fmaf(mm[2 * e], cl0, r.a0[n])) * rr[2 * e]), sc1(sc1(__builtin_fmaf(mm[2 * e + 1], cl0, r.a0[n + 1])) * rr[2 * e + 1]));
            o[2 + (i >> 1)][2 * (i & 1) + e] = cvtpk(sc1(sc1(__builtin_fmaf(mm[2 * e], cl1, r.a1[n])) * rr[2 * e]), sc1(sc1(__builtin_fmaf(mm[2 * e + 1], cl1, r.a1[n + 1])) * rr[2 * e + 1])); } }
        ST16(o[0], ad[0]); ST16(o[1], ad[0] + 1024); ST16(o[2], ad[1]); ST16(o[3], ad[1] + 1024);
#pragma unroll
        for (int cc = 0; cc < 2; ++cc) { const unsigned e = strad[cc] ? 0x3f803f80u : 0u; const u32x4 pad = {e, e, e, e}; ST16(pad, ad[2 + cc]); ST16(pad, ad[2 + cc] + 1024); }
      } else {
        const short lo = isK ? (short)-32768 : (short)0;
        epi_T(r.a0, cr0, rs, nmb, lo, o[0], o[1]);
#pragma unroll
        for (int s = 0; s < 2; ++s) { u32x4 v = o[s]; if (strad[s]) { v[2] = 0x3f803f80u; v[3] = 0x00003f80u; } ST16(v, ad[s]); }
        epi_T(r.a1, cr1, rs, nmb, lo, o[2], o[3]);
#pragma unroll
        for (int s = 2; s < 4; ++s) { u32x4 v = o[s]; if (strad[s]) { v[2] = 0x3f803f80u; v[3] = 0x00003f80u; } ST16(v, ad[s]); }
#pragma unroll
        for (int s = 0; s < 4; ++s) if (strad[s]) { const u32x4 e = {o[s][2], o[s][3], 0x3f803f80u, 0x00003f80u}; ST16(e, ad[s] + 262144); }
      }
      STAMP(13);
      __builtin_amdgcn_s_setprio(QPRIO);
      f32x16 a0, a1; f32x4 c0;
      const int rho = 4 * g16 + p4, sg = 2 * (rho >> 2) + (rho & 1), half = (rho >> 1) & 1;
      const int g = (j & 1) ? (sg < 3 ? 2 * sg + 1 + half : (half ? 7 : 0)) : 2 * sg + half;
      lptr qb[4];
#pragma unroll
      for (int s = 0; s < 4; ++s) { qb[s] = lds + L_WQ + (8 * hh + q4) * 64 + g * 8 + (s ^ rot) * 1024; PIN(qb[s]); }
      a0 = (f32x16){}; a1 = (f32x16){}; c0 = (f32x4){};
      pwait<L_WQL>(ev);
      s16x4 ql[6], qh[6];
#pragma unroll
      for (int st = 0; st < 4; ++st) { ql[st] = trd(qb[st]); qh[st] = trd(qb[st] + 256); }
#pragma unroll
      for (int st = 0; st < 28; ++st) {
        if (st == QSPLIT) {
      asm volatile("s_waitcnt vmcnt(0)" : "+v"(cpf0), "+v"(tch), "+v"(tagraw) :: "memory");
      *(LAS u64*)(lds + L_TAG) = tagraw;
      tag = (((u64)(unsigned)__builtin_amdgcn_readfirstlane((unsigned)(tagraw >> 32)) << 32) | (unsigned)__builtin_amdgcn_readfirstlane((unsigned)tagraw)) + 1;
      if (cnt_add(lds, C_KV) == 3u && lane == 0) __hip_atomic_store((GAS u64*)(ws + WS_FKV + fme), tag, RLX_AGENT);
      if (cnt_add(lds, C_H) == 7u && lane == 0) __hip_atomic_store((GAS u64*)(ws + WS_FH + fme), tag, RLX_AGENT);
        STAMP(2); }
        SBAR();
        if (st & 1) { a1 = MFMA32(cat(ql[st % 6], qh[st % 6]), xa[st], a1); PIN(a1); } else { a0 = MFMA32(cat(ql[st % 6], qh[st % 6]), xa[st], a0); PIN(a0); }
        SBAR();
        if (st + 4 < 28) { const int o_ = st + 4 < 24 ? ((st + 4) >> 2) * 4096 : -L_WQ; ql[(st + 4) % 6] = trd(qb[st & 3] + o_); qh[(st + 4) % 6] = trd(qb[st & 3] + o_ + 256); }
        SBAR();
        c0 = MFMA16(sel, cat(ql[st % 6], qh[st % 6]), c0); PIN(c0);
      }
#pragma unroll
      for (int i = 0; i < 16; ++i) a0[i] = sc1(a0[i] + a1[i]);
      if (lane < 16) { *(LAS float*)(priv + lane * 4) = c0[0]; *(LAS float*)(priv + 64 + lane * 4) = c0[1]; }
      float cr[16];
#pragma unroll
      for (int i = 0; i < 4; ++i) { const f32x4 q = ldsf4(priv + (8 * i + 4 * hh) * 4); cr[4 * i] = q[0]; cr[4 * i + 1] = q[1]; cr[4 * i + 2] = q[2]; cr[4 * i + 3] = q[3]; }
      epi_T(a0, cr, rs * QSCALE, nmb, (short)-32768, o[0], o[1]);
      if (hh) { o[1][2] = 0u; o[1][3] = 0u; }
      *(LAS u32x4*)(lds + L_QX + t * 2048 + lane * 16) = o[0]; *(LAS u32x4*)(lds + L_QX + t * 2048 + 1024 + lane * 16) = o[1];
    }
    post<A_QW>(ev);
    const f32x16 zero = {};
    f32x16 o[4]; u32x4 kv[4][4], pA0 = {}, pA1 = {}, pB0 = {}, pB1 = {};
#pragma unroll
    for (int q = 0; q < 4; ++q) { o[q] = zero; PIN(o[q]); }
    kv[3][2] = (u32x4){}; kv[3][3] = (u32x4){}; PIN(kv[3][2]); PIN(kv[3][3]); PIN(pB0); PIN(pB1);
    STAMP(4);
    { const int c0 = (28 * j) >> 6, c1 = (28 * j + 27) >> 6, sel4 = lane >> 4;
      int jj = sel4 == 0 ? c0 : sel4 == 1 ? c1 : sel4 == 2 ? 7 + c0 : 7 + c1;
      if (j == 0 && (sel4 & 1)) jj = 14 + (sel4 >> 1);
      poll_flags<1>(ws + WS_FKV + ((lane & 15) * 16 + jj) * 128, tag); }
    STAMP(5);
    const unsigned kvb = WS_KV + j * 262144 + t * 65536 + lane * 16;
#pragma unroll
    for (int u = 0; u < 3; ++u)
#pragma unroll
      for (int e = 0; e < 4; ++e) kv[u][e] = LD16(kvb + u * 4096 + e * 1024);
    pwait<A_QW>(ev);
#pragma unroll
    for (int q = 0; q < 4; ++q) { const lptr a = lds + L_QX + ((q + t + 1) & 3) * 2048 + lane * 16; qf[q][0] = ldsb128(a); qf[q][1] = ldsb128(a + 1024); }
    float mref[4]; bool redo;
#pragma unroll
    for (int q = 0; q < 4; ++q) {
      f32x16 s = MFMA32(asfrag(kv[0][0]), qf[q][0], zero); s = MFMA32(asfrag(kv[0][1]), qf[q][1], s);
      mref[q] = set_ref(qf[q][1], tile_max(s) + REF_MARGIN, hh);
    }
    redo = !(__builtin_fmaxf(__builtin_fmaxf(__builtin_fabsf(mref[0]), __builtin_fabsf(mref[1])), __builtin_fmaxf(__builtin_fabsf(mref[2]), __builtin_fabsf(mref[3]))) < REF_LIMIT);
    __builtin_amdgcn_s_setprio(LPRIO);
    f32x16 sA, sB;
    sA = MFMA32(asfrag(kv[0][0]), qf[0][0], zero); sA = MFMA32(asfrag(kv[0][1]), qf[0][1], sA);
    PIN(sA); STAMP(6);
    auto area = [&](int dst, int e) -> lptr { const int n = dst * 3 + e; return lds + (n < 10 ? 14 * SLOT + n * 4096 : (n - 10) * 4096) + lane * 16; };
    auto put_o = [&](const f32x16& v, float m, int q) { const lptr a = area((q + t + 1) & 3, q);
      *(LAS f32x4*)a = (f32x4){v[0], v[1], v[2], v[3]}; *(LAS f32x4*)(a + 1024) = (f32x4){v[4], v[5], v[6], v[7]};
      *(LAS f32x4*)(a + 2048) = (f32x4){v[8], v[9], v[10], v[11]}; *(LAS f32x4*)(a + 3072) = (f32x4){v[12], hh ? m : v[13], v[14], v[15]}; };
#define PUT_O(q) put_o(o[q], mref[q], q)
#define EX4(S, i) do { S[i] = __builtin_amdgcn_exp2f(S[i]); S[i + 1] = __builtin_amdgcn_exp2f(S[i + 1]); S[i + 2] = __builtin_amdgcn_exp2f(S[i + 2]); S[i + 3] = __builtin_amdgcn_exp2f(S[i + 3]); } while (0)
#define UNIT(SC, SN, PC0, PC1, PP0, PP1, KN0, KN1, QN, QP, VV0, VV1, EXTRA) do { \
      SBAR(); if (QN >= 0) { SN = MFMA32(asfrag(KN0), qf[QN & 3][0], zero); PIN(SN); } SBAR(); EX4(SC, 0); PIN(SC); SBAR(); \
      if (QN >= 0) { SN = MFMA32(asfrag(KN1), qf[QN & 3][1], SN); PIN(SN); } SBAR(); EX4(SC, 4); PIN(SC); SBAR(); \
      o[QP] = MFMA32(asfrag(VV0), asfrag(PP0), o[QP]); PIN(o[QP]); SBAR(); EXTRA; EX4(SC, 8); PIN(SC); SBAR(); \
      o[QP] = MFMA32(asfrag(VV1), asfrag(PP1), o[QP]); PIN(o[QP]); SBAR(); EX4(SC, 12); SBAR(); \
      PC0 = (u32x4){cvtpk(SC[0], SC[1]), cvtpk(SC[2], SC[3]), cvtpk(SC[4], SC[5]), cvtpk(SC[6], SC[7])}; \
      PC1 = (u32x4){cvtpk(SC[8], SC[9]), cvtpk(SC[10], SC[11]), cvtpk(SC[12], SC[13]), cvtpk(SC[14], SC[15])}; PIN(PC0); PIN(PC1); SBAR(); } while (0)
#define LDKV(UL, e) kv[UL][e] = __builtin_amdgcn_raw_buffer_load_b128(rsw, kvb + (e) * 1024, nt, LDAUX)
#define KEYTILE(U, UN, UL, LOAD, QLAST, X2, X3) do { const int nt = (kt + (U) + 3) * 4096; \
      UNIT(sA, sB, pA0, pA1, pB0, pB1, kv[U][0], kv[U][1], 1, 3, kv[UL][2], kv[UL][3], if (LOAD) { LDKV(UL, 0); LDKV(UL, 1); }); \
      UNIT(sB, sA, pB0, pB1, pA0, pA1, kv[U][0], kv[U][1], 2, 0, kv[U][2], kv[U][3], if (LOAD) { LDKV(UL, 2); LDKV(UL, 3); }); \
      UNIT(sA, sB, pA0, pA1, pB0, pB1, kv[U][0], kv[U][1], 3, 1, kv[U][2], kv[U][3], X2); \
      UNIT(sB, sA, pB0, pB1, pA0, pA1, kv[UN][0], kv[UN][1], QLAST, 2, kv[U][2], kv[U][3], X3); \
    } while (0)
    for (int kt = 0; kt < 12; kt += 4) {
      LSTAMP(16 + (kt >> 1)); KEYTILE(0, 1, 3, true, 0, , ); KEYTILE(1, 2, 0, true, 0, , ); LSTAMP(17 + (kt >> 1)); KEYTILE(2, 3, 1, true, 0, , ); KEYTILE(3, 0, 2, true, 0, , );
      const float lm = __builtin_fmaxf(__builtin_fmaxf(o[0][12], o[1][12]), __builtin_fmaxf(o[2][12], o[3][12]));
      if (!NOCHECK && __builtin_expect((unsigned)(__ballot(lm > 1.099511627776e12f) >> 32) != 0u, 0)) {
        NRESC;
        o[3] = MFMA32(asfrag(kv[3][2]), asfrag(pB0), o[3]); o[3] = MFMA32(asfrag(kv[3][3]), asfrag(pB1), o[3]); pB0 = (u32x4){}; pB1 = (u32x4){};
#pragma unroll
        for (int q = 0; q < 4; ++q) { const float l = upper_bcast(o[q][12]); redo |= !(l < 1.2980742146337069e33f);
          const float mn = set_ref(qf[q][1], mref[q] + (float)max(__builtin_amdgcn_frexp_expf(l) + (int)REF_MARGIN, 0), hh);
          const int e = (int)(mn - mref[q]); mref[q] = mn; redo |= !(__builtin_fabsf(mn) < REF_LIMIT);
#pragma unroll
          for (int r = 0; r < 16; ++r) o[q][r] = __builtin_ldexpf(o[q][r], -e);
          if (q == 0) {
#pragma unroll
            for (int r = 0; r < 16; ++r) sA[r] -= (float)e; } } }
    }
    { const int kt = 12;
      if (__builtin_expect(__any(redo), 0)) { mref[0] = mref[1] = mref[2] = mref[3] = __builtin_nanf(""); }
      pwait<L_CSBP>(ev);
      LSTAMP(22); KEYTILE(0, 1, 3, true, 0, , ); KEYTILE(1, 2, 0, false, 0, , ); LSTAMP(23); KEYTILE(2, 3, 1, false, 0, , );
      post<A_DONE>(ev); __builtin_amdgcn_s_setprio(3);
      KEYTILE(3, 0, 2, false, -1, PUT_O(0), PUT_O(1)); }
    o[3] = MFMA32(asfrag(kv[3][2]), asfrag(pB0), o[3]); o[3] = MFMA32(asfrag(kv[3][3]), asfrag(pB1), o[3]); PIN(o[3]); SBAR();
    PUT_O(2);
    STAMP(7);
    VSTAMP(28, nresc + (__any(redo) ? 1000 : 0));
    auto combine = [&]() -> bool {
      f32x4 pp[3][4]; float ms[3]; f32x16 of;
#pragma unroll
      for (int e = 0; e < 3; ++e) { const lptr a = area(t, e); pp[e][3] = ldsf4(a + 3072); }
#pragma unroll
      for (int e = 0; e < 3; ++e) { const lptr a = area(t, e);
#pragma unroll
        for (int i = 0; i < 3; ++i) pp[e][i] = ldsf4(a + i * 1024);
        ms[e] = upper_bcast(pp[e][3][1]); }
      const float mx = __builtin_fmaxf(__builtin_fmaxf(mref[3], ms[0]), __builtin_fmaxf(ms[1], ms[2])) + 2.f;
      float f0 = __builtin_amdgcn_exp2f(mref[3] - mx), f1 = __builtin_amdgcn_exp2f(ms[0] - mx), f2 = __builtin_amdgcn_exp2f(ms[1] - mx), f3 = __builtin_amdgcn_exp2f(ms[2] - mx);
      const float l = upper_bcast(__builtin_fmaf(pp[2][3][0], f3, __builtin_fmaf(pp[1][3][0], f2, __builtin_fmaf(pp[0][3][0], f1, o[3][12] * f0))));
      const float inv = __builtin_amdgcn_rcpf(l);
      f0 = sc1(f0 * inv); f1 = sc1(f1 * inv); f2 = sc1(f2 * inv); f3 = sc1(f3 * inv);
#pragma unroll
      for (int r = 0; r < 16; ++r) of[r] = sc1(__builtin_fmaf(pp[2][r >> 2][r & 3], f3, sc1(__builtin_fmaf(pp[1][r >> 2][r & 3], f2, sc1(__builtin_fmaf(pp[0][r >> 2][r & 3], f1, sc1(o[3][r] * f0)))))));
      u32x4 e0, e1;
#pragma unroll
      for (int p = 0; p < 4; ++p) { e0[p] = cvtpk(of[2 * p], of[2 * p + 1]); e1[p] = cvtpk(of[8 + 2 * p], of[9 + 2 * p]); }
      if (hh) { e1[2] = 0u; e1[3] = 0u; }
      const unsigned a = WS_O + rt * 32768 + j * 2048 + lane * 16;
      ST16(e0, a); ST16(e1, a + 1024);
      unsigned ex = 0;
#pragma unroll
      for (int p = 0; p < 4; ++p) ex |= ((e0[p] & 0x7f807f80u) + 0x00800080u) | ((e1[p] & 0x7f807f80u) + 0x00800080u);
      return __any((ex & 0x80008000u) != 0u || !(l < 1.2676506002282294e30f));
    };
    post<A_CMB>(ev); STAMP(9); pwait<A_CMB>(ev);
    if (__builtin_expect(combine(), 0)) *(LAS unsigned*)(lds + L_CNT + 4 * C_BAD) = 1u;
    STAMP(10);
    WAIT_VM(0);
    const unsigned arr = cnt_add(lds, C_O);
    __builtin_amdgcn_s_setprio(0);
    if (arr != 3u) pwait_long<C_O>(ev);
    const bool bad = __builtin_amdgcn_readfirstlane(*(volatile LAS unsigned*)(lds + L_CNT + 4 * C_BAD)) != 0u;
    if (!bad && arr == 3u && lane == 0) __hip_atomic_store((GAS u64*)(ws + WS_FO + fme), tag, RLX_AGENT);
    if (__builtin_expect(bad, 0)) {
#pragma unroll
      for (int q = 0; q < 4; ++q) { set_ref(qf[q][1], 0.f, hh); o[q] = zero; mref[q] = -3.0e38f; }
#pragma nounroll
      for (int kt = 0; kt < 16; ++kt) {
        const u32x4 k0 = LD16(kvb + kt * 4096), k1 = LD16(kvb + kt * 4096 + 1024), v0 = LD16(kvb + kt * 4096 + 2048), v1 = LD16(kvb + kt * 4096 + 3072);
#pragma unroll
        for (int q = 0; q < 4; ++q) {
          f32x16 s = MFMA32(asfrag(k0), qf[q][0], zero); s = MFMA32(asfrag(k1), qf[q][1], s);
          const float mn = __builtin_fmaxf(mref[q], tile_max(s)), al = __builtin_amdgcn_exp2f(mref[q] - mn); mref[q] = mn;
#pragma unroll
          for (int r = 0; r < 16; ++r) { o[q][r] *= al; s[r] = __builtin_amdgcn_exp2f(s[r] - mn); }
          const u32x4 p0 = {cvtpk(s[0], s[1]), cvtpk(s[2], s[3]), cvtpk(s[4], s[5]), cvtpk(s[6], s[7])}, p1 = {cvtpk(s[8], s[9]), cvtpk(s[10], s[11]), cvtpk(s[12], s[13]), cvtpk(s[14], s[15])};
          o[q] = MFMA32(asfrag(v0), asfrag(p0), o[q]); o[q] = MFMA32(asfrag(v1), asfrag(p1), o[q]);
        }
      }
      PUT_O(0); PUT_O(1); PUT_O(2);
      post<A_CMB2>(ev); pwait<A_CMB2>(ev);
      combine();
      WAIT_VM(0);
      if (cnt_add(lds, C_O2) == 3u && lane == 0) __hip_atomic_store((GAS u64*)(ws + WS_FO + fme), tag, RLX_AGENT);
    }
    STAMP(8); CSTAMP(31); STAMP_OUT();
  } else {
    Ld k;
    k.xsrc = x + (rb * 128 + t * 32) * 896; k.xvo0 = (lane >> 3) * 896 + (((lane & 7) ^ (lane >> 4)) << 4); k.xvo1 = k.xvo0 ^ 64u;
    k.xdst = lds0 + t * 4096; k.xdst6 = lds0 + (t < 2 ? 18 * SLOT + t * 4096 : (t - 2) * 4096);
#pragma unroll
    for (int i = 0; i < 7; ++i) k.ck[i] = i + r7 >= 7 ? i + r7 - 7 : i + r7;
    { const int row = 16 * t + (lane >> 3); const unsigned piece = ((lane & 7) ^ (((lane >> 4) & 1) << 2)) << 4;
      k.w0rs = j < 14 ? 2688u : 3584u; k.w0src = j < 14 ? wqkv + 896 + j * 128 : w1 + (j - 14) * 128;
      k.fasrc = w1 + (2 + j) * 128; k.fbsrc = w1 + (12 + j) * 128; k.dst = lds0 + t * 2048;
      k.w0vo = row * k.w0rs + piece; k.wfvo = row * 3584 + piece;
      k.wqsrc = wqkv + j * 56; k.wqvo = (16 * t + (lane >> 2)) * 2688 + (lane & 3) * 16; k.qdst = lds0 + t * 1024; k.ev = ev; }
    {
      COMMON_SETUP();
      STAMP(14);
      p1_stream<0>(k, xa, xrd, xwrap STA);
      STAMP(15);
      __builtin_amdgcn_s_setprio(GPRIO);
#pragma unroll
      for (int s = 0; s < 4; ++s) { wr0[s] += L_FA; wr1[s] += L_FA; PIN(wr0[s]); PIN(wr1[s]); }
      WF f[3]; f32x16 a0 = {}, a1 = {}; f32x4 c0 = {}, c1 = {}; float cr[16]; u32x4 o[2];
      WAIT_VM(14); post<L_FAL>(ev);
      pwait<A_CS0>(ev); pwait<L_FAL>(ev);
#pragma unroll
      for (int i = 0; i < 7; ++i) glds16(k.wqsrc + k.ck[i] * (64 * 2688), k.wqvo, k.qdst + (i < 6 ? L_WQ + i * 4096 : 0));
      wf_read(f[0], wr0[0], wr1[0]); wf_read(f[1], wr0[1], wr1[1]);
      f_steps<0, 0>(k, xa, f, a0, a1, c0, c1, sel, wr0, wr1);
      cs_put(lds, L_CSA, 0, t, lane, c0); cs_put(lds, L_CSA, 1, t, lane, c1);
      pwait<A_ST>(ev);
      rs = *(LAS float*)(lds + L_STAT + t * 256 + r32 * 4); nmb = *(LAS float*)(lds + L_STAT + t * 256 + 128 + r32 * 4);
      WAIT_VM(0); post<L_CSAP>(ev); pwait<L_CSAP>(ev);
      STAMP(2);
      __builtin_amdgcn_s_setprio(3);
      { const unsigned ad = WS_H + rt * 114688 + (2 + j) * 4096 + lane * 16;
        lane_to_regs(priv, r32, hh, cs_get(lds, L_CSA, 0, r32), cr); epi_T(a0, cr, rs, nmb, (short)0, o[0], o[1]); ST16(o[0], ad); ST16(o[1], ad + 1024);
        lane_to_regs(priv + 128, r32, hh, cs_get(lds, L_CSA, 1, r32), cr); epi_T(a1, cr, rs, nmb, (short)0, o[0], o[1]); ST16(o[0], ad + 2048); ST16(o[1], ad + 3072); }
      STAMP(3);
      if (j >= 6) {
#pragma unroll
        for (int s = 0; s < 4; ++s) { wr0[s] += L_FB - L_FA; wr1[s] += L_FB - L_FA; PIN(wr0[s]); PIN(wr1[s]); }
        a0 = (f32x16){}; a1 = (f32x16){}; c0 = (f32x4){}; c1 = (f32x4){};
        wf_read(f[0], wr0[0], wr1[0]); wf_read(f[1], wr0[1], wr1[1]);
        f_steps<1, 0>(k, xa, f, a0, a1, c0, c1, sel, wr0, wr1);
        cs_put(lds, L_CSB, 0, t, lane, c0); cs_put(lds, L_CSB, 1, t, lane, c1);
        post<L_CSBP>(ev); pwait<L_CSBP>(ev);
        const unsigned ad = WS_H + rt * 114688 + (12 + j) * 4096 + lane * 16;
        lane_to_regs(priv, r32, hh, cs_get(lds, L_CSB, 0, r32), cr); epi_T(a0, cr, rs, nmb, (short)0, o[0], o[1]); ST16(o[0], ad); ST16(o[1], ad + 1024);
        lane_to_regs(priv + 128, r32, hh, cs_get(lds, L_CSB, 1, r32), cr); epi_T(a1, cr, rs, nmb, (short)0, o[0], o[1]); ST16(o[0], ad + 2048); ST16(o[1], ad + 3072);
      } else post<L_CSBP>(ev);
    }
    __builtin_amdgcn_s_setprio(0);
    pwait<A_QW>(ev);
    u64 tagraw = *(LAS u64*)(lds + L_TAG);
    GET_TAG();
    WAIT_VM(0);
    if (cnt_add(lds, C_H) == 7u && lane == 0) __hip_atomic_store((GAS u64*)(ws + WS_FH + fme), tag, RLX_AGENT);
    STAMP(4);
    const int cg = bi >> 2;
    if (cg < 7) {
      const int rt0 = 8 * xg + 2 * (bi & 3), rb3 = rt0 >> 2;
      const int ring = L_RING + 3 * t * SLOT;
      const int ct = t >> 1, rtl = t & 1;
      G2 g; g.ev = ev; g.t = t; g.ringdst = lds0 + ring; g.lv = lane * 16;
      g.w2vo = (lane >> 3) * 896 + (((lane & 7) ^ (((lane >> 4) & 1) << 2)) << 4);
      g.w2s = w2 + t * 448 * 896 + cg * 128; g.wos = wo + t * 112 * 896 + cg * 128;
      g.hb0 = ws + WS_H + (rt0 + rtl) * 114688 + t * 28672; g.hb1 = ws + WS_H + (rt0 + (rtl ^ 1)) * 114688 + t * 28672;
      g.br0 = wrd0 + ring + ((ct ^ (q4 >> 1)) << 6); g.br1 = wrd0 + ring + ((ct ^ 1 ^ (q4 >> 1)) << 6);
      g2_ws<0, 12>(g);
      const int fb3 = rb3 * 16 + (lane & 15);
      __builtin_amdgcn_s_setprio(0);
      poll_flags<8>(ws + WS_FH + fb3 * 128, tag);
      __builtin_amdgcn_s_setprio(G2PRIO);
      WAIT_VM(0);
      STAMP(5);
      u32x4 hf[14][2];
#pragma unroll
      for (int s = 0; s < 14; ++s) { ldg_sc1(hf[s][0], g.hb0 + s * 1024, g.lv); ldg_sc1(hf[s][1], g.hb1 + s * 1024, g.lv); }
      f32x16 a00 = {}, a01 = {}, a10 = {}, a11 = {};
      PIN(g.br0); PIN(g.br1);
      { WF f[3]; wf_read(f[0], g.br0, g.br1); wf_read(f[1], g.br0 + 2048, g.br1 + 2048);
        g2_steps<0>(g, hf, f, a00, a01, a10, a11); }
      STAMP(6);
      WAIT_VM(0);
      bf16x8 wf[8][2];
      { const lptr o0 = lds + ring + SLOT + (4 * hh + q4) * 128 + pi_std * 8;
#pragma unroll
        for (int e = 0; e < 8; ++e)
#pragma unroll
          for (int cc = 0; cc < 2; ++cc) wf[e][cc] = trfrag(o0 + ((cc ^ ct ^ (q4 >> 1)) << 6) + ((e >> 1) * 28 + (e & 1) * 16) * 128, 1024); }
      const size_t oo = (size_t)((rt0 + rtl) * 32 + r32) * 896 + cg * 128 + ct * 64 + hh * 16;
      const u32x4 xr0 = *(const GAS u32x4*)(x + oo), xr1 = *(const GAS u32x4*)(x + oo + 32);
      poll_flags<1>(ws + WS_FO + fb3 * 128, tag);
      STAMP(7);
      { const unsigned ob0 = WS_O + (rt0 + rtl) * 32768 + t * 8192 + lane * 16, ob1 = ob0 ^ 32768u;
        u32x4 of[8][2];
#pragma unroll
        for (int e = 0; e < 8; ++e) { of[e][0] = LD16(ob0 + e * 1024); of[e][1] = LD16(ob1 + e * 1024); }
        SBAR();
#pragma unroll
        for (int e = 0; e < 8; ++e) { const bf16x8 h0 = asfrag(of[e][0]), h1 = asfrag(of[e][1]);
          a11 = MFMA32(wf[e][1], h1, a11); a10 = MFMA32(wf[e][1], h0, a10); a01 = MFMA32(wf[e][0], h1, a01); a00 = MFMA32(wf[e][0], h0, a00);
          PIN(a00); PIN(a01); PIN(a10); PIN(a11); } }
      STAMP(9);
      if (b == 0 && tid == 256) __hip_atomic_store((GAS u64*)(ws + WS_EPOCH), tag, RLX_AGENT);
      { const lptr mine = lds + ring + lane * 16;
        auto put = [&](const f32x16& a, int e) {
#pragma unroll
          for (int i = 0; i < 4; ++i) *(LAS f32x4*)(mine + e * 4096 + i * 1024) = (f32x4){a[4 * i], a[4 * i + 1], a[4 * i + 2], a[4 * i + 3]}; };
        put(a11, 3); put(a10, 2); put(a01, 1); }
      f32x16 fin = a00;
      post<L_RED>(ev); STAMP(10); pwait<L_RED>(ev);
#pragma unroll
      for (int e = 1; e < 4; ++e) { const lptr a = lds + L_RING + 3 * (t ^ e) * SLOT + e * 4096 + lane * 16;
#pragma unroll
        for (int i = 0; i < 4; ++i) { const f32x4 v = ldsf4(a + i * 1024); fin[4 * i] += v[0]; fin[4 * i + 1] += v[1]; fin[4 * i + 2] += v[2]; fin[4 * i + 3] += v[3]; } }
      u32x4 r0, r1;
#pragma unroll
      for (int p = 0; p < 4; ++p) { r0[p] = cvtpk(fin[2 * p] + bflo(xr0[p]), fin[2 * p + 1] + bfhi(xr0[p])); r1[p] = cvtpk(fin[8 + 2 * p] + bflo(xr1[p]), fin[9 + 2 * p] + bfhi(xr1[p])); }
      *(GAS u32x4*)(out + oo) = r0; *(GAS u32x4*)(out + oo + 32) = r1;
      STAMP(8);
    }
    STAMP_OUT();
  }
}

extern "C" void kernel_launch(void* const* d_in, const int* in_sizes, int n_in, void* d_out, int out_size, void* d_ws, size_t ws_size, hipStream_t stream) {
  fused_block<<<256, 512, 0, stream>>>((const char*)d_in[0], (const char*)d_in[1], (const char*)d_in[2], (const char*)d_in[4], (const char*)d_in[5], (char*)d_out, (char*)d_ws + WS_BASE);
}
```

```cpp
#include <hip/hip_runtime.h>
#include <stdint.h>

typedef short bf16x8 __attribute__((ext_vector_type(8)));
typedef short s16x4 __attribute__((ext_vector_type(4)));
typedef float f32x16 __attribute__((ext_vector_type(16)));
typedef float f32x4 __attribute__((ext_vector_type(4)));
typedef float f32x2 __attribute__((ext_vector_type(2)));
typedef __bf16 bf16x2v __attribute__((ext_vector_type(2)));
typedef unsigned u32x4 __attribute__((ext_vector_type(4)));
typedef unsigned long long u64;
#define LAS __attribute__((address_space(3)))
#define GAS __attribute__((address_space(1)))
#define DI __device__ __forceinline__
#define MFMA32(a, b, c) __builtin_amdgcn_mfma_f32_32x32x16_bf16(a, b, c, 0, 0, 0)
#define MFMA16(a, b, c) __builtin_amdgcn_mfma_f32_16x16x32_bf16(a, b, c, 0, 0, 0)
#define SBAR() __builtin_amdgcn_sched_barrier(0)
#define PIN(x) asm volatile("" : "+v"(x))
#define RLX_AGENT __ATOMIC_RELAXED, __HIP_MEMORY_SCOPE_AGENT
#define WAIT_BAR(N) asm volatile("s_waitcnt vmcnt(" #N ") lgkmcnt(0)\n\ts_barrier" ::: "memory")
#define WAIT_VM(N) asm volatile("s_waitcnt vmcnt(" #N ")" ::: "memory")
#define WAIT_LGKM0() asm volatile("s_waitcnt lgkmcnt(0)" ::: "memory")

constexpr unsigned WS_EPOCH = 0;
constexpr unsigned WS_FKV = 4096, WS_FH = WS_FKV + 32768, WS_FO = WS_FH + 32768;
constexpr unsigned WS_KV = 1u << 20;
constexpr unsigned WS_H = 5u << 20;
constexpr unsigned WS_O = 13u << 20;
constexpr unsigned WS_BYTES = 15u << 20;
constexpr int SLOT = 8192, NSLOT = 19;
constexpr int L_MISC = NSLOT * SLOT;
constexpr int L_STAT = L_MISC;
constexpr int L_CS0 = L_MISC + 2048, L_CSA = L_CS0 + 1024, L_CSB = L_CSA + 1024;
constexpr int L_PRIV = L_MISC + 5120;
constexpr int L_CNT = L_MISC + 7168;
constexpr int L_TAG = L_MISC + 7424;
constexpr int LDS_BYTES = 163840;
constexpr int L_FA = 2 * SLOT, L_WQ = 9 * SLOT, L_FB = 13 * SLOT, L_QX = SLOT;
constexpr int ring_of(int t) { return (t < 2 ? 2 + 3 * t : 7 + 3 * t) * SLOT; }
constexpr int p1_slot(int item) { return (item % NSLOT) * SLOT; }
enum { C_KV, C_H, C_O, C_O2, C_BAD,
       A_RD0, A_CS0 = A_RD0 + 6, A_CSX, A_ST, A_FB, A_QW, A_DONE, A_CMB, A_CMB2,
       L_LD0, L_X6 = L_LD0 + 7, L_FAL, L_WQL, L_CSAP, L_CSBP, L_WO, L_WOR, L_RED, L_FIN, N_EV };

#define WS_BASE 0
#define TOUCH 0
#define ROT7 0
#define MAPA 8
#define G2_SPLIT 14
#define CODE_BYTES 55296
constexpr float QSCALE = 0.18898223650461362f * 1.4426950408889634f;
constexpr float REF_MARGIN = 64.f;
constexpr float REF_LIMIT = 4096.f;
constexpr unsigned SPIN_CAP = 1u << 16;

DI unsigned cvtpk(float lo, float hi) { f32x2 v = {lo, hi}; return __builtin_bit_cast(unsigned, __builtin_convertvector(v, bf16x2v)); }
DI float sc1(float x) { asm("" : "+v"(x)); return x; }
DI float bf16r(float x) { return __uint_as_float(cvtpk(x, 0.f) << 16); }
DI float bflo(unsigned v) { return __uint_as_float(v << 16); }
DI float bfhi(unsigned v) { return __uint_as_float(v & 0xffff0000u); }
DI float swap_add(float v) { auto r = __builtin_amdgcn_permlane32_swap(__float_as_uint(v), __float_as_uint(v), false, false); return __uint_as_float(r[0]) + __uint_as_float(r[1]); }
DI float swap_max(float v) { auto r = __builtin_amdgcn_permlane32_swap(__float_as_uint(v), __float_as_uint(v), false, false); return __builtin_fmaxf(__uint_as_float(r[0]), __uint_as_float(r[1])); }
DI float upper_bcast(float v) { auto r = __builtin_amdgcn_permlane32_swap(__float_as_uint(v), __float_as_uint(v), false, false); return __uint_as_float(r[1]); }
DI void glds16(const void* sbase, unsigned voff, unsigned ldsdst) {
  unsigned keep;
  asm volatile("s_mov_b32 %0, m0\n\ts_mov_b32 m0, %3\n\ts_nop 0\n\tglobal_load_lds_dwordx4 %1, %2\n\ts_mov_b32 m0, %0"
               : "=&s"(keep) : "v"(voff), "s"(sbase), "s"(ldsdst) : "memory");
}
typedef LAS char* lptr;
typedef short v4i16_t __attribute__((ext_vector_type(4)));
DI s16x4 trd(lptr p) { return __builtin_bit_cast(s16x4, __builtin_amdgcn_ds_read_tr16_b64_v4i16((LAS v4i16_t*)p)); }
DI bf16x8 trfrag(lptr p, int second) { s16x4 lo = trd(p), hi = trd(p + second); return __builtin_shufflevector(lo, hi, 0, 1, 2, 3, 4, 5, 6, 7); }
DI bf16x8 ldsb128(lptr p) { return *(LAS bf16x8*)p; }
DI f32x4 ldsf4(lptr p) { return *(LAS f32x4*)p; }
DI bf16x8 asfrag(u32x4 v) { return __builtin_bit_cast(bf16x8, v); }
struct Ev { unsigned base, one; };
template <int E> DI void post(const Ev& e) {
  asm volatile("s_mov_b64 exec, 1\n\tds_add_u32 %0, %1 offset:%2\n\ts_mov_b64 exec, -1" :: "v"(e.base), "v"(e.one), "i"(4 * E) : "memory"); }
template <int E> DI void pwait(const Ev& e) {
  unsigned a;
  asm volatile("1:\n\tds_read_b32 %0, %1 offset:%2\n\ts_waitcnt lgkmcnt(0)\n\tv_cmp_gt_u32 vcc, 4, %0\n\ts_cbranch_vccnz 1b"
               : "=&v"(a) : "v"(e.base), "i"(4 * E) : "vcc", "memory"); }
template <int E> DI void pwait_long(const Ev& e) {
  unsigned a;
  asm volatile("1:\n\ts_sleep 8\n\tds_read_b32 %0, %1 offset:%2\n\ts_waitcnt lgkmcnt(0)\n\tv_cmp_gt_u32 vcc, 4, %0\n\ts_cbranch_vccnz 1b"
               : "=&v"(a) : "v"(e.base), "i"(4 * E) : "vcc", "memory"); }
DI unsigned cnt_add(lptr lds, int c) { unsigned r = 0; if ((threadIdx.x & 63) == 0) r = __hip_atomic_fetch_add((LAS unsigned*)(lds + L_CNT + 4 * c), 1u, __ATOMIC_RELAXED, __HIP_MEMORY_SCOPE_WORKGROUP);
  return __builtin_amdgcn_readfirstlane(r); }
template <int SLEEP> DI void poll_flags(const char* flag, u64 tag) {
  u64 v; unsigned n = SPIN_CAP;
  asm volatile("1:\n\tglobal_load_dwordx2 %0, %2, off sc1\n\ts_sub_u32 %1, %1, 1\n\ts_cbranch_scc1 2f\n\ts_sleep %4\n\ts_waitcnt vmcnt(0)\n\t"
               "v_cmp_ne_u64 vcc, %3, %0\n\ts_cbranch_vccnz 1b\n2:\n\ts_waitcnt vmcnt(0)"
               : "=&v"(v), "+s"(n) : "v"(flag), "s"(tag), "i"(SLEEP) : "vcc", "scc", "memory");
}
DI unsigned poll_bit(const char* flag, u64 tag, int h) {
  u64 v, m; unsigned n = SPIN_CAP;
  asm volatile("1:\n\tglobal_load_dwordx2 %0, %3, off sc1\n\ts_sub_u32 %2, %2, 1\n\ts_cbranch_scc1 2f\n\ts_waitcnt vmcnt(0)\n\t"
               "v_cmp_eq_u64 %1, %4, %0\n\ts_nop 3\n\ts_bitcmp0_b64 %1, %5\n\ts_cbranch_scc1 1b\n2:\n\ts_waitcnt vmcnt(0)"
               : "=&v"(v), "=&s"(m), "+s"(n) : "v"(flag), "s"(tag), "i"(h) : "scc", "memory");
  return (unsigned)m;
}
#define STAMP(i) do {} while (0)
#define CSTAMP(i) do {} while (0)
#define VSTAMP(i, v) do {} while (0)
#define STAMP_OUT() do {} while (0)
#define NRESC
#define STV
#define STA
#define FSTAMP(i) do {} while (0)
#define LSTAMP(i) do {} while (0)
#define BAR() asm volatile("s_waitcnt lgkmcnt(0)\n\ts_barrier" ::: "memory")

#define QPRIO 3
#define LPRIO 2
#define GPRIO 1
#define G2PRIO 0
#define NOCHECK 0
#define QSPLIT 8
#define OLAG 4
#define LOOK 4
DI float pick(u64 m, float hi, float lo) { float r; asm("v_cndmask_b32 %0, %1, %2, %3" : "=v"(r) : "v"(lo), "v"(hi), "s"(m)); return r; }
DI float gram_diag(const f32x16& g) {
  const u64 m1 = 0xaaaaaaaaaaaaaaaaull, m2 = 0xccccccccccccccccull, m4 = 0xf0f0f0f0f0f0f0f0ull, m8 = 0xff00ff00ff00ff00ull, m16 = 0xffff0000ffff0000ull;
  const float b0 = pick(m2, pick(m1, g[3], g[2]), pick(m1, g[1], g[0])), b1 = pick(m2, pick(m1, g[7], g[6]), pick(m1, g[5], g[4]));
  const float b2 = pick(m2, pick(m1, g[11], g[10]), pick(m1, g[9], g[8])), b3 = pick(m2, pick(m1, g[15], g[14]), pick(m1, g[13], g[12]));
  float d = pick(m16, pick(m8, b3, b2), pick(m8, b1, b0));
  asm volatile("s_nop 1" : "+v"(d));
  auto r = __builtin_amdgcn_permlane32_swap(__float_as_uint(d), __float_as_uint(d), false, false);
  return pick(m4, __uint_as_float(r[1]), __uint_as_float(r[0]));
}
template <int C> DI void x_frags(bf16x8 (&xa)[28], const lptr (&xrd)[4], int xwrap) {
#pragma unroll
  for (int s = 0; s < 4; ++s) xa[4 * C + s] = ldsb128(xrd[s] + p1_slot(3 * C) + (C == 6 ? xwrap : 0));
}
DI void row_norm(float s1, float s2, float& rs, float& nmb) {
  const float mean = sc1(s1 * (1.f / 448.f)), var = __builtin_fmaxf(sc1(s2 * (1.f / 448.f)) - mean * mean, 0.f);
  const float sd = bf16r(__builtin_amdgcn_sqrtf(bf16r(bf16r(var) + 1e-5f))); rs = __builtin_amdgcn_rcpf(sd); nmb = -bf16r(mean);
}
struct P1A { lptr x[4], w0[4], w1[4]; int xwrap; Ev ev; };
struct WF { s16x4 l0, h0, l1, h1; };
DI bf16x8 cat(const s16x4& lo, const s16x4& hi) { return __builtin_shufflevector(lo, hi, 0, 1, 2, 3, 4, 5, 6, 7); }
struct P1R { f32x16 a0, a1; f32x4 c0, c1, s1; f32x16 s2; };
#define MM(VT, acc, xf, wf) do { if constexpr (VT) acc = MFMA32(xf, wf, acc); else acc = MFMA32(wf, xf, acc); PIN(acc); SBAR(); } while (0)
template <bool VT, int ST, int C, int S> DI void p1_group(bf16x8 (&xa)[28], WF (&f)[4], P1R& r, const bf16x8& sel, const P1A& p) {
  constexpr int xo = p1_slot(3 * C) - (C >> 1) * 6 * SLOT, wo = p1_slot(3 * C + 2) - (C >> 1) * 6 * SLOT;
  if constexpr (ST >= 0) MM(VT, r.a0, xa[ST], cat(f[ST & 3].l0, f[ST & 3].h0));
  xa[4 * C + S] = ldsb128(p.x[S] + xo); f[S].l0 = trd(p.w0[S] + wo); SBAR();
  if constexpr (ST >= 0) MM(VT, r.a1, xa[ST], cat(f[ST & 3].l1, f[ST & 3].h1));
  f[S].h0 = trd(p.w0[S] + wo + 512); f[S].l1 = trd(p.w1[S] + wo); SBAR();
  if constexpr (ST >= 0) { r.s2 = MFMA32(xa[ST], xa[ST], r.s2); PIN(r.s2); SBAR(); }
  f[S].h1 = trd(p.w1[S] + wo + 512); SBAR();
  if constexpr (ST >= 0) { r.s1 = MFMA16(sel, xa[ST], r.s1); PIN(r.s1);
    if constexpr ((ST & 3) == 0) { r.c0 = MFMA16(sel, cat(f[0].l0, f[0].h0), r.c0); r.c1 = MFMA16(sel, cat(f[0].l1, f[0].h1), r.c1); PIN(r.c0); PIN(r.c1); }
    SBAR(); }
}
template <bool VT, int C> DI void p1_compute(bf16x8 (&xa)[28], WF (&f)[4], P1R& r, const bf16x8& sel, P1A& p STV) {
  if constexpr (C == 0) pwait<L_LD0>(p.ev);
  if (C == 0) STAMP(12);
  FSTAMP(15 + C);
  if constexpr (C < 6) {
    p1_group<VT, 4 * C - 2, C, 0>(xa, f, r, sel, p); p1_group<VT, 4 * C - 1, C, 1>(xa, f, r, sel, p); p1_group<VT, 4 * C, C, 2>(xa, f, r, sel, p);
    const unsigned nxt = *(volatile LAS unsigned*)(uintptr_t)(p.ev.base + 4 * (L_LD0 + C + 1));
    p1_group<VT, 4 * C + 1, C, 3>(xa, f, r, sel, p);
    post<A_RD0 + C>(p.ev);
    if constexpr (C == 1 || C == 3) {
#pragma unroll
      for (int s = 0; s < 4; ++s) { p.x[s] += 6 * SLOT; p.w0[s] += 6 * SLOT; p.w1[s] += 6 * SLOT; PIN(p.x[s]); PIN(p.w0[s]); PIN(p.w1[s]); }
    }
    if constexpr (C == 5) {
#pragma unroll
      for (int s = 0; s < 4; ++s) { p.x[s] += p.xwrap; p.w0[s] -= 12 * SLOT; p.w1[s] -= 12 * SLOT; PIN(p.x[s]); PIN(p.w0[s]); PIN(p.w1[s]); }
    }
    SBAR();
    if (__builtin_expect(__builtin_amdgcn_readfirstlane(nxt) < 4u, 0)) pwait<L_LD0 + C + 1>(p.ev);
    p1_compute<VT, C + 1>(xa, f, r, sel, p STA);
  }
}
struct Ld { const char *xsrc, *w0src, *wqsrc, *fasrc; unsigned xvo0, xvo1, w0vo, wfvo, wqvo, w0rs, dst, xdst, xdst6, qdst; Ev ev; int ck[7]; };
template <int N, int E> DI void ld_piece(const Ld& k) {
  const unsigned dst = k.dst + p1_slot(N) + E * 1024;
  if constexpr (N < 21) {
    constexpr int c = N / 3, q = N % 3;
    if constexpr (q < 2) glds16(k.xsrc + (q * 16 + E * 8) * 896 + k.ck[c] * 128, E ? k.xvo1 : k.xvo0, (c < 6 ? k.xdst + p1_slot(3 * c) : k.xdst6) + (2 * q + E) * 1024);
    else glds16(k.w0src + (k.ck[c] * 64 + E * 8) * k.w0rs, k.w0vo, dst);
  } else if constexpr (N < 28) glds16(k.fasrc + (k.ck[N - 21] * 64 + E * 8) * 3584, k.wfvo, dst);
  else { constexpr int c = 2 * (N - 28) + E;
    glds16(k.wqsrc + k.ck[c < 7 ? c : 0] * (64 * 2688), k.wqvo, k.qdst + c * 4096); }
}
template <int N> DI void p1_stream(const Ld& k, bf16x8 (&xa)[28], const lptr (&xrd)[4], int xwrap STV) {
  if constexpr (N < 32) {
    if constexpr (N == 18 || (N > 19 && (N - 19) % 3 == 0)) { pwait<A_RD0 + (N - 18) / 3>(k.ev); pwait<L_LD0 + (N - 18) / 3 + 1>(k.ev); }
    ld_piece<N, 0>(k); ld_piece<N, 1>(k);
    if constexpr (N == 2) asm volatile("s_barrier" ::: "memory");
    constexpr int M = N - LOOK;
    if constexpr (M >= 2 && M <= 20 && M % 3 == 2) {
      constexpr int C = M / 3;
      asm volatile("s_waitcnt vmcnt(%0)" :: "i"(2 * LOOK) : "memory");
      post<L_LD0 + C>(k.ev);
      if (C == 0) STAMP(12);
      x_frags<C>(xa, xrd, xwrap); SBAR();
      if (C == 6) STAMP(1);
    } else if constexpr (M == 21) post<L_X6>(k.ev);
    else if constexpr (M == 27) { asm volatile("s_waitcnt vmcnt(%0)" :: "i"(2 * LOOK) : "memory"); post<L_FAL>(k.ev); }
    p1_stream<N + 1>(k, xa, xrd, xwrap STA);
  }
}
DI void cs_put(lptr lds, int area, int c, int t, int lane, const f32x4& cs) {
  if (lane < 16) { *(LAS float*)(lds + area + c * 512 + lane * 16 + t * 4) = cs[0]; *(LAS float*)(lds + area + c * 512 + (lane + 16) * 16 + t * 4) = cs[1]; }
}
DI float cs_get(lptr lds, int area, int c, int r32) { const f32x4 p = ldsf4(lds + area + c * 512 + r32 * 16); return sc1(sc1(p[0] + p[1]) + sc1(p[2] + p[3])); }
DI void lane_to_regs(lptr priv, int r32, int hh, float v, float (&o)[16]) {
  *(LAS float*)(priv + r32 * 4) = v;
#pragma unroll
  for (int i = 0; i < 4; ++i) { const f32x4 q = ldsf4(priv + (8 * i + 4 * hh) * 4); o[4 * i] = q[0]; o[4 * i + 1] = q[1]; o[4 * i + 2] = q[2]; o[4 * i + 3] = q[3]; }
}
typedef short s16x2 __attribute__((ext_vector_type(2)));
template <bool RELU> DI void epi_T(const f32x16& acc, const float (&cs)[16], float sc, float nmb, u32x4& o0, u32x4& o1) {
#pragma unroll
  for (int p = 0; p < 8; ++p) {
    const float v0 = sc1(sc1(__builtin_fmaf(cs[2 * p], nmb, acc[2 * p])) * sc), v1 = sc1(sc1(__builtin_fmaf(cs[2 * p + 1], nmb, acc[2 * p + 1])) * sc);
    unsigned w = cvtpk(v0, v1);
    if (RELU) w = __builtin_bit_cast(unsigned, __builtin_elementwise_max(__builtin_bit_cast(s16x2, w), (s16x2){0, 0}));
    if (p < 4) o0[p] = w; else o1[p - 4] = w;
  }
}
#define ST16(v, off) __builtin_amdgcn_raw_buffer_store_b128(v, rsw, off, 0, 16)
#define ABL_NOKV 0
#define LDAUX 16
#define LD16(off) __builtin_amdgcn_raw_buffer_load_b128(rsw, off, 0, LDAUX)

DI float set_ref(bf16x8& q1, float m, int hh) {
  const unsigned w = cvtpk(-__builtin_rintf(m), 0.f);
  u32x4 v = __builtin_bit_cast(u32x4, q1); v[2] = hh ? w : v[2]; q1 = __builtin_bit_cast(bf16x8, v);
  return -__uint_as_float(w << 16);
}
DI float tile_max(const f32x16& s) {
  float a = __builtin_fmaxf(__builtin_fmaxf(s[0], s[1]), s[2]), b = __builtin_fmaxf(__builtin_fmaxf(s[3], s[4]), s[5]);
#pragma unroll
  for (int r = 6; r < 14; r += 4) { a = __builtin_fmaxf(__builtin_fmaxf(a, s[r]), s[r + 1]); b = __builtin_fmaxf(__builtin_fmaxf(b, s[r + 2]), s[r + 3]); }
  a = __builtin_fmaxf(__builtin_fmaxf(a, s[14]), s[15]);
  return swap_max(__builtin_fmaxf(a, b));
}

template <int P, int ST> DI void f_steps(const Ev& ev, const bf16x8 (&xa)[28], WF (&f)[3], f32x16& a0, f32x16& a1, f32x4& c0, f32x4& c1, const bf16x8& sel, const lptr (&w0)[4], const lptr (&w1)[4]) {
  if constexpr (ST < 28) {
    constexpr int N = ST + 2, off = p1_slot((P ? 32 : 21) + ((N < 28 ? N : 0) >> 2)) - (P ? L_FB : L_FA);
    SBAR(); MM(false, a0, xa[ST], cat(f[ST % 3].l0, f[ST % 3].h0));
    if constexpr (N < 28) { f[N % 3].l0 = trd(w0[N & 3] + off); f[N % 3].h0 = trd(w0[N & 3] + off + 512); SBAR(); }
    MM(false, a1, xa[ST], cat(f[ST % 3].l1, f[ST % 3].h1));
    if constexpr (N < 28) { f[N % 3].l1 = trd(w1[N & 3] + off); f[N % 3].h1 = trd(w1[N & 3] + off + 512); SBAR(); }
    if constexpr ((ST & 3) == 0) { c0 = MFMA16(sel, cat(f[ST % 3].l0, f[ST % 3].h0), c0); c1 = MFMA16(sel, cat(f[ST % 3].l1, f[ST % 3].h1), c1); PIN(c0); PIN(c1); }
    if constexpr (P == 0 && ST == 6) { WAIT_VM(0); post<L_WQL>(ev); }
    f_steps<P, ST + 1>(ev, xa, f, a0, a1, c0, c1, sel, w0, w1);
  }
}
DI void wf_read(WF& f, lptr p0, lptr p1) { f.l0 = trd(p0); f.h0 = trd(p0 + 512); f.l1 = trd(p1); f.h1 = trd(p1 + 512); }

constexpr int g2_issued(int st) { int n = 28; for (int s = 0; s < st; ++s) { if (s < 14) n += 2; if (s < 24) n += 2; } return n; }
constexpr int g2_pos_h(int s) { return s < 14 ? 2 * s + 1 : g2_issued(s - 14) + 1; }
constexpr int g2_pos_w(int s) { return s < 12 ? -1 : g2_issued(s - 12) + (s - 12 < 14 ? 2 : 0) + 1; }
constexpr int g2_wait(int st) { const int a = g2_pos_h(st), b = st + 2 < 28 ? g2_pos_w(st + 2) : -1; const int n = g2_issued(st) - 1 - (a > b ? a : b); return n > 63 ? 63 : n; }
DI void ldg_sc1(u32x4& d, const char* sbase, unsigned voff) { asm volatile("global_load_dwordx4 %0, %1, %2 sc1" : "=v"(d) : "v"(voff), "s"(sbase) : "memory"); }
struct G2 { const char *hb0, *hb1, *w2s, *wos; unsigned lv, w2vo, ringdst; Ev ev; int t; lptr br0, br1; };
template <int ST, int E> DI void g2_w(const G2& g) {
  const unsigned dst = g.ringdst + (ST % 12) * 2048 + E * 1024;
  if constexpr (ST < 28) glds16(g.w2s + (ST * 16 + E * 8) * 896, g.w2vo, dst);
  else if constexpr (ST < 35) glds16(g.wos + ((ST - 28) * 16 + E * 8) * 896, g.w2vo, dst);
  else glds16(g.wos + (g.t == 3 ? 104 : 112 + E * 8) * 896, g.w2vo, dst);
}
template <int A, int B> DI void g2_ws(const G2& g) { if constexpr (A < B) { g2_w<A, 0>(g); g2_w<A, 1>(g); g2_ws<A + 1, B>(g); } }
template <int ST> DI void g2_steps(const G2& g, u32x4 (&hf)[14][2], WF (&f)[3], f32x16& a00, f32x16& a01, f32x16& a10, f32x16& a11) {
  if constexpr (ST < 28) {
    if constexpr (ST == G2_SPLIT) { __builtin_amdgcn_s_setprio(0); pwait_long<A_DONE>(g.ev); __builtin_amdgcn_s_setprio(GPRIO); }
    asm volatile("s_waitcnt vmcnt(%2)" : "+v"(hf[ST % 14][0]), "+v"(hf[ST % 14][1]) : "i"(g2_wait(ST)) : "memory");
    constexpr int N = ST + 2, off = (N % 12) * 2048;
    const bf16x8 h0 = asfrag(hf[ST % 14][0]), h1 = asfrag(hf[ST % 14][1]), w0 = cat(f[ST % 3].l0, f[ST % 3].h0), w1 = cat(f[ST % 3].l1, f[ST % 3].h1);
    SBAR(); MM(false, a00, h0, w0);
    if constexpr (N < 28) { f[N % 3].l0 = trd(g.br0 + off); f[N % 3].h0 = trd(g.br0 + off + 512); SBAR(); }
    MM(false, a01, h1, w0);
    if constexpr (N < 28) { f[N % 3].l1 = trd(g.br1 + off); f[N % 3].h1 = trd(g.br1 + off + 512); SBAR(); }
    MM(false, a10, h0, w1);
    MM(false, a11, h1, w1);
    if constexpr (ST < 14) { ldg_sc1(hf[ST][0], g.hb0 + (ST + 14) * 1024, g.lv); ldg_sc1(hf[ST][1], g.hb1 + (ST + 14) * 1024, g.lv); }
    if constexpr (ST < 24) { g2_w<ST + 12, 0>(g); g2_w<ST + 12, 1>(g); }
    g2_steps<ST + 1>(g, hf, f, a00, a01, a10, a11);
  }
}

__global__ void __launch_bounds__(512, 2) fused_block(const char* x, const char* wqkv, const char* wo, const char* w1, const char* w2, char* out, char* ws) {
  asm volatile("" : "+s"(x), "+s"(wqkv), "+s"(wo), "+s"(w1), "+s"(w2), "+s"(out), "+s"(ws));
  __shared__ __attribute__((aligned(16))) char smem[LDS_BYTES];
  u64 pc; asm volatile("s_getpc_b64 %0" : "=s"(pc));
  const lptr lds = (lptr)smem; const unsigned lds0 = (unsigned)(uintptr_t)lds;
  const int tid = threadIdx.x, lane = tid & 63, r32 = lane & 31, hh = lane >> 5;
  const int w = __builtin_amdgcn_readfirstlane(tid >> 6), t = w & 3, c = w >> 2;
  const int b = blockIdx.x, xg = b & 7, bi = b >> 3;
  const int j = (xg & 3) * 4 + (bi >> 3), rb = (xg >> 2) * 8 + (bi & 7);
  const int fme = (rb * 16 + j) * 128, rt = rb * 4 + t;
  STAMP(0); CSTAMP(30); VSTAMP(29, (unsigned)__builtin_amdgcn_s_getreg(20 | (3 << 11)) & 0xfu);
  const int rot = t, r7 = ROT7 ? (rb + 2 * j) % 7 : 0;
  const int q4 = (lane & 15) >> 2, p4 = lane & 3, g16 = (lane >> 4) & 1;
  const int pi_std = 4 * g16 + 2 * (p4 & 1) + (p4 >> 1);
  const lptr wrd0 = lds + (8 * hh + q4) * 128 + pi_std * 8;
  const lptr priv = lds + L_PRIV + w * 256;
  Ev ev = {lds0 + L_CNT, 1u}; PIN(ev.base); PIN(ev.one);
  const __amdgpu_buffer_rsrc_t rsw = __builtin_amdgcn_make_buffer_rsrc(ws, 0, WS_BYTES, 0x00020000);
#define COMMON_SETUP() \
  lptr xrd[4], wr0[4], wr1[4]; \
  _Pragma("unroll") for (int s = 0; s < 4; ++s) { xrd[s] = lds + (t * 32 + r32) * 128 + (((2 * (s ^ rot) + hh) ^ ((r32 >> 1) & 7)) << 4); \
    wr0[s] = wrd0 + ((q4 >> 1) << 6) + (s ^ rot) * 2048; wr1[s] = wrd0 + ((1 ^ (q4 >> 1)) << 6) + (s ^ rot) * 2048; } \
  const int xwrap = (t >> 1) ? -NSLOT * SLOT : 0; \
  bf16x8 sel; { const short one = ((lane & 15) == ((lane >> 4) & 1)) ? (short)0x3f80 : (short)0; sel = (bf16x8){one, one, one, one, one, one, one, one}; } \
  bf16x8 xa[28]; float rs, nmb
#define GET_TAG() PIN(tagraw); const u64 tag = (((u64)(unsigned)__builtin_amdgcn_readfirstlane((unsigned)(tagraw >> 32)) << 32) | (unsigned)__builtin_amdgcn_readfirstlane((unsigned)tagraw)) + 1

  if (c == 0) {
    unsigned cpf0;
    asm volatile("global_load_dword %0, %1, %2" : "=v"(cpf0) : "v"(min((unsigned)(bi * 16 + (tid & 15)) * 128u, (unsigned)CODE_BYTES - 128u)), "s"(pc) : "memory");
    unsigned tch = 0;
    u64 tagraw = __hip_atomic_load((GAS u64*)(ws + WS_EPOCH), RLX_AGENT);
    if (tid < N_EV) *(LAS unsigned*)(lds + L_CNT + 4 * tid) = 0u;
    BAR();
    const bool isK = j < 7, isV = j >= 7 && j < 14;
    bf16x8 qf[4][2]; u64 tag;
    {
      COMMON_SETUP();
      unsigned ad[4]; bool strad[4] = {false, false, false, false};
      if (isV) { const int rho = r32 >> 2, col0 = (j - 7) * 64 + 4 * ((rho & 4) + 2 * (rho & 1) + ((rho >> 1) & 1)) + (r32 & 3);
#pragma unroll
        for (int cc = 0; cc < 2; ++cc) { const int col = col0 + cc * 32, hd = col / 28, d = col - hd * 28;
          ad[cc] = WS_KV + hd * 262144 + rt * 4096 + 2048 + (hh * 32 + d) * 16; ad[2 + cc] = d >= 24 ? ad[cc] + 64 : 0x80000000u; strad[cc] = d == 24; }
      } else if (isK) {
#pragma unroll
        for (int s = 0; s < 4; ++s) { const int P = j * 8 + 2 * s + hh, hp = P / 7, rr = P - hp * 7, hd = 2 * hp + (rr > 3), sg = rr > 3 ? rr - 4 : rr;
          ad[s] = WS_KV + hd * 262144 + rt * 4096 + sg * 512 + r32 * 16; strad[s] = rr == 3; }
      } else {
#pragma unroll
        for (int s = 0; s < 4; ++s) ad[s] = WS_H + rt * 114688 + (j - 14) * 4096 + s * 1024 + lane * 16;
      }
      PIN(ad[0]); PIN(ad[1]); PIN(ad[2]); PIN(ad[3]);
      asm volatile("s_waitcnt vmcnt(0)" : "+v"(cpf0), "+v"(tch), "+v"(tagraw) :: "memory");
      *(LAS u64*)(lds + L_TAG) = tagraw;
      tag = (((u64)(unsigned)__builtin_amdgcn_readfirstlane((unsigned)(tagraw >> 32)) << 32) | (unsigned)__builtin_amdgcn_readfirstlane((unsigned)tagraw)) + 1;
      P1R r = {}; float cr0[16], cr1[16], cl0, cl1;
      { P1A p; p.xwrap = xwrap; p.ev = ev; WF f[4];
#pragma unroll
        for (int s = 0; s < 4; ++s) { p.x[s] = xrd[s]; p.w0[s] = wr0[s]; p.w1[s] = wr1[s]; }
        const lptr csw = lds + L_CS0 + lane * 16 + t * 4;
#define GS(st) do { r.s2 = MFMA32(xa[st], xa[st], r.s2); PIN(r.s2); r.s1 = MFMA16(sel, xa[st], r.s1); PIN(r.s1); SBAR(); } while (0)
#define MA(VT, st) MM(VT, r.a0, xa[st], cat(f[st & 3].l0, f[st & 3].h0))
#define MB(VT, st) MM(VT, r.a1, xa[st], cat(f[st & 3].l1, f[st & 3].h1))
#define WRD(s) do { wf_read(f[s], p.w0[s] + SLOT, p.w1[s] + SLOT); SBAR(); } while (0)
#define P1_TAIL(VT) do { \
        p1_compute<VT, 0>(xa, f, r, sel, p STA); \
        __builtin_amdgcn_s_setprio(3); \
        _Pragma("unroll") for (int s = 0; s < 4; ++s) xa[24 + s] = ldsb128(p.x[s] + 6 * SLOT); \
        SBAR(); GS(22); WRD(0); GS(23); WRD(1); GS(24); \
        r.c0 = MFMA16(sel, cat(f[0].l0, f[0].h0), r.c0); r.c1 = MFMA16(sel, cat(f[0].l1, f[0].h1), r.c1); PIN(r.c0); PIN(r.c1); SBAR(); \
        GS(25); GS(26); \
        if (lane < 16) { *(LAS float*)csw = r.c0[0]; *(LAS float*)(csw + 256) = r.c0[1]; *(LAS float*)(csw + 512) = r.c1[0]; *(LAS float*)(csw + 768) = r.c1[1]; } \
        post<A_CSX>(ev); SBAR(); \
        GS(27); MA(VT, 22); MB(VT, 22); WRD(2); MA(VT, 23); \
        if (lane < 16) { *(LAS float*)(priv + lane * 4) = r.s1[0]; *(LAS float*)(priv + 64 + lane * 4) = r.s1[1]; } \
        const float sx_ = *(LAS float*)(priv + r32 * 4); SBAR(); \
        MB(VT, 23); WRD(3); post<A_CS0>(ev); MA(VT, 24); \
        const float sq_ = gram_diag(r.s2); SBAR(); \
        MB(VT, 24); \
        pwait<A_CSX>(ev); \
        const f32x4 p0_ = ldsf4(lds + L_CS0 + r32 * 16), p1_ = ldsf4(lds + L_CS0 + 512 + r32 * 16); SBAR(); \
        MA(VT, 25); \
        row_norm(sx_, sq_, rs, nmb); PIN(rs); PIN(nmb); SBAR(); \
        MB(VT, 25); \
        cl0 = sc1(sc1(p0_[0] + p0_[1]) + sc1(p0_[2] + p0_[3])); cl1 = sc1(sc1(p1_[0] + p1_[1]) + sc1(p1_[2] + p1_[3])); \
        *(LAS float*)(lds + L_STAT + t * 256 + lane * 4) = hh ? nmb : rs; post<A_ST>(ev); SBAR();     \
        MA(VT, 26); \
        if constexpr (!VT) { lane_to_regs(priv, r32, hh, cl0, cr0); } SBAR(); \
        MA(VT, 27); \
        if constexpr (!VT) { lane_to_regs(priv + 128, r32, hh, cl1, cr1); } SBAR(); \
        MB(VT, 26); MB(VT, 27); } while (0)
        if (isV) P1_TAIL(true); else P1_TAIL(false); }
      STAMP(1);
      u32x4 o[4];
      if (isV) {
#pragma unroll
        for (int i = 0; i < 4; ++i) { const f32x4 rr = ldsf4(lds + L_STAT + t * 256 + (8 * i + 4 * hh) * 4), mm = ldsf4(lds + L_STAT + t * 256 + 128 + (8 * i + 4 * hh) * 4);
#pragma unroll
          for (int e = 0; e < 2; ++e) {
            const int n = 4 * i + 2 * e;
            o[i >> 1][2 * (i & 1) + e] = cvtpk(sc1(sc1(__builtin_fmaf(mm[2 * e], cl0, r.a0[n])) * rr[2 * e]), sc1(sc1(__builtin_fmaf(mm[2 * e + 1], cl0, r.a0[n + 1])) * rr[2 * e + 1]));
            o[2 + (i >> 1)][2 * (i & 1) + e] = cvtpk(sc1(sc1(__builtin_fmaf(mm[2 * e], cl1, r.a1[n])) * rr[2 * e]), sc1(sc1(__builtin_fmaf(mm[2 * e + 1], cl1, r.a1[n + 1])) * rr[2 * e + 1])); } }
        ST16(o[0], ad[0]); ST16(o[1], ad[0] + 1024); ST16(o[2], ad[1]); ST16(o[3], ad[1] + 1024);
#pragma unroll
        for (int cc = 0; cc < 2; ++cc) { const unsigned e = strad[cc] ? 0x3f803f80u : 0u; const u32x4 pad = {e, e, e, e}; ST16(pad, ad[2 + cc]); ST16(pad, ad[2 + cc] + 1024); }
      } else {
        if (isK) {
          epi_T<false>(r.a0, cr0, rs, nmb, o[0], o[1]);
#pragma unroll
          for (int s = 0; s < 2; ++s) { u32x4 v = o[s]; if (strad[s]) { v[2] = 0x3f803f80u; v[3] = 0x00003f80u; } ST16(v, ad[s]); }
          epi_T<false>(r.a1, cr1, rs, nmb, o[2], o[3]);
#pragma unroll
          for (int s = 2; s < 4; ++s) { u32x4 v = o[s]; if (strad[s]) { v[2] = 0x3f803f80u; v[3] = 0x00003f80u; } ST16(v, ad[s]); }
#pragma unroll
          for (int s = 0; s < 4; ++s) if (strad[s]) { const u32x4 e = {o[s][2], o[s][3], 0x3f803f80u, 0x00003f80u}; ST16(e, ad[s] + 262144); }
        } else {
          epi_T<true>(r.a0, cr0, rs, nmb, o[0], o[1]); epi_T<true>(r.a1, cr1, rs, nmb, o[2], o[3]);
#pragma unroll
          for (int s = 0; s < 4; ++s) ST16(o[s], ad[s]);
        }
      }
      STAMP(13);
      if (j >= 6) {
        pwait<A_CS0>(ev); pwait<L_X6>(ev);
        const unsigned vo = (16 * t + (lane >> 3)) * 3584 + (((lane & 7) ^ (((lane >> 4) & 1) << 2)) << 4);
#pragma unroll
        for (int i = 0; i < 14; ++i) { const int c7 = (i >> 1) + r7 >= 7 ? (i >> 1) + r7 - 7 : (i >> 1) + r7;
          glds16(w1 + (12 + j) * 128 + (c7 * 64 + (i & 1) * 8) * 3584, vo, lds0 + t * 2048 + p1_slot(32 + (i >> 1)) + (i & 1) * 1024); }
        asm volatile("s_waitcnt vmcnt(14)" ::: "memory");
      } else WAIT_VM(0);
      if (cnt_add(lds, C_KV) == 3u && lane == 0) __hip_atomic_store((GAS u64*)(ws + WS_FKV + fme), tag, RLX_AGENT);
      if (cnt_add(lds, C_H) == 7u && lane == 0) __hip_atomic_store((GAS u64*)(ws + WS_FH + fme), tag, RLX_AGENT);
      STAMP(2);
      __builtin_amdgcn_s_setprio(QPRIO);
      f32x16 a0, a1; f32x4 c0;
      const int rho = 4 * g16 + p4, sg = 2 * (rho >> 2) + (rho & 1), half = (rho >> 1) & 1;
      const int g = (j & 1) ? (sg < 3 ? 2 * sg + 1 + half : (half ? 7 : 0)) : 2 * sg + half;
      lptr qb[4];
#pragma unroll
      for (int s = 0; s < 4; ++s) { qb[s] = lds + L_WQ + (8 * hh + q4) * 64 + g * 8 + (s ^ rot) * 1024; PIN(qb[s]); }
      a0 = (f32x16){}; a1 = (f32x16){}; c0 = (f32x4){};
      pwait<L_WQL>(ev);
      s16x4 ql[6], qh[6];
#pragma unroll
      for (int st = 0; st < 4; ++st) { ql[st] = trd(qb[st]); qh[st] = trd(qb[st] + 256); }
#pragma unroll
      for (int st = 0; st < 28; ++st) {
        if (st == QSPLIT) {
          WAIT_VM(0); post<A_FB>(ev); }
        SBAR();
        if (st & 1) { a1 = MFMA32(cat(ql[st % 6], qh[st % 6]), xa[st], a1); PIN(a1); } else { a0 = MFMA32(cat(ql[st % 6], qh[st % 6]), xa[st], a0); PIN(a0); }
        SBAR();
        if (st + 4 < 28) { const int o_ = ((st + 4) >> 2) * 4096; ql[(st + 4) % 6] = trd(qb[st & 3] + o_); qh[(st + 4) % 6] = trd(qb[st & 3] + o_ + 256); }
        SBAR();
        c0 = MFMA16(sel, cat(ql[st % 6], qh[st % 6]), c0); PIN(c0);
      }
#pragma unroll
      for (int i = 0; i < 16; ++i) a0[i] = sc1(a0[i] + a1[i]);
      if (lane < 16) { *(LAS float*)(priv + lane * 4) = c0[0]; *(LAS float*)(priv + 64 + lane * 4) = c0[1]; }
      float cr[16];
#pragma unroll
      for (int i = 0; i < 4; ++i) { const f32x4 q = ldsf4(priv + (8 * i + 4 * hh) * 4); cr[4 * i] = q[0]; cr[4 * i + 1] = q[1]; cr[4 * i + 2] = q[2]; cr[4 * i + 3] = q[3]; }
      epi_T<false>(a0, cr, rs * QSCALE, nmb, o[0], o[1]);
      if (hh) { o[1][2] = 0u; o[1][3] = 0u; }
      *(LAS u32x4*)(lds + L_QX + t * 2048 + lane * 16) = o[0]; *(LAS u32x4*)(lds + L_QX + t * 2048 + 1024 + lane * 16) = o[1];
    }
    post<A_QW>(ev);
    const f32x16 zero = {};
    f32x16 o[4]; u32x4 kv[4][4], pA0 = {}, pA1 = {}, pB0 = {}, pB1 = {};
#pragma unroll
    for (int q = 0; q < 4; ++q) { o[q] = zero; PIN(o[q]); }
    kv[3][2] = (u32x4){}; kv[3][3] = (u32x4){}; PIN(kv[3][2]); PIN(kv[3][3]); PIN(pB0); PIN(pB1);
    STAMP(4);
    { const int c0 = (28 * j) >> 6, c1 = (28 * j + 27) >> 6, sel4 = lane >> 4;
      int jj = sel4 == 0 ? c0 : sel4 == 1 ? c1 : sel4 == 2 ? 7 + c0 : 7 + c1;
      if (j == 0 && (sel4 & 1)) jj = 14 + (sel4 >> 1);
      poll_flags<0>(ws + WS_FKV + ((lane & 15) * 16 + jj) * 128, tag); }
    STAMP(5);
    const unsigned kvb = WS_KV + j * 262144 + t * 65536 + lane * 16;
#pragma unroll
    for (int u = 0; u < 3; ++u)
#pragma unroll
      for (int e = 0; e < 4; ++e) kv[u][e] = LD16(kvb + u * 4096 + e * 1024);
    pwait<A_QW>(ev);
#pragma unroll
    for (int q = 0; q < 4; ++q) { const lptr a = lds + L_QX + ((q + t + 1) & 3) * 2048 + lane * 16; qf[q][0] = ldsb128(a); qf[q][1] = ldsb128(a + 1024); }
    float mref[4]; bool redo;
#pragma unroll
    for (int q = 0; q < 4; ++q) {
      f32x16 s = MFMA32(asfrag(kv[0][0]), qf[q][0], zero); s = MFMA32(asfrag(kv[0][1]), qf[q][1], s);
      mref[q] = set_ref(qf[q][1], tile_max(s) + REF_MARGIN, hh);
    }
    redo = !(__builtin_fmaxf(__builtin_fmaxf(__builtin_fabsf(mref[0]), __builtin_fabsf(mref[1])), __builtin_fmaxf(__builtin_fabsf(mref[2]), __builtin_fabsf(mref[3]))) < REF_LIMIT);
    __builtin_amdgcn_s_setprio(LPRIO);
    f32x16 sA, sB;
    sA = MFMA32(asfrag(kv[0][0]), qf[0][0], zero); sA = MFMA32(asfrag(kv[0][1]), qf[0][1], sA);
    PIN(sA); STAMP(6);
    auto area = [&](int dst, int e) -> lptr { const int n = dst * 3 + e; return lds + (n < 10 ? 8 * SLOT + n * 4096 : (n - 10) * 4096) + lane * 16; };
    auto put_o = [&](const f32x16& v, float m, int q) { const lptr a = area((q + t + 1) & 3, q);
      *(LAS f32x4*)a = (f32x4){v[0], v[1], v[2], v[3]}; *(LAS f32x4*)(a + 1024) = (f32x4){v[4], v[5], v[6], v[7]};
      *(LAS f32x4*)(a + 2048) = (f32x4){v[8], v[9], v[10], v[11]}; *(LAS f32x4*)(a + 3072) = (f32x4){v[12], hh ? m : v[13], v[14], v[15]}; };
#define PUT_O(q) put_o(o[q], mref[q], q)
#define EX4(S, i) do { S[i] = __builtin_amdgcn_exp2f(S[i]); S[i + 1] = __builtin_amdgcn_exp2f(S[i + 1]); S[i + 2] = __builtin_amdgcn_exp2f(S[i + 2]); S[i + 3] = __builtin_amdgcn_exp2f(S[i + 3]); } while (0)
#define UNIT(SC, SN, PC0, PC1, PP0, PP1, KN0, KN1, QN, QP, VV0, VV1, EXTRA) do { \
      SBAR(); if (QN >= 0) { SN = MFMA32(asfrag(KN0), qf[QN & 3][0], zero); PIN(SN); } SBAR(); EX4(SC, 0); PIN(SC); SBAR(); \
      if (QN >= 0) { SN = MFMA32(asfrag(KN1), qf[QN & 3][1], SN); PIN(SN); } SBAR(); EX4(SC, 4); PIN(SC); SBAR(); \
      o[QP] = MFMA32(asfrag(VV0), asfrag(PP0), o[QP]); PIN(o[QP]); SBAR(); EXTRA; EX4(SC, 8); PIN(SC); SBAR(); \
      o[QP] = MFMA32(asfrag(VV1), asfrag(PP1), o[QP]); PIN(o[QP]); SBAR(); EX4(SC, 12); SBAR(); \
      PC0 = (u32x4){cvtpk(SC[0], SC[1]), cvtpk(SC[2], SC[3]), cvtpk(SC[4], SC[5]), cvtpk(SC[6], SC[7])}; \
      PC1 = (u32x4){cvtpk(SC[8], SC[9]), cvtpk(SC[10], SC[11]), cvtpk(SC[12], SC[13]), cvtpk(SC[14], SC[15])}; PIN(PC0); PIN(PC1); SBAR(); } while (0)
#define LDKV(UL, e) kv[UL][e] = __builtin_amdgcn_raw_buffer_load_b128(rsw, kvb + (e) * 1024, nt, LDAUX)
#define KEYTILE(U, UN, UL, LOAD, QLAST, X2, X3) do { const int nt = (kt + (U) + 3) * 4096; \
      UNIT(sA, sB, pA0, pA1, pB0, pB1, kv[U][0], kv[U][1], 1, 3, kv[UL][2], kv[UL][3], if (LOAD) { LDKV(UL, 0); LDKV(UL, 1); }); \
      UNIT(sB, sA, pB0, pB1, pA0, pA1, kv[U][0], kv[U][1], 2, 0, kv[U][2], kv[U][3], if (LOAD) { LDKV(UL, 2); LDKV(UL, 3); }); \
      UNIT(sA, sB, pA0, pA1, pB0, pB1, kv[U][0], kv[U][1], 3, 1, kv[U][2], kv[U][3], X2); \
      UNIT(sB, sA, pB0, pB1, pA0, pA1, kv[UN][0], kv[UN][1], QLAST, 2, kv[U][2], kv[U][3], X3); \
    } while (0)
    for (int kt = 0; kt < 12; kt += 4) {
      LSTAMP(16 + (kt >> 1)); KEYTILE(0, 1, 3, true, 0, , ); KEYTILE(1, 2, 0, true, 0, , ); LSTAMP(17 + (kt >> 1)); KEYTILE(2, 3, 1, true, 0, , ); KEYTILE(3, 0, 2, true, 0, , );
      const float lm = __builtin_fmaxf(__builtin_fmaxf(o[0][12], o[1][12]), __builtin_fmaxf(o[2][12], o[3][12]));
      if (!NOCHECK && __builtin_expect((unsigned)(__ballot(lm > 1.099511627776e12f) >> 32) != 0u, 0)) {
        NRESC;
        o[3] = MFMA32(asfrag(kv[3][2]), asfrag(pB0), o[3]); o[3] = MFMA32(asfrag(kv[3][3]), asfrag(pB1), o[3]); pB0 = (u32x4){}; pB1 = (u32x4){};
#pragma unroll
        for (int q = 0; q < 4; ++q) { const float l = upper_bcast(o[q][12]); redo |= !(l < 1.2980742146337069e33f);
          const float mn = set_ref(qf[q][1], mref[q] + (float)max(__builtin_amdgcn_frexp_expf(l) + (int)REF_MARGIN, 0), hh);
          const int e = (int)(mn - mref[q]); mref[q] = mn; redo |= !(__builtin_fabsf(mn) < REF_LIMIT);
#pragma unroll
          for (int r = 0; r < 16; ++r) o[q][r] = __builtin_ldexpf(o[q][r], -e);
          if (q == 0) {
#pragma unroll
            for (int r = 0; r < 16; ++r) sA[r] -= (float)e; } } }
    }
    { const int kt = 12;
      if (__builtin_expect(__any(redo), 0)) { mref[0] = mref[1] = mref[2] = mref[3] = __builtin_nanf(""); }
      pwait<L_CSBP>(ev);
      LSTAMP(22); KEYTILE(0, 1, 3, true, 0, , ); KEYTILE(1, 2, 0, false, 0, , ); LSTAMP(23); KEYTILE(2, 3, 1, false, 0, , );
      post<A_DONE>(ev); __builtin_amdgcn_s_setprio(3);
      KEYTILE(3, 0, 2, false, -1, PUT_O(0), PUT_O(1)); }
    o[3] = MFMA32(asfrag(kv[3][2]), asfrag(pB0), o[3]); o[3] = MFMA32(asfrag(kv[3][3]), asfrag(pB1), o[3]); PIN(o[3]); SBAR();
    PUT_O(2);
    STAMP(7);
    VSTAMP(28, nresc + (__any(redo) ? 1000 : 0));
    auto combine = [&]() -> bool {
      f32x4 pp[3][4]; float ms[3]; f32x16 of;
#pragma unroll
      for (int e = 0; e < 3; ++e) { const lptr a = area(t, e); pp[e][3] = ldsf4(a + 3072); }
#pragma unroll
      for (int e = 0; e < 3; ++e) { const lptr a = area(t, e);
#pragma unroll
        for (int i = 0; i < 3; ++i) pp[e][i] = ldsf4(a + i * 1024);
        ms[e] = upper_bcast(pp[e][3][1]); }
      const float mx = __builtin_fmaxf(__builtin_fmaxf(mref[3], ms[0]), __builtin_fmaxf(ms[1], ms[2])) + 2.f;
      float f0 = __builtin_amdgcn_exp2f(mref[3] - mx), f1 = __builtin_amdgcn_exp2f(ms[0] - mx), f2 = __builtin_amdgcn_exp2f(ms[1] - mx), f3 = __builtin_amdgcn_exp2f(ms[2] - mx);
      const float l = upper_bcast(__builtin_fmaf(pp[2][3][0], f3, __builtin_fmaf(pp[1][3][0], f2, __builtin_fmaf(pp[0][3][0], f1, o[3][12] * f0))));
      const float inv = __builtin_amdgcn_rcpf(l);
      f0 = sc1(f0 * inv); f1 = sc1(f1 * inv); f2 = sc1(f2 * inv); f3 = sc1(f3 * inv);
#pragma unroll
      for (int r = 0; r < 16; ++r) of[r] = sc1(__builtin_fmaf(pp[2][r >> 2][r & 3], f3, sc1(__builtin_fmaf(pp[1][r >> 2][r & 3], f2, sc1(__builtin_fmaf(pp[0][r >> 2][r & 3], f1, sc1(o[3][r] * f0)))))));
      u32x4 e0, e1;
#pragma unroll
      for (int p = 0; p < 4; ++p) { e0[p] = cvtpk(of[2 * p], of[2 * p + 1]); e1[p] = cvtpk(of[8 + 2 * p], of[9 + 2 * p]); }
      if (hh) { e1[2] = 0u; e1[3] = 0u; }
      const unsigned a = WS_O + rt * 32768 + j * 2048 + lane * 16;
      ST16(e0, a); ST16(e1, a + 1024);
      unsigned ex = 0;
#pragma unroll
      for (int p = 0; p < 4; ++p) ex |= ((e0[p] & 0x7f807f80u) + 0x00800080u) | ((e1[p] & 0x7f807f80u) + 0x00800080u);
      return __any((ex & 0x80008000u) != 0u || !(l < 1.2676506002282294e30f));
    };
    post<A_CMB>(ev); STAMP(9); pwait<A_CMB>(ev);
    if (__builtin_expect(combine(), 0)) *(LAS unsigned*)(lds + L_CNT + 4 * C_BAD) = 1u;
    STAMP(10);
    WAIT_VM(0);
    const unsigned arr = cnt_add(lds, C_O);
    __builtin_amdgcn_s_setprio(0);
    if (arr != 3u) pwait_long<C_O>(ev);
    const bool bad = __builtin_amdgcn_readfirstlane(*(volatile LAS unsigned*)(lds + L_CNT + 4 * C_BAD)) != 0u;
    if (!bad && arr == 3u && lane == 0) __hip_atomic_store((GAS u64*)(ws + WS_FO + fme), tag, RLX_AGENT);
    if (__builtin_expect(bad, 0)) {
#pragma unroll
      for (int q = 0; q < 4; ++q) { set_ref(qf[q][1], 0.f, hh); o[q] = zero; mref[q] = -3.0e38f; }
#pragma nounroll
      for (int kt = 0; kt < 16; ++kt) {
        const u32x4 k0 = LD16(kvb + kt * 4096), k1 = LD16(kvb + kt * 4096 + 1024), v0 = LD16(kvb + kt * 4096 + 2048), v1 = LD16(kvb + kt * 4096 + 3072);
#pragma unroll
        for (int q = 0; q < 4; ++q) {
          f32x16 s = MFMA32(asfrag(k0), qf[q][0], zero); s = MFMA32(asfrag(k1), qf[q][1], s);
          const float mn = __builtin_fmaxf(mref[q], tile_max(s)), al = __builtin_amdgcn_exp2f(mref[q] - mn); mref[q] = mn;
#pragma unroll
          for (int r = 0; r < 16; ++r) { o[q][r] *= al; s[r] = __builtin_amdgcn_exp2f(s[r] - mn); }
          const u32x4 p0 = {cvtpk(s[0], s[1]), cvtpk(s[2], s[3]), cvtpk(s[4], s[5]), cvtpk(s[6], s[7])}, p1 = {cvtpk(s[8], s[9]), cvtpk(s[10], s[11]), cvtpk(s[12], s[13]), cvtpk(s[14], s[15])};
          o[q] = MFMA32(asfrag(v0), asfrag(p0), o[q]); o[q] = MFMA32(asfrag(v1), asfrag(p1), o[q]);
        }
      }
      PUT_O(0); PUT_O(1); PUT_O(2);
      post<A_CMB2>(ev); pwait<A_CMB2>(ev);
      combine();
      WAIT_VM(0);
      if (cnt_add(lds, C_O2) == 3u && lane == 0) __hip_atomic_store((GAS u64*)(ws + WS_FO + fme), tag, RLX_AGENT);
    }
    STAMP(8); CSTAMP(31); STAMP_OUT();
  } else {
    Ld k;
    k.xsrc = x + (rb * 128 + t * 32) * 896; k.xvo0 = (lane >> 3) * 896 + (((lane & 7) ^ (lane >> 4)) << 4); k.xvo1 = k.xvo0 ^ 64u;
    k.xdst = lds0 + t * 4096; k.xdst6 = lds0 + (t < 2 ? 18 * SLOT + t * 4096 : (t - 2) * 4096);
#pragma unroll
    for (int i = 0; i < 7; ++i) k.ck[i] = i + r7 >= 7 ? i + r7 - 7 : i + r7;
    { const int row = 16 * t + (lane >> 3); const unsigned piece = ((lane & 7) ^ (((lane >> 4) & 1) << 2)) << 4;
      k.w0rs = j < 14 ? 2688u : 3584u; k.w0src = j < 14 ? wqkv + 896 + j * 128 : w1 + (j - 14) * 128;
      k.fasrc = w1 + (2 + j) * 128; k.dst = lds0 + t * 2048;
      k.w0vo = row * k.w0rs + piece; k.wfvo = row * 3584 + piece;
      k.wqsrc = wqkv + j * 56; k.wqvo = (16 * t + (lane >> 2)) * 2688 + (lane & 3) * 16; k.qdst = lds0 + L_WQ + t * 1024; k.ev = ev; }
    {
      COMMON_SETUP();
      STAMP(14);
      p1_stream<0>(k, xa, xrd, xwrap STA);
      STAMP(15);
      __builtin_amdgcn_s_setprio(GPRIO);
#pragma unroll
      for (int s = 0; s < 4; ++s) { wr0[s] += L_FA; wr1[s] += L_FA; PIN(wr0[s]); PIN(wr1[s]); }
      WF f[3]; f32x16 a0 = {}, a1 = {}; f32x4 c0 = {}, c1 = {}; float cr[16]; u32x4 o[2];
      pwait<L_FAL>(ev);
      wf_read(f[0], wr0[0], wr1[0]); wf_read(f[1], wr0[1], wr1[1]);
      f_steps<0, 0>(ev, xa, f, a0, a1, c0, c1, sel, wr0, wr1);
      cs_put(lds, L_CSA, 0, t, lane, c0); cs_put(lds, L_CSA, 1, t, lane, c1);
      pwait<A_ST>(ev);
      rs = *(LAS float*)(lds + L_STAT + t * 256 + r32 * 4); nmb = *(LAS float*)(lds + L_STAT + t * 256 + 128 + r32 * 4);
      post<L_CSAP>(ev); pwait<L_CSAP>(ev);
      STAMP(2);
      __builtin_amdgcn_s_setprio(3);
      { const unsigned ad = WS_H + rt * 114688 + (2 + j) * 4096 + lane * 16;
        lane_to_regs(priv, r32, hh, cs_get(lds, L_CSA, 0, r32), cr); epi_T<true>(a0, cr, rs, nmb, o[0], o[1]); ST16(o[0], ad); ST16(o[1], ad + 1024);
        lane_to_regs(priv + 128, r32, hh, cs_get(lds, L_CSA, 1, r32), cr); epi_T<true>(a1, cr, rs, nmb, o[0], o[1]); ST16(o[0], ad + 2048); ST16(o[1], ad + 3072); }
      STAMP(3);
      if (j >= 6) {
#pragma unroll
        for (int s = 0; s < 4; ++s) { wr0[s] += L_FB - L_FA; wr1[s] += L_FB - L_FA; PIN(wr0[s]); PIN(wr1[s]); }
        a0 = (f32x16){}; a1 = (f32x16){}; c0 = (f32x4){}; c1 = (f32x4){};
        pwait<A_FB>(ev);
        wf_read(f[0], wr0[0], wr1[0]); wf_read(f[1], wr0[1], wr1[1]);
        f_steps<1, 0>(ev, xa, f, a0, a1, c0, c1, sel, wr0, wr1);
        cs_put(lds, L_CSB, 0, t, lane, c0); cs_put(lds, L_CSB, 1, t, lane, c1);
        post<L_CSBP>(ev); pwait<L_CSBP>(ev);
        const unsigned ad = WS_H + rt * 114688 + (12 + j) * 4096 + lane * 16;
        lane_to_regs(priv, r32, hh, cs_get(lds, L_CSB, 0, r32), cr); epi_T<true>(a0, cr, rs, nmb, o[0], o[1]); ST16(o[0], ad); ST16(o[1], ad + 1024);
        lane_to_regs(priv + 128, r32, hh, cs_get(lds, L_CSB, 1, r32), cr); epi_T<true>(a1, cr, rs, nmb, o[0], o[1]); ST16(o[0], ad + 2048); ST16(o[1], ad + 3072);
      } else post<L_CSBP>(ev);
    }
    __builtin_amdgcn_s_setprio(0);
    u64 tagraw = *(LAS u64*)(lds + L_TAG);
    GET_TAG();
    WAIT_VM(0);
    if (cnt_add(lds, C_H) == 7u && lane == 0) __hip_atomic_store((GAS u64*)(ws + WS_FH + fme), tag, RLX_AGENT);
    STAMP(4);
    const int cg = bi >> 2;
    if (cg < 7) {
      const int rt0 = 8 * xg + 2 * (bi & 3), rb3 = rt0 >> 2;
      const int ring = ring_of(t);
      const int ct = t >> 1, rtl = t & 1;
      G2 g; g.ev = ev; g.t = t; g.ringdst = lds0 + ring; g.lv = lane * 16;
      g.w2vo = (lane >> 3) * 896 + (((lane & 7) ^ (((lane >> 4) & 1) << 2)) << 4);
      g.w2s = w2 + t * 448 * 896 + cg * 128; g.wos = wo + t * 112 * 896 + cg * 128;
      g.hb0 = ws + WS_H + (rt0 + rtl) * 114688 + t * 28672; g.hb1 = ws + WS_H + (rt0 + (rtl ^ 1)) * 114688 + t * 28672;
      g.br0 = wrd0 + ring + ((ct ^ (q4 >> 1)) << 6); g.br1 = wrd0 + ring + ((ct ^ 1 ^ (q4 >> 1)) << 6);
      g2_ws<0, 12>(g);
      const int fb3 = rb3 * 16 + (lane & 15);
      __builtin_amdgcn_s_setprio(0);
      poll_flags<8>(ws + WS_FH + fb3 * 128, tag);
      __builtin_amdgcn_s_setprio(G2PRIO);
      WAIT_VM(0);
      STAMP(5);
      u32x4 hf[14][2];
#pragma unroll
      for (int s = 0; s < 14; ++s) { ldg_sc1(hf[s][0], g.hb0 + s * 1024, g.lv); ldg_sc1(hf[s][1], g.hb1 + s * 1024, g.lv); }
      f32x16 a00 = {}, a01 = {}, a10 = {}, a11 = {};
      PIN(g.br0); PIN(g.br1);
      { WF f[3]; wf_read(f[0], g.br0, g.br1); wf_read(f[1], g.br0 + 2048, g.br1 + 2048);
        g2_steps<0>(g, hf, f, a00, a01, a10, a11); }
      STAMP(6);
      WAIT_VM(0); post<L_WO>(ev);
      const size_t oo = (size_t)((rt0 + rtl) * 32 + r32) * 896 + cg * 128 + ct * 64 + hh * 16;
      const u32x4 xr0 = *(const GAS u32x4*)(x + oo), xr1 = *(const GAS u32x4*)(x + oo + 32);
      pwait<L_WO>(ev);
      bf16x8 wf[16][2];
#pragma unroll
      for (int g2 = 0; g2 < 2; ++g2) { const lptr o0 = lds + ring_of(2 * ct + g2) + SLOT + (4 * hh + q4) * 128 + pi_std * 8;
#pragma unroll
        for (int e = 0; e < 8; ++e)
#pragma unroll
          for (int cc = 0; cc < 2; ++cc) wf[8 * g2 + e][cc] = trfrag(o0 + ((cc ^ ct ^ (q4 >> 1)) << 6) + ((e >> 1) * 28 + (e & 1) * 16) * 128, 1024); }
      post<L_WOR>(ev); pwait<L_WOR>(ev);
      auto put = [&](const f32x16& a, int e) { const lptr m = lds + ring + e * 4096 + lane * 16;
#pragma unroll
        for (int i = 0; i < 4; ++i) *(LAS f32x4*)(m + i * 1024) = (f32x4){a[4 * i], a[4 * i + 1], a[4 * i + 2], a[4 * i + 3]}; };
      auto add = [&](f32x16& a, int src, int e) { const lptr m = lds + ring_of(src) + e * 4096 + lane * 16;
#pragma unroll
        for (int i = 0; i < 4; ++i) { const f32x4 v = ldsf4(m + i * 1024);
#pragma unroll
          for (int n = 0; n < 4; ++n) a[4 * i + n] = sc1(a[4 * i + n] + v[n]); } };
      put(a11, 3); put(a10, 2); put(a01, 1);
      post<L_RED>(ev); pwait<L_RED>(ev);
      add(a00, t ^ 1, 1); add(a00, t ^ 2, 2); add(a00, t ^ 3, 3);
#pragma unroll
      for (int p = 0; p < 4; ++p) { a00[2 * p] = sc1(a00[2 * p] + bflo(xr0[p])); a00[2 * p + 1] = sc1(a00[2 * p + 1] + bfhi(xr0[p]));
        a00[8 + 2 * p] = sc1(a00[8 + 2 * p] + bflo(xr1[p])); a00[9 + 2 * p] = sc1(a00[9 + 2 * p] + bfhi(xr1[p])); }
      a10 = (f32x16){}; PIN(a00); PIN(a10);
      STAMP(11);
      { const char* fl = ws + WS_FO + (rb3 * 16 + ct * 8 + (lane & 7)) * 128; const unsigned ob = WS_O + (rt0 + rtl) * 32768 + ct * 16384 + lane * 16;
        unsigned ready = 0; u32x4 of[8][2];
#pragma unroll
        for (int h = 0; h < 8 + OLAG; ++h) {
          if (h < 8) {
            if (__builtin_expect(!((ready >> h) & 1u), 0)) ready = poll_bit(fl, tag, h);
            if (h == 7) STAMP(7);
            of[h][0] = LD16(ob + h * 2048); of[h][1] = LD16(ob + h * 2048 + 1024); }
          if (h >= OLAG) {
#pragma unroll
            for (int s = 0; s < 2; ++s) { const bf16x8 o_ = asfrag(of[h - OLAG][s]); a10 = MFMA32(wf[2 * (h - OLAG) + s][1], o_, a10); a00 = MFMA32(wf[2 * (h - OLAG) + s][0], o_, a00); }
            PIN(a10); PIN(a00); }
          SBAR(); } }
      STAMP(9);
      put(a10, 0);
      post<L_FIN>(ev);
      STAMP(10); pwait<L_FIN>(ev);
      add(a00, t ^ 2, 0);
      u32x4 r0, r1;
#pragma unroll
      for (int p = 0; p < 4; ++p) { r0[p] = cvtpk(a00[2 * p], a00[2 * p + 1]); r1[p] = cvtpk(a00[8 + 2 * p], a00[9 + 2 * p]); }
      *(GAS u32x4*)(out + oo) = r0; *(GAS u32x4*)(out + oo + 32) = r1;
      if (b == 0 && tid == 256) __hip_atomic_store((GAS u64*)(ws + WS_EPOCH), tag, RLX_AGENT);
      STAMP(8);
    }
    STAMP_OUT();
  }
}

extern "C" void kernel_launch(void* const* d_in, const int* in_sizes, int n_in, void* d_out, int out_size, void* d_ws, size_t ws_size, hipStream_t stream) {
  fused_block<<<256, 512, 0, stream>>>((const char*)d_in[0], (const char*)d_in[1], (const char*)d_in[2], (const char*)d_in[4], (const char*)d_in[5], (char*)d_out, (char*)d_ws + WS_BASE);
}
```

```cpp
#define METRIC 0
#include <hip/hip_runtime.h>
#include <stdint.h>

typedef short bf16x8 __attribute__((ext_vector_type(8)));
typedef short s16x4 __attribute__((ext_vector_type(4)));
typedef float f32x16 __attribute__((ext_vector_type(16)));
typedef float f32x4 __attribute__((ext_vector_type(4)));
typedef float f32x2 __attribute__((ext_vector_type(2)));
typedef __bf16 bf16x2v __attribute__((ext_vector_type(2)));
typedef unsigned u32x4 __attribute__((ext_vector_type(4)));
typedef unsigned long long u64;
#define LAS __attribute__((address_space(3)))
#define GAS __attribute__((address_space(1)))
#define DI __device__ __forceinline__
#define MFMA32(a, b, c) __builtin_amdgcn_mfma_f32_32x32x16_bf16(a, b, c, 0, 0, 0)
#define MFMA16(a, b, c) __builtin_amdgcn_mfma_f32_16x16x32_bf16(a, b, c, 0, 0, 0)
#define SBAR() __builtin_amdgcn_sched_barrier(0)
#define PIN(x) asm volatile("" : "+v"(x))
#define RLX_AGENT __ATOMIC_RELAXED, __HIP_MEMORY_SCOPE_AGENT
#define WAIT_BAR(N) asm volatile("s_waitcnt vmcnt(" #N ") lgkmcnt(0)\n\ts_barrier" ::: "memory")
#define WAIT_VM(N) asm volatile("s_waitcnt vmcnt(" #N ")" ::: "memory")
#define WAIT_LGKM0() asm volatile("s_waitcnt lgkmcnt(0)" ::: "memory")

constexpr unsigned WS_EPOCH = 0;
constexpr unsigned WS_FKV = 4096, WS_FH = WS_FKV + 32768, WS_FO = WS_FH + 32768;
constexpr unsigned WS_KV = 1u << 20;
constexpr unsigned WS_H = 5u << 20;
constexpr unsigned WS_O = 13u << 20;
constexpr unsigned WS_BYTES = 15u << 20;
constexpr int SLOT = 8192, NSLOT = 19;
constexpr int L_MISC = NSLOT * SLOT;
constexpr int L_STAT = L_MISC;
constexpr int L_CS0 = L_MISC + 2048, L_CSA = L_CS0 + 1024, L_CSB = L_CSA + 1024;
constexpr int L_PRIV = L_MISC + 5120;
constexpr int L_CNT = L_MISC + 7168;
constexpr int L_TAG = L_MISC + 7424;
constexpr int LDS_BYTES = 163840;
constexpr int L_FA = 2 * SLOT, L_WQ = 9 * SLOT, L_FB = 13 * SLOT, L_QX = SLOT;
constexpr int ring_of(int t) { return (t < 2 ? 2 + 3 * t : 7 + 3 * t) * SLOT; }
constexpr int p1_slot(int item) { return (item % NSLOT) * SLOT; }
enum { C_KV, C_H, C_O, C_O2, C_BAD,
       A_RD0, A_CS0 = A_RD0 + 6, A_CSX, A_ST, A_FB, A_QW, A_DONE, A_CMB, A_CMB2,
       L_LD0, L_X6 = L_LD0 + 7, L_FAL, L_WQL, L_CSAP, L_CSBP, L_WO, L_WOR, L_RED, L_FIN, N_EV };

#define WS_BASE 0
#define TOUCH 0
#define ROT7 0
#define MAPA 8
#define G2_SPLIT 14
#define CODE_BYTES 55296
constexpr float QSCALE = 0.18898223650461362f * 1.4426950408889634f;
constexpr float REF_MARGIN = 64.f;
constexpr float REF_LIMIT = 4096.f;
constexpr unsigned SPIN_CAP = 1u << 16;

DI unsigned cvtpk(float lo, float hi) { f32x2 v = {lo, hi}; return __builtin_bit_cast(unsigned, __builtin_convertvector(v, bf16x2v)); }
DI float sc1(float x) { asm("" : "+v"(x)); return x; }
DI float bf16r(float x) { return __uint_as_float(cvtpk(x, 0.f) << 16); }
DI float bflo(unsigned v) { return __uint_as_float(v << 16); }
DI float bfhi(unsigned v) { return __uint_as_float(v & 0xffff0000u); }
DI float swap_add(float v) { auto r = __builtin_amdgcn_permlane32_swap(__float_as_uint(v), __float_as_uint(v), false, false); return __uint_as_float(r[0]) + __uint_as_float(r[1]); }
DI float swap_max(float v) { auto r = __builtin_amdgcn_permlane32_swap(__float_as_uint(v), __float_as_uint(v), false, false); return __builtin_fmaxf(__uint_as_float(r[0]), __uint_as_float(r[1])); }
DI float upper_bcast(float v) { auto r = __builtin_amdgcn_permlane32_swap(__float_as_uint(v), __float_as_uint(v), false, false); return __uint_as_float(r[1]); }
DI void glds16(const void* sbase, unsigned voff, unsigned ldsdst) {
  unsigned keep;
  asm volatile("s_mov_b32 %0, m0\n\ts_mov_b32 m0, %3\n\ts_nop 0\n\tglobal_load_lds_dwordx4 %1, %2\n\ts_mov_b32 m0, %0"
               : "=&s"(keep) : "v"(voff), "s"(sbase), "s"(ldsdst) : "memory");
}
typedef LAS char* lptr;
typedef short v4i16_t __attribute__((ext_vector_type(4)));
DI s16x4 trd(lptr p) { return __builtin_bit_cast(s16x4, __builtin_amdgcn_ds_read_tr16_b64_v4i16((LAS v4i16_t*)p)); }
DI bf16x8 trfrag(lptr p, int second) { s16x4 lo = trd(p), hi = trd(p + second); return __builtin_shufflevector(lo, hi, 0, 1, 2, 3, 4, 5, 6, 7); }
DI bf16x8 ldsb128(lptr p) { return *(LAS bf16x8*)p; }
DI f32x4 ldsf4(lptr p) { return *(LAS f32x4*)p; }
DI bf16x8 asfrag(u32x4 v) { return __builtin_bit_cast(bf16x8, v); }
struct Ev { unsigned base, one; };
template <int E> DI void post(const Ev& e) {
  asm volatile("s_mov_b64 exec, 1\n\tds_add_u32 %0, %1 offset:%2\n\ts_mov_b64 exec, -1" :: "v"(e.base), "v"(e.one), "i"(4 * E) : "memory"); }
template <int E> DI void pwait(const Ev& e) {
  unsigned a;
  asm volatile("1:\n\tds_read_b32 %0, %1 offset:%2\n\ts_waitcnt lgkmcnt(0)\n\tv_cmp_gt_u32 vcc, 4, %0\n\ts_cbranch_vccnz 1b"
               : "=&v"(a) : "v"(e.base), "i"(4 * E) : "vcc", "memory"); }
template <int E> DI void pwait_long(const Ev& e) {
  unsigned a;
  asm volatile("1:\n\ts_sleep 8\n\tds_read_b32 %0, %1 offset:%2\n\ts_waitcnt lgkmcnt(0)\n\tv_cmp_gt_u32 vcc, 4, %0\n\ts_cbranch_vccnz 1b"
               : "=&v"(a) : "v"(e.base), "i"(4 * E) : "vcc", "memory"); }
DI unsigned cnt_add(lptr lds, int c) { unsigned r = 0; if ((threadIdx.x & 63) == 0) r = __hip_atomic_fetch_add((LAS unsigned*)(lds + L_CNT + 4 * c), 1u, __ATOMIC_RELAXED, __HIP_MEMORY_SCOPE_WORKGROUP);
  return __builtin_amdgcn_readfirstlane(r); }
template <int SLEEP> DI void poll_flags(const char* flag, u64 tag) {
  u64 v; unsigned n = SPIN_CAP;
  asm volatile("1:\n\tglobal_load_dwordx2 %0, %2, off sc1\n\ts_sub_u32 %1, %1, 1\n\ts_cbranch_scc1 2f\n\ts_sleep %4\n\ts_waitcnt vmcnt(0)\n\t"
               "v_cmp_ne_u64 vcc, %3, %0\n\ts_cbranch_vccnz 1b\n2:\n\ts_waitcnt vmcnt(0)"
               : "=&v"(v), "+s"(n) : "v"(flag), "s"(tag), "i"(SLEEP) : "vcc", "scc", "memory");
}
DI unsigned poll_bit(const char* flag, u64 tag, int h) {
  u64 v, m; unsigned n = SPIN_CAP;
  asm volatile("1:\n\tglobal_load_dwordx2 %0, %3, off sc1\n\ts_sub_u32 %2, %2, 1\n\ts_cbranch_scc1 2f\n\ts_waitcnt vmcnt(0)\n\t"
               "v_cmp_eq_u64 %1, %4, %0\n\ts_nop 3\n\ts_bitcmp0_b64 %1, %5\n\ts_cbranch_scc1 1b\n2:\n\ts_waitcnt vmcnt(0)"
               : "=&v"(v), "=&s"(m), "+s"(n) : "v"(flag), "s"(tag), "i"(h) : "scc", "memory");
  return (unsigned)m;
}
#define STAMP(i) do {} while (0)
#define CSTAMP(i) do {} while (0)
#define VSTAMP(i, v) do {} while (0)
#define STAMP_OUT() do {} while (0)
#define NRESC
#define STV
#define STA
#define FSTAMP(i) do {} while (0)
#define LSTAMP(i) do {} while (0)
#define BAR() asm volatile("s_waitcnt lgkmcnt(0)\n\ts_barrier" ::: "memory")

#define QPRIO 3
#define LPRIO 2
#define GPRIO 1
#define G2PRIO 0
#define NOCHECK 0
#define QSPLIT 8
#define OLAG 4
#define LOOK 4
DI float pick(u64 m, float hi, float lo) { float r; asm("v_cndmask_b32 %0, %1, %2, %3" : "=v"(r) : "v"(lo), "v"(hi), "s"(m)); return r; }
DI float gram_diag(const f32x16& g) {
  const u64 m1 = 0xaaaaaaaaaaaaaaaaull, m2 = 0xccccccccccccccccull, m4 = 0xf0f0f0f0f0f0f0f0ull, m8 = 0xff00ff00ff00ff00ull, m16 = 0xffff0000ffff0000ull;
  const float b0 = pick(m2, pick(m1, g[3], g[2]), pick(m1, g[1], g[0])), b1 = pick(m2, pick(m1, g[7], g[6]), pick(m1, g[5], g[4]));
  const float b2 = pick(m2, pick(m1, g[11], g[10]), pick(m1, g[9], g[8])), b3 = pick(m2, pick(m1, g[15], g[14]), pick(m1, g[13], g[12]));
  float d = pick(m16, pick(m8, b3, b2), pick(m8, b1, b0));
  asm volatile("s_nop 1" : "+v"(d));
  auto r = __builtin_amdgcn_permlane32_swap(__float_as_uint(d), __float_as_uint(d), false, false);
  return pick(m4, __uint_as_float(r[1]), __uint_as_float(r[0]));
}
template <int C> DI void x_frags(bf16x8 (&xa)[28], const lptr (&xrd)[4], int xwrap) {
#pragma unroll
  for (int s = 0; s < 4; ++s) xa[4 * C + s] = ldsb128(xrd[s] + p1_slot(3 * C) + (C == 6 ? xwrap : 0));
}
DI void row_norm(float s1, float s2, float& rs, float& nmb) {
  const float mean = sc1(s1 * (1.f / 448.f)), var = __builtin_fmaxf(sc1(s2 * (1.f / 448.f)) - mean * mean, 0.f);
  const float sd = bf16r(__builtin_amdgcn_sqrtf(bf16r(bf16r(var) + 1e-5f))); rs = __builtin_amdgcn_rcpf(sd); nmb = -bf16r(mean);
}
struct P1A { lptr x[4], w0[4], w1[4]; int xwrap; Ev ev; };
struct WF { s16x4 l0, h0, l1, h1; };
DI bf16x8 cat(const s16x4& lo, const s16x4& hi) { return __builtin_shufflevector(lo, hi, 0, 1, 2, 3, 4, 5, 6, 7); }
struct P1R { f32x16 a0, a1; f32x4 c0, c1, s1; f32x16 s2; };
#define MM(VT, acc, xf, wf) do { if constexpr (VT) acc = MFMA32(xf, wf, acc); else acc = MFMA32(wf, xf, acc); PIN(acc); SBAR(); } while (0)
template <bool VT, int ST, int C, int S> DI void p1_group(bf16x8 (&xa)[28], WF (&f)[4], P1R& r, const bf16x8& sel, const P1A& p) {
  constexpr int xo = p1_slot(3 * C) - (C >> 1) * 6 * SLOT, wo = p1_slot(3 * C + 2) - (C >> 1) * 6 * SLOT;
  if constexpr (ST >= 0) MM(VT, r.a0, xa[ST], cat(f[ST & 3].l0, f[ST & 3].h0));
  xa[4 * C + S] = ldsb128(p.x[S] + xo); f[S].l0 = trd(p.w0[S] + wo); SBAR();
  if constexpr (ST >= 0) MM(VT, r.a1, xa[ST], cat(f[ST & 3].l1, f[ST & 3].h1));
  f[S].h0 = trd(p.w0[S] + wo + 512); f[S].l1 = trd(p.w1[S] + wo); SBAR();
  if constexpr (ST >= 0) { r.s2 = MFMA32(xa[ST], xa[ST], r.s2); PIN(r.s2); SBAR(); }
  f[S].h1 = trd(p.w1[S] + wo + 512); SBAR();
  if constexpr (ST >= 0) { r.s1 = MFMA16(sel, xa[ST], r.s1); PIN(r.s1);
    if constexpr ((ST & 3) == 0) { r.c0 = MFMA16(sel, cat(f[0].l0, f[0].h0), r.c0); r.c1 = MFMA16(sel, cat(f[0].l1, f[0].h1), r.c1); PIN(r.c0); PIN(r.c1); }
    SBAR(); }
}
template <bool VT, int C> DI void p1_compute(bf16x8 (&xa)[28], WF (&f)[4], P1R& r, const bf16x8& sel, P1A& p STV) {
  if constexpr (C == 0) pwait<L_LD0>(p.ev);
  if (C == 0) STAMP(12);
  FSTAMP(15 + C);
  if constexpr (C < 6) {
    p1_group<VT, 4 * C - 2, C, 0>(xa, f, r, sel, p); p1_group<VT, 4 * C - 1, C, 1>(xa, f, r, sel, p); p1_group<VT, 4 * C, C, 2>(xa, f, r, sel, p);
    const unsigned nxt = *(volatile LAS unsigned*)(uintptr_t)(p.ev.base + 4 * (L_LD0 + C + 1));
    p1_group<VT, 4 * C + 1, C, 3>(xa, f, r, sel, p);
    post<A_RD0 + C>(p.ev);
    if constexpr (C == 1 || C == 3) {
#pragma unroll
      for (int s = 0; s < 4; ++s) { p.x[s] += 6 * SLOT; p.w0[s] += 6 * SLOT; p.w1[s] += 6 * SLOT; PIN(p.x[s]); PIN(p.w0[s]); PIN(p.w1[s]); }
    }
    if constexpr (C == 5) {
#pragma unroll
      for (int s = 0; s < 4; ++s) { p.x[s] += p.xwrap; p.w0[s] -= 12 * SLOT; p.w1[s] -= 12 * SLOT; PIN(p.x[s]); PIN(p.w0[s]); PIN(p.w1[s]); }
    }
    SBAR();
    if (__builtin_expect(__builtin_amdgcn_readfirstlane(nxt) < 4u, 0)) pwait<L_LD0 + C + 1>(p.ev);
    p1_compute<VT, C + 1>(xa, f, r, sel, p STA);
  }
}
struct Ld { const char *xsrc, *w0src, *wqsrc, *fasrc; unsigned xvo0, xvo1, w0vo, wfvo, wqvo, w0rs, dst, xdst, xdst6, qdst; Ev ev; int ck[7]; };
template <int N, int E> DI void ld_piece(const Ld& k) {
  const unsigned dst = k.dst + p1_slot(N) + E * 1024;
  if constexpr (N < 21) {
    constexpr int c = N / 3, q = N % 3;
    if constexpr (q < 2) glds16(k.xsrc + (q * 16 + E * 8) * 896 + k.ck[c] * 128, E ? k.xvo1 : k.xvo0, (c < 6 ? k.xdst + p1_slot(3 * c) : k.xdst6) + (2 * q + E) * 1024);
    else glds16(k.w0src + (k.ck[c] * 64 + E * 8) * k.w0rs, k.w0vo, dst);
  } else if constexpr (N < 28) glds16(k.fasrc + (k.ck[N - 21] * 64 + E * 8) * 3584, k.wfvo, dst);
  else { constexpr int c = 2 * (N - 28) + E;
    glds16(k.wqsrc + k.ck[c < 7 ? c : 0] * (64 * 2688), k.wqvo, k.qdst + c * 4096); }
}
template <int N> DI void p1_stream(const Ld& k, bf16x8 (&xa)[28], const lptr (&xrd)[4], int xwrap STV) {
  if constexpr (N < 32) {
    if constexpr (N == 18 || (N > 19 && (N - 19) % 3 == 0)) { pwait<A_RD0 + (N - 18) / 3>(k.ev); pwait<L_LD0 + (N - 18) / 3 + 1>(k.ev); }
    ld_piece<N, 0>(k); ld_piece<N, 1>(k);
    if constexpr (N == 2) asm volatile("s_barrier" ::: "memory");
    constexpr int M = N - LOOK;
    if constexpr (M >= 2 && M <= 20 && M % 3 == 2) {
      constexpr int C = M / 3;
      asm volatile("s_waitcnt vmcnt(%0)" :: "i"(2 * LOOK) : "memory");
      post<L_LD0 + C>(k.ev);
      if (C == 0) STAMP(12);
      x_frags<C>(xa, xrd, xwrap); SBAR();
      if (C == 6) STAMP(1);
    } else if constexpr (M == 21) post<L_X6>(k.ev);
    else if constexpr (M == 27) { asm volatile("s_waitcnt vmcnt(%0)" :: "i"(2 * LOOK) : "memory"); post<L_FAL>(k.ev); }
    p1_stream<N + 1>(k, xa, xrd, xwrap STA);
  }
}
DI void cs_put(lptr lds, int area, int c, int t, int lane, const f32x4& cs) {
  if (lane < 16) { *(LAS float*)(lds + area + c * 512 + lane * 16 + t * 4) = cs[0]; *(LAS float*)(lds + area + c * 512 + (lane + 16) * 16 + t * 4) = cs[1]; }
}
DI float cs_get(lptr lds, int area, int c, int r32) { const f32x4 p = ldsf4(lds + area + c * 512 + r32 * 16); return sc1(sc1(p[0] + p[1]) + sc1(p[2] + p[3])); }
DI void lane_to_regs(lptr priv, int r32, int hh, float v, float (&o)[16]) {
  *(LAS float*)(priv + r32 * 4) = v;
#pragma unroll
  for (int i = 0; i < 4; ++i) { const f32x4 q = ldsf4(priv + (8 * i + 4 * hh) * 4); o[4 * i] = q[0]; o[4 * i + 1] = q[1]; o[4 * i + 2] = q[2]; o[4 * i + 3] = q[3]; }
}
typedef short s16x2 __attribute__((ext_vector_type(2)));
template <bool RELU> DI void epi_T(const f32x16& acc, const float (&cs)[16], float sc, float nmb, u32x4& o0, u32x4& o1) {
#pragma unroll
  for (int p = 0; p < 8; ++p) {
    const float v0 = sc1(sc1(__builtin_fmaf(cs[2 * p], nmb, acc[2 * p])) * sc), v1 = sc1(sc1(__builtin_fmaf(cs[2 * p + 1], nmb, acc[2 * p + 1])) * sc);
    unsigned w = cvtpk(v0, v1);
    if (RELU) w = __builtin_bit_cast(unsigned, __builtin_elementwise_max(__builtin_bit_cast(s16x2, w), (s16x2){0, 0}));
    if (p < 4) o0[p] = w; else o1[p - 4] = w;
  }
}
#define ST16(v, off) __builtin_amdgcn_raw_buffer_store_b128(v, rsw, off, 0, 16)
#define ABL_NOKV 0
#define LDAUX 16
#define LD16(off) __builtin_amdgcn_raw_buffer_load_b128(rsw, off, 0, LDAUX)

DI float set_ref(bf16x8& q1, float m, int hh) {
  const unsigned w = cvtpk(-__builtin_rintf(m), 0.f);
  u32x4 v = __builtin_bit_cast(u32x4, q1); v[2] = hh ? w : v[2]; q1 = __builtin_bit_cast(bf16x8, v);
  return -__uint_as_float(w << 16);
}
DI float tile_max(const f32x16& s) {
  float a = __builtin_fmaxf(__builtin_fmaxf(s[0], s[1]), s[2]), b = __builtin_fmaxf(__builtin_fmaxf(s[3], s[4]), s[5]);
#pragma unroll
  for (int r = 6; r < 14; r += 4) { a = __builtin_fmaxf(__builtin_fmaxf(a, s[r]), s[r + 1]); b = __builtin_fmaxf(__builtin_fmaxf(b, s[r + 2]), s[r + 3]); }
  a = __builtin_fmaxf(__builtin_fmaxf(a, s[14]), s[15]);
  return swap_max(__builtin_fmaxf(a, b));
}

template <int P, int ST> DI void f_steps(const Ev& ev, const bf16x8 (&xa)[28], WF (&f)[3], f32x16& a0, f32x16& a1, f32x4& c0, f32x4& c1, const bf16x8& sel, const lptr (&w0)[4], const lptr (&w1)[4]) {
  if constexpr (ST < 28) {
    constexpr int N = ST + 2, off = p1_slot((P ? 32 : 21) + ((N < 28 ? N : 0) >> 2)) - (P ? L_FB : L_FA);
    SBAR(); MM(false, a0, xa[ST], cat(f[ST % 3].l0, f[ST % 3].h0));
    if constexpr (N < 28) { f[N % 3].l0 = trd(w0[N & 3] + off); f[N % 3].h0 = trd(w0[N & 3] + off + 512); SBAR(); }
    MM(false, a1, xa[ST], cat(f[ST % 3].l1, f[ST % 3].h1));
    if constexpr (N < 28) { f[N % 3].l1 = trd(w1[N & 3] + off); f[N % 3].h1 = trd(w1[N & 3] + off + 512); SBAR(); }
    if constexpr ((ST & 3) == 0) { c0 = MFMA16(sel, cat(f[ST % 3].l0, f[ST % 3].h0), c0); c1 = MFMA16(sel, cat(f[ST % 3].l1, f[ST % 3].h1), c1); PIN(c0); PIN(c1); }
    if constexpr (P == 0 && ST == 6) { WAIT_VM(0); post<L_WQL>(ev); }
    f_steps<P, ST + 1>(ev, xa, f, a0, a1, c0, c1, sel, w0, w1);
  }
}
DI void wf_read(WF& f, lptr p0, lptr p1) { f.l0 = trd(p0); f.h0 = trd(p0 + 512); f.l1 = trd(p1); f.h1 = trd(p1 + 512); }

constexpr int g2_issued(int st) { int n = 28; for (int s = 0; s < st; ++s) { if (s < 14) n += 2; if (s < 24) n += 2; } return n; }
constexpr int g2_pos_h(int s) { return s < 14 ? 2 * s + 1 : g2_issued(s - 14) + 1; }
constexpr int g2_pos_w(int s) { return s < 12 ? -1 : g2_issued(s - 12) + (s - 12 < 14 ? 2 : 0) + 1; }
constexpr int g2_wait(int st) { const int a = g2_pos_h(st), b = st + 2 < 28 ? g2_pos_w(st + 2) : -1; const int n = g2_issued(st) - 1 - (a > b ? a : b); return n > 63 ? 63 : n; }
DI void ldg_sc1(u32x4& d, const char* sbase, unsigned voff) { asm volatile("global_load_dwordx4 %0, %1, %2 sc1" : "=v"(d) : "v"(voff), "s"(sbase) : "memory"); }
struct G2 { const char *hb0, *hb1, *w2s, *wos; unsigned lv, w2vo, ringdst; Ev ev; int t; lptr br0, br1; };
template <int ST, int E> DI void g2_w(const G2& g) {
  const unsigned dst = g.ringdst + (ST % 12) * 2048 + E * 1024;
  if constexpr (ST < 28) glds16(g.w2s + (ST * 16 + E * 8) * 896, g.w2vo, dst);
  else if constexpr (ST < 35) glds16(g.wos + ((ST - 28) * 16 + E * 8) * 896, g.w2vo, dst);
  else glds16(g.wos + (g.t == 3 ? 104 : 112 + E * 8) * 896, g.w2vo, dst);
}
template <int A, int B> DI void g2_ws(const G2& g) { if constexpr (A < B) { g2_w<A, 0>(g); g2_w<A, 1>(g); g2_ws<A + 1, B>(g); } }
template <int ST> DI void g2_steps(const G2& g, u32x4 (&hf)[14][2], WF (&f)[3], f32x16& a00, f32x16& a01, f32x16& a10, f32x16& a11) {
  if constexpr (ST < 28) {
    if constexpr (ST == G2_SPLIT) { __builtin_amdgcn_s_setprio(0); pwait_long<A_DONE>(g.ev); __builtin_amdgcn_s_setprio(GPRIO); }
    asm volatile("s_waitcnt vmcnt(%2)" : "+v"(hf[ST % 14][0]), "+v"(hf[ST % 14][1]) : "i"(g2_wait(ST)) : "memory");
    constexpr int N = ST + 2, off = (N % 12) * 2048;
    const bf16x8 h0 = asfrag(hf[ST % 14][0]), h1 = asfrag(hf[ST % 14][1]), w0 = cat(f[ST % 3].l0, f[ST % 3].h0), w1 = cat(f[ST % 3].l1, f[ST % 3].h1);
    SBAR(); MM(false, a00, h0, w0);
    if constexpr (N < 28) { f[N % 3].l0 = trd(g.br0 + off); f[N % 3].h0 = trd(g.br0 + off + 512); SBAR(); }
    MM(false, a01, h1, w0);
    if constexpr (N < 28) { f[N % 3].l1 = trd(g.br1 + off); f[N % 3].h1 = trd(g.br1 + off + 512); SBAR(); }
    MM(false, a10, h0, w1);
    MM(false, a11, h1, w1);
    if constexpr (ST < 14) { ldg_sc1(hf[ST][0], g.hb0 + (ST + 14) * 1024, g.lv); ldg_sc1(hf[ST][1], g.hb1 + (ST + 14) * 1024, g.lv); }
    if constexpr (ST < 24) { g2_w<ST + 12, 0>(g); g2_w<ST + 12, 1>(g); }
    g2_steps<ST + 1>(g, hf, f, a00, a01, a10, a11);
  }
}

__global__ void __launch_bounds__(512, 2) fused_block(const char* x, const char* wqkv, const char* wo, const char* w1, const char* w2, char* out, char* ws) {
  asm volatile("" : "+s"(x), "+s"(wqkv), "+s"(wo), "+s"(w1), "+s"(w2), "+s"(out), "+s"(ws));
  __shared__ __attribute__((aligned(16))) char smem[LDS_BYTES];
  u64 pc; asm volatile("s_getpc_b64 %0" : "=s"(pc));
  const lptr lds = (lptr)smem; const unsigned lds0 = (unsigned)(uintptr_t)lds;
  const int tid = threadIdx.x, lane = tid & 63, r32 = lane & 31, hh = lane >> 5;
  const int w = __builtin_amdgcn_readfirstlane(tid >> 6), t = w & 3, c = w >> 2;
  const int b = blockIdx.x, xg = b & 7, bi = b >> 3;
  const int j = (xg & 3) * 4 + (bi >> 3), rb = (xg >> 2) * 8 + (bi & 7);
  const int fme = (rb * 16 + j) * 128, rt = rb * 4 + t;
  STAMP(0); CSTAMP(30); VSTAMP(29, (unsigned)__builtin_amdgcn_s_getreg(20 | (3 << 11)) & 0xfu);
  const u64 rt0_ = __builtin_amdgcn_s_memrealtime(), c0_ = __builtin_amdgcn_s_memtime();
  const int rot = t, r7 = ROT7 ? (rb + 2 * j) % 7 : 0;
  const int q4 = (lane & 15) >> 2, p4 = lane & 3, g16 = (lane >> 4) & 1;
  const int pi_std = 4 * g16 + 2 * (p4 & 1) + (p4 >> 1);
  const lptr wrd0 = lds + (8 * hh + q4) * 128 + pi_std * 8;
  const lptr priv = lds + L_PRIV + w * 256;
  Ev ev = {lds0 + L_CNT, 1u}; PIN(ev.base); PIN(ev.one);
  const __amdgpu_buffer_rsrc_t rsw = __builtin_amdgcn_make_buffer_rsrc(ws, 0, WS_BYTES, 0x00020000);
#define COMMON_SETUP() \
  lptr xrd[4], wr0[4], wr1[4]; \
  _Pragma("unroll") for (int s = 0; s < 4; ++s) { xrd[s] = lds + (t * 32 + r32) * 128 + (((2 * (s ^ rot) + hh) ^ ((r32 >> 1) & 7)) << 4); \
    wr0[s] = wrd0 + ((q4 >> 1) << 6) + (s ^ rot) * 2048; wr1[s] = wrd0 + ((1 ^ (q4 >> 1)) << 6) + (s ^ rot) * 2048; } \
  const int xwrap = (t >> 1) ? -NSLOT * SLOT : 0; \
  bf16x8 sel; { const short one = ((lane & 15) == ((lane >> 4) & 1)) ? (short)0x3f80 : (short)0; sel = (bf16x8){one, one, one, one, one, one, one, one}; } \
  bf16x8 xa[28]; float rs, nmb
#define GET_TAG() PIN(tagraw); const u64 tag = (((u64)(unsigned)__builtin_amdgcn_readfirstlane((unsigned)(tagraw >> 32)) << 32) | (unsigned)__builtin_amdgcn_readfirstlane((unsigned)tagraw)) + 1

  if (c == 0) {
    unsigned cpf0;
    asm volatile("global_load_dword %0, %1, %2" : "=v"(cpf0) : "v"(min((unsigned)(bi * 16 + (tid & 15)) * 128u, (unsigned)CODE_BYTES - 128u)), "s"(pc) : "memory");
    unsigned tch = 0;
    u64 tagraw = __hip_atomic_load((GAS u64*)(ws + WS_EPOCH), RLX_AGENT);
    if (tid < N_EV) *(LAS unsigned*)(lds + L_CNT + 4 * tid) = 0u;
    BAR();
    const bool isK = j < 7, isV = j >= 7 && j < 14;
    bf16x8 qf[4][2]; u64 tag;
    {
      COMMON_SETUP();
      unsigned ad[4]; bool strad[4] = {false, false, false, false};
      if (isV) { const int rho = r32 >> 2, col0 = (j - 7) * 64 + 4 * ((rho & 4) + 2 * (rho & 1) + ((rho >> 1) & 1)) + (r32 & 3);
#pragma unroll
        for (int cc = 0; cc < 2; ++cc) { const int col = col0 + cc * 32, hd = col / 28, d = col - hd * 28;
          ad[cc] = WS_KV + hd * 262144 + rt * 4096 + 2048 + (hh * 32 + d) * 16; ad[2 + cc] = d >= 24 ? ad[cc] + 64 : 0x80000000u; strad[cc] = d == 24; }
      } else if (isK) {
#pragma unroll
        for (int s = 0; s < 4; ++s) { const int P = j * 8 + 2 * s + hh, hp = P / 7, rr = P - hp * 7, hd = 2 * hp + (rr > 3), sg = rr > 3 ? rr - 4 : rr;
          ad[s] = WS_KV + hd * 262144 + rt * 4096 + sg * 512 + r32 * 16; strad[s] = rr == 3; }
      } else {
#pragma unroll
        for (int s = 0; s < 4; ++s) ad[s] = WS_H + rt * 114688 + (j - 14) * 4096 + s * 1024 + lane * 16;
      }
      PIN(ad[0]); PIN(ad[1]); PIN(ad[2]); PIN(ad[3]);
      asm volatile("s_waitcnt vmcnt(0)" : "+v"(cpf0), "+v"(tch), "+v"(tagraw) :: "memory");
      *(LAS u64*)(lds + L_TAG) = tagraw;
      tag = (((u64)(unsigned)__builtin_amdgcn_readfirstlane((unsigned)(tagraw >> 32)) << 32) | (unsigned)__builtin_amdgcn_readfirstlane((unsigned)tagraw)) + 1;
      P1R r = {}; float cr0[16], cr1[16], cl0, cl1;
      { P1A p; p.xwrap = xwrap; p.ev = ev; WF f[4];
#pragma unroll
        for (int s = 0; s < 4; ++s) { p.x[s] = xrd[s]; p.w0[s] = wr0[s]; p.w1[s] = wr1[s]; }
        const lptr csw = lds + L_CS0 + lane * 16 + t * 4;
#define GS(st) do { r.s2 = MFMA32(xa[st], xa[st], r.s2); PIN(r.s2); r.s1 = MFMA16(sel, xa[st], r.s1); PIN(r.s1); SBAR(); } while (0)
#define MA(VT, st) MM(VT, r.a0, xa[st], cat(f[st & 3].l0, f[st & 3].h0))
#define MB(VT, st) MM(VT, r.a1, xa[st], cat(f[st & 3].l1, f[st & 3].h1))
#define WRD(s) do { wf_read(f[s], p.w0[s] + SLOT, p.w1[s] + SLOT); SBAR(); } while (0)
#define P1_TAIL(VT) do { \
        p1_compute<VT, 0>(xa, f, r, sel, p STA); \
        __builtin_amdgcn_s_setprio(3); \
        _Pragma("unroll") for (int s = 0; s < 4; ++s) xa[24 + s] = ldsb128(p.x[s] + 6 * SLOT); \
        SBAR(); GS(22); WRD(0); GS(23); WRD(1); GS(24); \
        r.c0 = MFMA16(sel, cat(f[0].l0, f[0].h0), r.c0); r.c1 = MFMA16(sel, cat(f[0].l1, f[0].h1), r.c1); PIN(r.c0); PIN(r.c1); SBAR(); \
        GS(25); GS(26); \
        if (lane < 16) { *(LAS float*)csw = r.c0[0]; *(LAS float*)(csw + 256) = r.c0[1]; *(LAS float*)(csw + 512) = r.c1[0]; *(LAS float*)(csw + 768) = r.c1[1]; } \
        post<A_CSX>(ev); SBAR(); \
        GS(27); MA(VT, 22); MB(VT, 22); WRD(2); MA(VT, 23); \
        if (lane < 16) { *(LAS float*)(priv + lane * 4) = r.s1[0]; *(LAS float*)(priv + 64 + lane * 4) = r.s1[1]; } \
        const float sx_ = *(LAS float*)(priv + r32 * 4); SBAR(); \
        MB(VT, 23); WRD(3); post<A_CS0>(ev); MA(VT, 24); \
        const float sq_ = gram_diag(r.s2); SBAR(); \
        MB(VT, 24); \
        pwait<A_CSX>(ev); \
        const f32x4 p0_ = ldsf4(lds + L_CS0 + r32 * 16), p1_ = ldsf4(lds + L_CS0 + 512 + r32 * 16); SBAR(); \
        MA(VT, 25); \
        row_norm(sx_, sq_, rs, nmb); PIN(rs); PIN(nmb); SBAR(); \
        MB(VT, 25); \
        cl0 = sc1(sc1(p0_[0] + p0_[1]) + sc1(p0_[2] + p0_[3])); cl1 = sc1(sc1(p1_[0] + p1_[1]) + sc1(p1_[2] + p1_[3])); \
        *(LAS float*)(lds + L_STAT + t * 256 + lane * 4) = hh ? nmb : rs; post<A_ST>(ev); SBAR();     \
        MA(VT, 26); \
        if constexpr (!VT) { lane_to_regs(priv, r32, hh, cl0, cr0); } SBAR(); \
        MA(VT, 27); \
        if constexpr (!VT) { lane_to_regs(priv + 128, r32, hh, cl1, cr1); } SBAR(); \
        MB(VT, 26); MB(VT, 27); } while (0)
        if (isV) P1_TAIL(true); else P1_TAIL(false); }
      STAMP(1);
      u32x4 o[4];
      if (isV) {
#pragma unroll
        for (int i = 0; i < 4; ++i) { const f32x4 rr = ldsf4(lds + L_STAT + t * 256 + (8 * i + 4 * hh) * 4), mm = ldsf4(lds + L_STAT + t * 256 + 128 + (8 * i + 4 * hh) * 4);
#pragma unroll
          for (int e = 0; e < 2; ++e) {
            const int n = 4 * i + 2 * e;
            o[i >> 1][2 * (i & 1) + e] = cvtpk(sc1(sc1(__builtin_fmaf(mm[2 * e], cl0, r.a0[n])) * rr[2 * e]), sc1(sc1(__builtin_fmaf(mm[2 * e + 1], cl0, r.a0[n + 1])) * rr[2 * e + 1]));
            o[2 + (i >> 1)][2 * (i & 1) + e] = cvtpk(sc1(sc1(__builtin_fmaf(mm[2 * e], cl1, r.a1[n])) * rr[2 * e]), sc1(sc1(__builtin_fmaf(mm[2 * e + 1], cl1, r.a1[n + 1])) * rr[2 * e + 1])); } }
        ST16(o[0], ad[0]); ST16(o[1], ad[0] + 1024); ST16(o[2], ad[1]); ST16(o[3], ad[1] + 1024);
#pragma unroll
        for (int cc = 0; cc < 2; ++cc) { const unsigned e = strad[cc] ? 0x3f803f80u : 0u; const u32x4 pad = {e, e, e, e}; ST16(pad, ad[2 + cc]); ST16(pad, ad[2 + cc] + 1024); }
      } else {
        if (isK) {
          epi_T<false>(r.a0, cr0, rs, nmb, o[0], o[1]);
#pragma unroll
          for (int s = 0; s < 2; ++s) { u32x4 v = o[s]; if (strad[s]) { v[2] = 0x3f803f80u; v[3] = 0x00003f80u; } ST16(v, ad[s]); }
          epi_T<false>(r.a1, cr1, rs, nmb, o[2], o[3]);
#pragma unroll
          for (int s = 2; s < 4; ++s) { u32x4 v = o[s]; if (strad[s]) { v[2] = 0x3f803f80u; v[3] = 0x00003f80u; } ST16(v, ad[s]); }
#pragma unroll
          for (int s = 0; s < 4; ++s) if (strad[s]) { const u32x4 e = {o[s][2], o[s][3], 0x3f803f80u, 0x00003f80u}; ST16(e, ad[s] + 262144); }
        } else {
          epi_T<true>(r.a0, cr0, rs, nmb, o[0], o[1]); epi_T<true>(r.a1, cr1, rs, nmb, o[2], o[3]);
#pragma unroll
          for (int s = 0; s < 4; ++s) ST16(o[s], ad[s]);
        }
      }
      STAMP(13);
      if (j >= 6) {
        pwait<A_CS0>(ev); pwait<L_X6>(ev);
        const unsigned vo = (16 * t + (lane >> 3)) * 3584 + (((lane & 7) ^ (((lane >> 4) & 1) << 2)) << 4);
#pragma unroll
        for (int i = 0; i < 14; ++i) { const int c7 = (i >> 1) + r7 >= 7 ? (i >> 1) + r7 - 7 : (i >> 1) + r7;
          glds16(w1 + (12 + j) * 128 + (c7 * 64 + (i & 1) * 8) * 3584, vo, lds0 + t * 2048 + p1_slot(32 + (i >> 1)) + (i & 1) * 1024); }
        asm volatile("s_waitcnt vmcnt(14)" ::: "memory");
      } else WAIT_VM(0);
      if (cnt_add(lds, C_KV) == 3u && lane == 0) __hip_atomic_store((GAS u64*)(ws + WS_FKV + fme), tag, RLX_AGENT);
      if (cnt_add(lds, C_H) == 7u && lane == 0) __hip_atomic_store((GAS u64*)(ws + WS_FH + fme), tag, RLX_AGENT);
      STAMP(2);
      __builtin_amdgcn_s_setprio(QPRIO);
      f32x16 a0, a1; f32x4 c0;
      const int rho = 4 * g16 + p4, sg = 2 * (rho >> 2) + (rho & 1), half = (rho >> 1) & 1;
      const int g = (j & 1) ? (sg < 3 ? 2 * sg + 1 + half : (half ? 7 : 0)) : 2 * sg + half;
      lptr qb[4];
#pragma unroll
      for (int s = 0; s < 4; ++s) { qb[s] = lds + L_WQ + (8 * hh + q4) * 64 + g * 8 + (s ^ rot) * 1024; PIN(qb[s]); }
      a0 = (f32x16){}; a1 = (f32x16){}; c0 = (f32x4){};
      pwait<L_WQL>(ev);
      s16x4 ql[6], qh[6];
#pragma unroll
      for (int st = 0; st < 4; ++st) { ql[st] = trd(qb[st]); qh[st] = trd(qb[st] + 256); }
#pragma unroll
      for (int st = 0; st < 28; ++st) {
        if (st == QSPLIT) {
          WAIT_VM(0); post<A_FB>(ev); }
        SBAR();
        if (st & 1) { a1 = MFMA32(cat(ql[st % 6], qh[st % 6]), xa[st], a1); PIN(a1); } else { a0 = MFMA32(cat(ql[st % 6], qh[st % 6]), xa[st], a0); PIN(a0); }
        SBAR();
        if (st + 4 < 28) { const int o_ = ((st + 4) >> 2) * 4096; ql[(st + 4) % 6] = trd(qb[st & 3] + o_); qh[(st + 4) % 6] = trd(qb[st & 3] + o_ + 256); }
        SBAR();
        c0 = MFMA16(sel, cat(ql[st % 6], qh[st % 6]), c0); PIN(c0);
      }
#pragma unroll
      for (int i = 0; i < 16; ++i) a0[i] = sc1(a0[i] + a1[i]);
      if (lane < 16) { *(LAS float*)(priv + lane * 4) = c0[0]; *(LAS float*)(priv + 64 + lane * 4) = c0[1]; }
      float cr[16];
#pragma unroll
      for (int i = 0; i < 4; ++i) { const f32x4 q = ldsf4(priv + (8 * i + 4 * hh) * 4); cr[4 * i] = q[0]; cr[4 * i + 1] = q[1]; cr[4 * i + 2] = q[2]; cr[4 * i + 3] = q[3]; }
      epi_T<false>(a0, cr, rs * QSCALE, nmb, o[0], o[1]);
      if (hh) { o[1][2] = 0u; o[1][3] = 0u; }
      *(LAS u32x4*)(lds + L_QX + t * 2048 + lane * 16) = o[0]; *(LAS u32x4*)(lds + L_QX + t * 2048 + 1024 + lane * 16) = o[1];
    }
    post<A_QW>(ev);
    const f32x16 zero = {};
    f32x16 o[4]; u32x4 kv[4][4], pA0 = {}, pA1 = {}, pB0 = {}, pB1 = {};
#pragma unroll
    for (int q = 0; q < 4; ++q) { o[q] = zero; PIN(o[q]); }
    kv[3][2] = (u32x4){}; kv[3][3] = (u32x4){}; PIN(kv[3][2]); PIN(kv[3][3]); PIN(pB0); PIN(pB1);
    STAMP(4);
    { const int c0 = (28 * j) >> 6, c1 = (28 * j + 27) >> 6, sel4 = lane >> 4;
      int jj = sel4 == 0 ? c0 : sel4 == 1 ? c1 : sel4 == 2 ? 7 + c0 : 7 + c1;
      if (j == 0 && (sel4 & 1)) jj = 14 + (sel4 >> 1);
      poll_flags<0>(ws + WS_FKV + ((lane & 15) * 16 + jj) * 128, tag); }
    STAMP(5);
    const unsigned kvb = WS_KV + j * 262144 + t * 65536 + lane * 16;
#pragma unroll
    for (int u = 0; u < 3; ++u)
#pragma unroll
      for (int e = 0; e < 4; ++e) kv[u][e] = LD16(kvb + u * 4096 + e * 1024);
    pwait<A_QW>(ev);
#pragma unroll
    for (int q = 0; q < 4; ++q) { const lptr a = lds + L_QX + ((q + t + 1) & 3) * 2048 + lane * 16; qf[q][0] = ldsb128(a); qf[q][1] = ldsb128(a + 1024); }
    float mref[4]; bool redo;
#pragma unroll
    for (int q = 0; q < 4; ++q) {
      f32x16 s = MFMA32(asfrag(kv[0][0]), qf[q][0], zero); s = MFMA32(asfrag(kv[0][1]), qf[q][1], s);
      mref[q] = set_ref(qf[q][1], tile_max(s) + REF_MARGIN, hh);
    }
    redo = !(__builtin_fmaxf(__builtin_fmaxf(__builtin_fabsf(mref[0]), __builtin_fabsf(mref[1])), __builtin_fmaxf(__builtin_fabsf(mref[2]), __builtin_fabsf(mref[3]))) < REF_LIMIT);
    __builtin_amdgcn_s_setprio(LPRIO);
    f32x16 sA, sB;
    sA = MFMA32(asfrag(kv[0][0]), qf[0][0], zero); sA = MFMA32(asfrag(kv[0][1]), qf[0][1], sA);
    PIN(sA); STAMP(6);
    auto area = [&](int dst, int e) -> lptr { const int n = dst * 3 + e; return lds + (n < 10 ? 8 * SLOT + n * 4096 : (n - 10) * 4096) + lane * 16; };
    auto put_o = [&](const f32x16& v, float m, int q) { const lptr a = area((q + t + 1) & 3, q);
      *(LAS f32x4*)a = (f32x4){v[0], v[1], v[2], v[3]}; *(LAS f32x4*)(a + 1024) = (f32x4){v[4], v[5], v[6], v[7]};
      *(LAS f32x4*)(a + 2048) = (f32x4){v[8], v[9], v[10], v[11]}; *(LAS f32x4*)(a + 3072) = (f32x4){v[12], hh ? m : v[13], v[14], v[15]}; };
#define PUT_O(q) put_o(o[q], mref[q], q)
#define EX4(S, i) do { S[i] = __builtin_amdgcn_exp2f(S[i]); S[i + 1] = __builtin_amdgcn_exp2f(S[i + 1]); S[i + 2] = __builtin_amdgcn_exp2f(S[i + 2]); S[i + 3] = __builtin_amdgcn_exp2f(S[i + 3]); } while (0)
#define UNIT(SC, SN, PC0, PC1, PP0, PP1, KN0, KN1, QN, QP, VV0, VV1, EXTRA) do { \
      SBAR(); if (QN >= 0) { SN = MFMA32(asfrag(KN0), qf[QN & 3][0], zero); PIN(SN); } SBAR(); EX4(SC, 0); PIN(SC); SBAR(); \
      if (QN >= 0) { SN = MFMA32(asfrag(KN1), qf[QN & 3][1], SN); PIN(SN); } SBAR(); EX4(SC, 4); PIN(SC); SBAR(); \
      o[QP] = MFMA32(asfrag(VV0), asfrag(PP0), o[QP]); PIN(o[QP]); SBAR(); EXTRA; EX4(SC, 8); PIN(SC); SBAR(); \
      o[QP] = MFMA32(asfrag(VV1), asfrag(PP1), o[QP]); PIN(o[QP]); SBAR(); EX4(SC, 12); SBAR(); \
      PC0 = (u32x4){cvtpk(SC[0], SC[1]), cvtpk(SC[2], SC[3]), cvtpk(SC[4], SC[5]), cvtpk(SC[6], SC[7])}; \
      PC1 = (u32x4){cvtpk(SC[8], SC[9]), cvtpk(SC[10], SC[11]), cvtpk(SC[12], SC[13]), cvtpk(SC[14], SC[15])}; PIN(PC0); PIN(PC1); SBAR(); } while (0)
#define LDKV(UL, e) kv[UL][e] = __builtin_amdgcn_raw_buffer_load_b128(rsw, kvb + (e) * 1024, nt, LDAUX)
#define KEYTILE(U, UN, UL, LOAD, QLAST, X2, X3) do { const int nt = (kt + (U) + 3) * 4096; \
      UNIT(sA, sB, pA0, pA1, pB0, pB1, kv[U][0], kv[U][1], 1, 3, kv[UL][2], kv[UL][3], if (LOAD) { LDKV(UL, 0); LDKV(UL, 1); }); \
      UNIT(sB, sA, pB0, pB1, pA0, pA1, kv[U][0], kv[U][1], 2, 0, kv[U][2], kv[U][3], if (LOAD) { LDKV(UL, 2); LDKV(UL, 3); }); \
      UNIT(sA, sB, pA0, pA1, pB0, pB1, kv[U][0], kv[U][1], 3, 1, kv[U][2], kv[U][3], X2); \
      UNIT(sB, sA, pB0, pB1, pA0, pA1, kv[UN][0], kv[UN][1], QLAST, 2, kv[U][2], kv[U][3], X3); \
    } while (0)
    for (int kt = 0; kt < 12; kt += 4) {
      LSTAMP(16 + (kt >> 1)); KEYTILE(0, 1, 3, true, 0, , ); KEYTILE(1, 2, 0, true, 0, , ); LSTAMP(17 + (kt >> 1)); KEYTILE(2, 3, 1, true, 0, , ); KEYTILE(3, 0, 2, true, 0, , );
      const float lm = __builtin_fmaxf(__builtin_fmaxf(o[0][12], o[1][12]), __builtin_fmaxf(o[2][12], o[3][12]));
      if (!NOCHECK && __builtin_expect((unsigned)(__ballot(lm > 1.099511627776e12f) >> 32) != 0u, 0)) {
        NRESC;
        o[3] = MFMA32(asfrag(kv[3][2]), asfrag(pB0), o[3]); o[3] = MFMA32(asfrag(kv[3][3]), asfrag(pB1), o[3]); pB0 = (u32x4){}; pB1 = (u32x4){};
#pragma unroll
        for (int q = 0; q < 4; ++q) { const float l = upper_bcast(o[q][12]); redo |= !(l < 1.2980742146337069e33f);
          const float mn = set_ref(qf[q][1], mref[q] + (float)max(__builtin_amdgcn_frexp_expf(l) + (int)REF_MARGIN, 0), hh);
          const int e = (int)(mn - mref[q]); mref[q] = mn; redo |= !(__builtin_fabsf(mn) < REF_LIMIT);
#pragma unroll
          for (int r = 0; r < 16; ++r) o[q][r] = __builtin_ldexpf(o[q][r], -e);
          if (q == 0) {
#pragma unroll
            for (int r = 0; r < 16; ++r) sA[r] -= (float)e; } } }
    }
    { const int kt = 12;
      if (__builtin_expect(__any(redo), 0)) { mref[0] = mref[1] = mref[2] = mref[3] = __builtin_nanf(""); }
      pwait<L_CSBP>(ev);
      LSTAMP(22); KEYTILE(0, 1, 3, true, 0, , ); KEYTILE(1, 2, 0, false, 0, , ); LSTAMP(23); KEYTILE(2, 3, 1, false, 0, , );
      post<A_DONE>(ev); __builtin_amdgcn_s_setprio(3);
      KEYTILE(3, 0, 2, false, -1, PUT_O(0), PUT_O(1)); }
    o[3] = MFMA32(asfrag(kv[3][2]), asfrag(pB0), o[3]); o[3] = MFMA32(asfrag(kv[3][3]), asfrag(pB1), o[3]); PIN(o[3]); SBAR();
    PUT_O(2);
    STAMP(7);
    VSTAMP(28, nresc + (__any(redo) ? 1000 : 0));
    auto combine = [&]() -> bool {
      f32x4 pp[3][4]; float ms[3]; f32x16 of;
#pragma unroll
      for (int e = 0; e < 3; ++e) { const lptr a = area(t, e); pp[e][3] = ldsf4(a + 3072); }
#pragma unroll
      for (int e = 0; e < 3; ++e) { const lptr a = area(t, e);
#pragma unroll
        for (int i = 0; i < 3; ++i) pp[e][i] = ldsf4(a + i * 1024);
        ms[e] = upper_bcast(pp[e][3][1]); }
      const float mx = __builtin_fmaxf(__builtin_fmaxf(mref[3], ms[0]), __builtin_fmaxf(ms[1], ms[2])) + 2.f;
      float f0 = __builtin_amdgcn_exp2f(mref[3] - mx), f1 = __builtin_amdgcn_exp2f(ms[0] - mx), f2 = __builtin_amdgcn_exp2f(ms[1] - mx), f3 = __builtin_amdgcn_exp2f(ms[2] - mx);
      const float l = upper_bcast(__builtin_fmaf(pp[2][3][0], f3, __builtin_fmaf(pp[1][3][0], f2, __builtin_fmaf(pp[0][3][0], f1, o[3][12] * f0))));
      const float inv = __builtin_amdgcn_rcpf(l);
      f0 = sc1(f0 * inv); f1 = sc1(f1 * inv); f2 = sc1(f2 * inv); f3 = sc1(f3 * inv);
#pragma unroll
      for (int r = 0; r < 16; ++r) of[r] = sc1(__builtin_fmaf(pp[2][r >> 2][r & 3], f3, sc1(__builtin_fmaf(pp[1][r >> 2][r & 3], f2, sc1(__builtin_fmaf(pp[0][r >> 2][r & 3], f1, sc1(o[3][r] * f0)))))));
      u32x4 e0, e1;
#pragma unroll
      for (int p = 0; p < 4; ++p) { e0[p] = cvtpk(of[2 * p], of[2 * p + 1]); e1[p] = cvtpk(of[8 + 2 * p], of[9 + 2 * p]); }
      if (hh) { e1[2] = 0u; e1[3] = 0u; }
      const unsigned a = WS_O + rt * 32768 + j * 2048 + lane * 16;
      ST16(e0, a); ST16(e1, a + 1024);
      unsigned ex = 0;
#pragma unroll
      for (int p = 0; p < 4; ++p) ex |= ((e0[p] & 0x7f807f80u) + 0x00800080u) | ((e1[p] & 0x7f807f80u) + 0x00800080u);
      return __any((ex & 0x80008000u) != 0u || !(l < 1.2676506002282294e30f));
    };
    post<A_CMB>(ev); STAMP(9); pwait<A_CMB>(ev);
    if (__builtin_expect(combine(), 0)) *(LAS unsigned*)(lds + L_CNT + 4 * C_BAD) = 1u;
    STAMP(10);
    WAIT_VM(0);
    const unsigned arr = cnt_add(lds, C_O);
    __builtin_amdgcn_s_setprio(0);
    if (arr != 3u) pwait_long<C_O>(ev);
    const bool bad = __builtin_amdgcn_readfirstlane(*(volatile LAS unsigned*)(lds + L_CNT + 4 * C_BAD)) != 0u;
    if (!bad && arr == 3u && lane == 0) __hip_atomic_store((GAS u64*)(ws + WS_FO + fme), tag, RLX_AGENT);
    if (__builtin_expect(bad, 0)) {
#pragma unroll
      for (int q = 0; q < 4; ++q) { set_ref(qf[q][1], 0.f, hh); o[q] = zero; mref[q] = -3.0e38f; }
#pragma nounroll
      for (int kt = 0; kt < 16; ++kt) {
        const u32x4 k0 = LD16(kvb + kt * 4096), k1 = LD16(kvb + kt * 4096 + 1024), v0 = LD16(kvb + kt * 4096 + 2048), v1 = LD16(kvb + kt * 4096 + 3072);
#pragma unroll
        for (int q = 0; q < 4; ++q) {
          f32x16 s = MFMA32(asfrag(k0), qf[q][0], zero); s = MFMA32(asfrag(k1), qf[q][1], s);
          const float mn = __builtin_fmaxf(mref[q], tile_max(s)), al = __builtin_amdgcn_exp2f(mref[q] - mn); mref[q] = mn;
#pragma unroll
          for (int r = 0; r < 16; ++r) { o[q][r] *= al; s[r] = __builtin_amdgcn_exp2f(s[r] - mn); }
          const u32x4 p0 = {cvtpk(s[0], s[1]), cvtpk(s[2], s[3]), cvtpk(s[4], s[5]), cvtpk(s[6], s[7])}, p1 = {cvtpk(s[8], s[9]), cvtpk(s[10], s[11]), cvtpk(s[12], s[13]), cvtpk(s[14], s[15])};
          o[q] = MFMA32(asfrag(v0), asfrag(p0), o[q]); o[q] = MFMA32(asfrag(v1), asfrag(p1), o[q]);
        }
      }
      PUT_O(0); PUT_O(1); PUT_O(2);
      post<A_CMB2>(ev); pwait<A_CMB2>(ev);
      combine();
      WAIT_VM(0);
      if (cnt_add(lds, C_O2) == 3u && lane == 0) __hip_atomic_store((GAS u64*)(ws + WS_FO + fme), tag, RLX_AGENT);
    }
    STAMP(8); CSTAMP(31); STAMP_OUT();
  } else {
    Ld k;
    k.xsrc = x + (rb * 128 + t * 32) * 896; k.xvo0 = (lane >> 3) * 896 + (((lane & 7) ^ (lane >> 4)) << 4); k.xvo1 = k.xvo0 ^ 64u;
    k.xdst = lds0 + t * 4096; k.xdst6 = lds0 + (t < 2 ? 18 * SLOT + t * 4096 : (t - 2) * 4096);
#pragma unroll
    for (int i = 0; i < 7; ++i) k.ck[i] = i + r7 >= 7 ? i + r7 - 7 : i + r7;
    { const int row = 16 * t + (lane >> 3); const unsigned piece = ((lane & 7) ^ (((lane >> 4) & 1) << 2)) << 4;
      k.w0rs = j < 14 ? 2688u : 3584u; k.w0src = j < 14 ? wqkv + 896 + j * 128 : w1 + (j - 14) * 128;
      k.fasrc = w1 + (2 + j) * 128; k.dst = lds0 + t * 2048;
      k.w0vo = row * k.w0rs + piece; k.wfvo = row * 3584 + piece;
      k.wqsrc = wqkv + j * 56; k.wqvo = (16 * t + (lane >> 2)) * 2688 + (lane & 3) * 16; k.qdst = lds0 + L_WQ + t * 1024; k.ev = ev; }
    {
      COMMON_SETUP();
      STAMP(14);
      p1_stream<0>(k, xa, xrd, xwrap STA);
      STAMP(15);
      __builtin_amdgcn_s_setprio(GPRIO);
#pragma unroll
      for (int s = 0; s < 4; ++s) { wr0[s] += L_FA; wr1[s] += L_FA; PIN(wr0[s]); PIN(wr1[s]); }
      WF f[3]; f32x16 a0 = {}, a1 = {}; f32x4 c0 = {}, c1 = {}; float cr[16]; u32x4 o[2];
      pwait<L_FAL>(ev);
      wf_read(f[0], wr0[0], wr1[0]); wf_read(f[1], wr0[1], wr1[1]);
      f_steps<0, 0>(ev, xa, f, a0, a1, c0, c1, sel, wr0, wr1);
      cs_put(lds, L_CSA, 0, t, lane, c0); cs_put(lds, L_CSA, 1, t, lane, c1);
      pwait<A_ST>(ev);
      rs = *(LAS float*)(lds + L_STAT + t * 256 + r32 * 4); nmb = *(LAS float*)(lds + L_STAT + t * 256 + 128 + r32 * 4);
      post<L_CSAP>(ev); pwait<L_CSAP>(ev);
      STAMP(2);
      __builtin_amdgcn_s_setprio(3);
      { const unsigned ad = WS_H + rt * 114688 + (2 + j) * 4096 + lane * 16;
        lane_to_regs(priv, r32, hh, cs_get(lds, L_CSA, 0, r32), cr); epi_T<true>(a0, cr, rs, nmb, o[0], o[1]); ST16(o[0], ad); ST16(o[1], ad + 1024);
        lane_to_regs(priv + 128, r32, hh, cs_get(lds, L_CSA, 1, r32), cr); epi_T<true>(a1, cr, rs, nmb, o[0], o[1]); ST16(o[0], ad + 2048); ST16(o[1], ad + 3072); }
      STAMP(3);
      if (j >= 6) {
#pragma unroll
        for (int s = 0; s < 4; ++s) { wr0[s] += L_FB - L_FA; wr1[s] += L_FB - L_FA; PIN(wr0[s]); PIN(wr1[s]); }
        a0 = (f32x16){}; a1 = (f32x16){}; c0 = (f32x4){}; c1 = (f32x4){};
        pwait<A_FB>(ev);
        wf_read(f[0], wr0[0], wr1[0]); wf_read(f[1], wr0[1], wr1[1]);
        f_steps<1, 0>(ev, xa, f, a0, a1, c0, c1, sel, wr0, wr1);
        cs_put(lds, L_CSB, 0, t, lane, c0); cs_put(lds, L_CSB, 1, t, lane, c1);
        post<L_CSBP>(ev); pwait<L_CSBP>(ev);
        const unsigned ad = WS_H + rt * 114688 + (12 + j) * 4096 + lane * 16;
        lane_to_regs(priv, r32, hh, cs_get(lds, L_CSB, 0, r32), cr); epi_T<true>(a0, cr, rs, nmb, o[0], o[1]); ST16(o[0], ad); ST16(o[1], ad + 1024);
        lane_to_regs(priv + 128, r32, hh, cs_get(lds, L_CSB, 1, r32), cr); epi_T<true>(a1, cr, rs, nmb, o[0], o[1]); ST16(o[0], ad + 2048); ST16(o[1], ad + 3072);
      } else post<L_CSBP>(ev);
    }
    __builtin_amdgcn_s_setprio(0);
    u64 tagraw = *(LAS u64*)(lds + L_TAG);
    GET_TAG();
    WAIT_VM(0);
    if (cnt_add(lds, C_H) == 7u && lane == 0) __hip_atomic_store((GAS u64*)(ws + WS_FH + fme), tag, RLX_AGENT);
    STAMP(4);
    const int cg = bi >> 2;
    if (cg < 7) {
      const int rt0 = 8 * xg + 2 * (bi & 3), rb3 = rt0 >> 2;
      const int ring = ring_of(t);
      const int ct = t >> 1, rtl = t & 1;
      G2 g; g.ev = ev; g.t = t; g.ringdst = lds0 + ring; g.lv = lane * 16;
      g.w2vo = (lane >> 3) * 896 + (((lane & 7) ^ (((lane >> 4) & 1) << 2)) << 4);
      g.w2s = w2 + t * 448 * 896 + cg * 128; g.wos = wo + t * 112 * 896 + cg * 128;
      g.hb0 = ws + WS_H + (rt0 + rtl) * 114688 + t * 28672; g.hb1 = ws + WS_H + (rt0 + (rtl ^ 1)) * 114688 + t * 28672;
      g.br0 = wrd0 + ring + ((ct ^ (q4 >> 1)) << 6); g.br1 = wrd0 + ring + ((ct ^ 1 ^ (q4 >> 1)) << 6);
      g2_ws<0, 12>(g);
      const int fb3 = rb3 * 16 + (lane & 15);
      __builtin_amdgcn_s_setprio(0);
      poll_flags<8>(ws + WS_FH + fb3 * 128, tag);
      __builtin_amdgcn_s_setprio(G2PRIO);
      WAIT_VM(0);
      STAMP(5);
      u32x4 hf[14][2];
#pragma unroll
      for (int s = 0; s < 14; ++s) { ldg_sc1(hf[s][0], g.hb0 + s * 1024, g.lv); ldg_sc1(hf[s][1], g.hb1 + s * 1024, g.lv); }
      f32x16 a00 = {}, a01 = {}, a10 = {}, a11 = {};
      PIN(g.br0); PIN(g.br1);
      { WF f[3]; wf_read(f[0], g.br0, g.br1); wf_read(f[1], g.br0 + 2048, g.br1 + 2048);
        g2_steps<0>(g, hf, f, a00, a01, a10, a11); }
      STAMP(6);
      WAIT_VM(0); post<L_WO>(ev);
      const size_t oo = (size_t)((rt0 + rtl) * 32 + r32) * 896 + cg * 128 + ct * 64 + hh * 16;
      const u32x4 xr0 = *(const GAS u32x4*)(x + oo), xr1 = *(const GAS u32x4*)(x + oo + 32);
      pwait<L_WO>(ev);
      bf16x8 wf[16][2];
#pragma unroll
      for (int g2 = 0; g2 < 2; ++g2) { const lptr o0 = lds + ring_of(2 * ct + g2) + SLOT + (4 * hh + q4) * 128 + pi_std * 8;
#pragma unroll
        for (int e = 0; e < 8; ++e)
#pragma unroll
          for (int cc = 0; cc < 2; ++cc) wf[8 * g2 + e][cc] = trfrag(o0 + ((cc ^ ct ^ (q4 >> 1)) << 6) + ((e >> 1) * 28 + (e & 1) * 16) * 128, 1024); }
      post<L_WOR>(ev); pwait<L_WOR>(ev);
      auto put = [&](const f32x16& a, int e) { const lptr m = lds + ring + e * 4096 + lane * 16;
#pragma unroll
        for (int i = 0; i < 4; ++i) *(LAS f32x4*)(m + i * 1024) = (f32x4){a[4 * i], a[4 * i + 1], a[4 * i + 2], a[4 * i + 3]}; };
      auto add = [&](f32x16& a, int src, int e) { const lptr m = lds + ring_of(src) + e * 4096 + lane * 16;
#pragma unroll
        for (int i = 0; i < 4; ++i) { const f32x4 v = ldsf4(m + i * 1024);
#pragma unroll
          for (int n = 0; n < 4; ++n) a[4 * i + n] = sc1(a[4 * i + n] + v[n]); } };
      put(a11, 3); put(a10, 2); put(a01, 1);
      post<L_RED>(ev); pwait<L_RED>(ev);
      add(a00, t ^ 1, 1); add(a00, t ^ 2, 2); add(a00, t ^ 3, 3);
#pragma unroll
      for (int p = 0; p < 4; ++p) { a00[2 * p] = sc1(a00[2 * p] + bflo(xr0[p])); a00[2 * p + 1] = sc1(a00[2 * p + 1] + bfhi(xr0[p]));
        a00[8 + 2 * p] = sc1(a00[8 + 2 * p] + bflo(xr1[p])); a00[9 + 2 * p] = sc1(a00[9 + 2 * p] + bfhi(xr1[p])); }
      a10 = (f32x16){}; PIN(a00); PIN(a10);
      STAMP(11);
      { const char* fl = ws + WS_FO + (rb3 * 16 + ct * 8 + (lane & 7)) * 128; const unsigned ob = WS_O + (rt0 + rtl) * 32768 + ct * 16384 + lane * 16;
        unsigned ready = 0; u32x4 of[8][2];
#pragma unroll
        for (int h = 0; h < 8 + OLAG; ++h) {
          if (h < 8) {
            if (__builtin_expect(!((ready >> h) & 1u), 0)) ready = poll_bit(fl, tag, h);
            if (h == 7) STAMP(7);
            of[h][0] = LD16(ob + h * 2048); of[h][1] = LD16(ob + h * 2048 + 1024); }
          if (h >= OLAG) {
#pragma unroll
            for (int s = 0; s < 2; ++s) { const bf16x8 o_ = asfrag(of[h - OLAG][s]); a10 = MFMA32(wf[2 * (h - OLAG) + s][1], o_, a10); a00 = MFMA32(wf[2 * (h - OLAG) + s][0], o_, a00); }
            PIN(a10); PIN(a00); }
          SBAR(); } }
      STAMP(9);
      put(a10, 0);
      post<L_FIN>(ev);
      STAMP(10); pwait<L_FIN>(ev);
      add(a00, t ^ 2, 0);
      u32x4 r0, r1;
#pragma unroll
      for (int p = 0; p < 4; ++p) { r0[p] = cvtpk(a00[2 * p], a00[2 * p + 1]); r1[p] = cvtpk(a00[8 + 2 * p], a00[9 + 2 * p]); }
      *(GAS u32x4*)(out + oo) = r0; *(GAS u32x4*)(out + oo + 32) = r1;
      if (b == 0 && tid == 256) __hip_atomic_store((GAS u64*)(ws + WS_EPOCH), tag, RLX_AGENT);
      if (w == 4) { asm volatile("s_waitcnt vmcnt(0)" ::: "memory"); const u64 now = __builtin_amdgcn_s_memrealtime(), cn = __builtin_amdgcn_s_memtime();
        u64 X = METRIC == 1 ? now - rt0_ : METRIC == 2 ? (cn - c0_) / 20 : 0; X = X > 6000 ? 6000 : X;
        while (__builtin_amdgcn_s_memrealtime() - now < X) __builtin_amdgcn_s_sleep(2); }
    }
    STAMP_OUT();
  }
}

extern "C" void kernel_launch(void* const* d_in, const int* in_sizes, int n_in, void* d_out, int out_size, void* d_ws, size_t ws_size, hipStream_t stream) {
  fused_block<<<256, 512, 0, stream>>>((const char*)d_in[0], (const char*)d_in[1], (const char*)d_in[2], (const char*)d_in[4], (const char*)d_in[5], (char*)d_out, (char*)d_ws + WS_BASE);
}
```

```cpp
#define MULT 0
#include <hip/hip_runtime.h>
#include <stdint.h>

typedef short bf16x8 __attribute__((ext_vector_type(8)));
typedef short s16x4 __attribute__((ext_vector_type(4)));
typedef float f32x16 __attribute__((ext_vector_type(16)));
typedef float f32x4 __attribute__((ext_vector_type(4)));
typedef float f32x2 __attribute__((ext_vector_type(2)));
typedef __bf16 bf16x2v __attribute__((ext_vector_type(2)));
typedef unsigned u32x4 __attribute__((ext_vector_type(4)));
typedef unsigned long long u64;
#define LAS __attribute__((address_space(3)))
#define GAS __attribute__((address_space(1)))
#define DI __device__ __forceinline__
#define MFMA32(a, b, c) __builtin_amdgcn_mfma_f32_32x32x16_bf16(a, b, c, 0, 0, 0)
#define MFMA16(a, b, c) __builtin_amdgcn_mfma_f32_16x16x32_bf16(a, b, c, 0, 0, 0)
#define SBAR() __builtin_amdgcn_sched_barrier(0)
#define PIN(x) asm volatile("" : "+v"(x))
#define RLX_AGENT __ATOMIC_RELAXED, __HIP_MEMORY_SCOPE_AGENT
#define WAIT_BAR(N) asm volatile("s_waitcnt vmcnt(" #N ") lgkmcnt(0)\n\ts_barrier" ::: "memory")
#define WAIT_VM(N) asm volatile("s_waitcnt vmcnt(" #N ")" ::: "memory")
#define WAIT_LGKM0() asm volatile("s_waitcnt lgkmcnt(0)" ::: "memory")

constexpr unsigned WS_EPOCH = 0;
constexpr unsigned WS_FKV = 4096, WS_FH = WS_FKV + 32768, WS_FO = WS_FH + 32768;
constexpr unsigned WS_KV = 1u << 20;
constexpr unsigned WS_H = 5u << 20;
constexpr unsigned WS_O = 13u << 20;
constexpr unsigned WS_BYTES = 15u << 20;
constexpr int SLOT = 8192, NSLOT = 19;
constexpr int L_MISC = NSLOT * SLOT;
constexpr int L_STAT = L_MISC;
constexpr int L_CS0 = L_MISC + 2048, L_CSA = L_CS0 + 1024, L_CSB = L_CSA + 1024;
constexpr int L_PRIV = L_MISC + 5120;
constexpr int L_CNT = L_MISC + 7168;
constexpr int L_TAG = L_MISC + 7424;
constexpr int LDS_BYTES = 163840;
constexpr int L_FA = 2 * SLOT, L_WQ = 9 * SLOT, L_FB = 13 * SLOT, L_QX = SLOT;
constexpr int ring_of(int t) { return (t < 2 ? 2 + 3 * t : 7 + 3 * t) * SLOT; }
constexpr int p1_slot(int item) { return (item % NSLOT) * SLOT; }
enum { C_KV, C_H, C_O, C_O2, C_BAD,
       A_RD0, A_CS0 = A_RD0 + 6, A_CSX, A_ST, A_FB, A_QW, A_DONE, A_CMB, A_CMB2,
       L_LD0, L_X6 = L_LD0 + 7, L_FAL, L_WQL, L_CSAP, L_CSBP, L_WO, L_WOR, L_RED, L_FIN, N_EV };

#define WS_BASE 0
#define TOUCH 0
#define ROT7 0
#define MAPA 8
#define G2_SPLIT 14
#define CODE_BYTES 55296
constexpr float QSCALE = 0.18898223650461362f * 1.4426950408889634f;
constexpr float REF_MARGIN = 64.f;
constexpr float REF_LIMIT = 4096.f;
constexpr unsigned SPIN_CAP = 1u << 16;

DI unsigned cvtpk(float lo, float hi) { f32x2 v = {lo, hi}; return __builtin_bit_cast(unsigned, __builtin_convertvector(v, bf16x2v)); }
DI float sc1(float x) { asm("" : "+v"(x)); return x; }
DI float bf16r(float x) { return __uint_as_float(cvtpk(x, 0.f) << 16); }
DI float bflo(unsigned v) { return __uint_as_float(v << 16); }
DI float bfhi(unsigned v) { return __uint_as_float(v & 0xffff0000u); }
DI float swap_add(float v) { auto r = __builtin_amdgcn_permlane32_swap(__float_as_uint(v), __float_as_uint(v), false, false); return __uint_as_float(r[0]) + __uint_as_float(r[1]); }
DI float swap_max(float v) { auto r = __builtin_amdgcn_permlane32_swap(__float_as_uint(v), __float_as_uint(v), false, false); return __builtin_fmaxf(__uint_as_float(r[0]), __uint_as_float(r[1])); }
DI float upper_bcast(float v) { auto r = __builtin_amdgcn_permlane32_swap(__float_as_uint(v), __float_as_uint(v), false, false); return __uint_as_float(r[1]); }
DI void glds16(const void* sbase, unsigned voff, unsigned ldsdst) {
  unsigned keep;
  asm volatile("s_mov_b32 %0, m0\n\ts_mov_b32 m0, %3\n\ts_nop 0\n\tglobal_load_lds_dwordx4 %1, %2\n\ts_mov_b32 m0, %0"
               : "=&s"(keep) : "v"(voff), "s"(sbase), "s"(ldsdst) : "memory");
}
typedef LAS char* lptr;
typedef short v4i16_t __attribute__((ext_vector_type(4)));
DI s16x4 trd(lptr p) { return __builtin_bit_cast(s16x4, __builtin_amdgcn_ds_read_tr16_b64_v4i16((LAS v4i16_t*)p)); }
DI bf16x8 trfrag(lptr p, int second) { s16x4 lo = trd(p), hi = trd(p + second); return __builtin_shufflevector(lo, hi, 0, 1, 2, 3, 4, 5, 6, 7); }
DI bf16x8 ldsb128(lptr p) { return *(LAS bf16x8*)p; }
DI f32x4 ldsf4(lptr p) { return *(LAS f32x4*)p; }
DI bf16x8 asfrag(u32x4 v) { return __builtin_bit_cast(bf16x8, v); }
struct Ev { unsigned base, one; };
template <int E> DI void post(const Ev& e) {
  asm volatile("s_mov_b64 exec, 1\n\tds_add_u32 %0, %1 offset:%2\n\ts_mov_b64 exec, -1" :: "v"(e.base), "v"(e.one), "i"(4 * E) : "memory"); }
template <int E> DI void pwait(const Ev& e) {
  unsigned a;
  asm volatile("1:\n\tds_read_b32 %0, %1 offset:%2\n\ts_waitcnt lgkmcnt(0)\n\tv_cmp_gt_u32 vcc, 4, %0\n\ts_cbranch_vccnz 1b"
               : "=&v"(a) : "v"(e.base), "i"(4 * E) : "vcc", "memory"); }
template <int E> DI void pwait_long(const Ev& e) {
  unsigned a;
  asm volatile("1:\n\ts_sleep 8\n\tds_read_b32 %0, %1 offset:%2\n\ts_waitcnt lgkmcnt(0)\n\tv_cmp_gt_u32 vcc, 4, %0\n\ts_cbranch_vccnz 1b"
               : "=&v"(a) : "v"(e.base), "i"(4 * E) : "vcc", "memory"); }
DI unsigned cnt_add(lptr lds, int c) { unsigned r = 0; if ((threadIdx.x & 63) == 0) r = __hip_atomic_fetch_add((LAS unsigned*)(lds + L_CNT + 4 * c), 1u, __ATOMIC_RELAXED, __HIP_MEMORY_SCOPE_WORKGROUP);
  return __builtin_amdgcn_readfirstlane(r); }
template <int SLEEP> DI void poll_flags(const char* flag, u64 tag) {
  u64 v; unsigned n = SPIN_CAP;
  asm volatile("1:\n\tglobal_load_dwordx2 %0, %2, off sc1\n\ts_sub_u32 %1, %1, 1\n\ts_cbranch_scc1 2f\n\ts_sleep %4\n\ts_waitcnt vmcnt(0)\n\t"
               "v_cmp_ne_u64 vcc, %3, %0\n\ts_cbranch_vccnz 1b\n2:\n\ts_waitcnt vmcnt(0)"
               : "=&v"(v), "+s"(n) : "v"(flag), "s"(tag), "i"(SLEEP) : "vcc", "scc", "memory");
}
DI unsigned poll_bit(const char* flag, u64 tag, int h) {
  u64 v, m; unsigned n = SPIN_CAP;
  asm volatile("1:\n\tglobal_load_dwordx2 %0, %3, off sc1\n\ts_sub_u32 %2, %2, 1\n\ts_cbranch_scc1 2f\n\ts_waitcnt vmcnt(0)\n\t"
               "v_cmp_eq_u64 %1, %4, %0\n\ts_nop 3\n\ts_bitcmp0_b64 %1, %5\n\ts_cbranch_scc1 1b\n2:\n\ts_waitcnt vmcnt(0)"
               : "=&v"(v), "=&s"(m), "+s"(n) : "v"(flag), "s"(tag), "i"(h) : "scc", "memory");
  return (unsigned)m;
}
#define STAMP(i) do {} while (0)
#define CSTAMP(i) do {} while (0)
#define VSTAMP(i, v) do {} while (0)
#define STAMP_OUT() do {} while (0)
#define NRESC
#define STV
#define STA
#define FSTAMP(i) do {} while (0)
#define LSTAMP(i) do {} while (0)
#define BAR() asm volatile("s_waitcnt lgkmcnt(0)\n\ts_barrier" ::: "memory")

#define QPRIO 3
#define LPRIO 2
#define GPRIO 1
#define G2PRIO 0
#define NOCHECK 0
#define QSPLIT 8
#define OLAG 4
#define LOOK 4
DI float pick(u64 m, float hi, float lo) { float r; asm("v_cndmask_b32 %0, %1, %2, %3" : "=v"(r) : "v"(lo), "v"(hi), "s"(m)); return r; }
DI float gram_diag(const f32x16& g) {
  const u64 m1 = 0xaaaaaaaaaaaaaaaaull, m2 = 0xccccccccccccccccull, m4 = 0xf0f0f0f0f0f0f0f0ull, m8 = 0xff00ff00ff00ff00ull, m16 = 0xffff0000ffff0000ull;
  const float b0 = pick(m2, pick(m1, g[3], g[2]), pick(m1, g[1], g[0])), b1 = pick(m2, pick(m1, g[7], g[6]), pick(m1, g[5], g[4]));
  const float b2 = pick(m2, pick(m1, g[11], g[10]), pick(m1, g[9], g[8])), b3 = pick(m2, pick(m1, g[15], g[14]), pick(m1, g[13], g[12]));
  float d = pick(m16, pick(m8, b3, b2), pick(m8, b1, b0));
  asm volatile("s_nop 1" : "+v"(d));
  auto r = __builtin_amdgcn_permlane32_swap(__float_as_uint(d), __float_as_uint(d), false, false);
  return pick(m4, __uint_as_float(r[1]), __uint_as_float(r[0]));
}
template <int C> DI void x_frags(bf16x8 (&xa)[28], const lptr (&xrd)[4], int xwrap) {
#pragma unroll
  for (int s = 0; s < 4; ++s) xa[4 * C + s] = ldsb128(xrd[s] + p1_slot(3 * C) + (C == 6 ? xwrap : 0));
}
DI void row_norm(float s1, float s2, float& rs, float& nmb) {
  const float mean = sc1(s1 * (1.f / 448.f)), var = __builtin_fmaxf(sc1(s2 * (1.f / 448.f)) - mean * mean, 0.f);
  const float sd = bf16r(__builtin_amdgcn_sqrtf(bf16r(bf16r(var) + 1e-5f))); rs = __builtin_amdgcn_rcpf(sd); nmb = -bf16r(mean);
}
struct P1A { lptr x[4], w0[4], w1[4]; int xwrap; Ev ev; };
struct WF { s16x4 l0, h0, l1, h1; };
DI bf16x8 cat(const s16x4& lo, const s16x4& hi) { return __builtin_shufflevector(lo, hi, 0, 1, 2, 3, 4, 5, 6, 7); }
struct P1R { f32x16 a0, a1; f32x4 c0, c1, s1; f32x16 s2; };
#define MM(VT, acc, xf, wf) do { if constexpr (VT) acc = MFMA32(xf, wf, acc); else acc = MFMA32(wf, xf, acc); PIN(acc); SBAR(); } while (0)
template <bool VT, int ST, int C, int S> DI void p1_group(bf16x8 (&xa)[28], WF (&f)[4], P1R& r, const bf16x8& sel, const P1A& p) {
  constexpr int xo = p1_slot(3 * C) - (C >> 1) * 6 * SLOT, wo = p1_slot(3 * C + 2) - (C >> 1) * 6 * SLOT;
  if constexpr (ST >= 0) MM(VT, r.a0, xa[ST], cat(f[ST & 3].l0, f[ST & 3].h0));
  xa[4 * C + S] = ldsb128(p.x[S] + xo); f[S].l0 = trd(p.w0[S] + wo); SBAR();
  if constexpr (ST >= 0) MM(VT, r.a1, xa[ST], cat(f[ST & 3].l1, f[ST & 3].h1));
  f[S].h0 = trd(p.w0[S] + wo + 512); f[S].l1 = trd(p.w1[S] + wo); SBAR();
  if constexpr (ST >= 0) { r.s2 = MFMA32(xa[ST], xa[ST], r.s2); PIN(r.s2); SBAR(); }
  f[S].h1 = trd(p.w1[S] + wo + 512); SBAR();
  if constexpr (ST >= 0) { r.s1 = MFMA16(sel, xa[ST], r.s1); PIN(r.s1);
    if constexpr ((ST & 3) == 0) { r.c0 = MFMA16(sel, cat(f[0].l0, f[0].h0), r.c0); r.c1 = MFMA16(sel, cat(f[0].l1, f[0].h1), r.c1); PIN(r.c0); PIN(r.c1); }
    SBAR(); }
}
template <bool VT, int C> DI void p1_compute(bf16x8 (&xa)[28], WF (&f)[4], P1R& r, const bf16x8& sel, P1A& p STV) {
  if constexpr (C == 0) pwait<L_LD0>(p.ev);
  if (C == 0) STAMP(12);
  FSTAMP(15 + C);
  if constexpr (C < 6) {
    p1_group<VT, 4 * C - 2, C, 0>(xa, f, r, sel, p); p1_group<VT, 4 * C - 1, C, 1>(xa, f, r, sel, p); p1_group<VT, 4 * C, C, 2>(xa, f, r, sel, p);
    const unsigned nxt = *(volatile LAS unsigned*)(uintptr_t)(p.ev.base + 4 * (L_LD0 + C + 1));
    p1_group<VT, 4 * C + 1, C, 3>(xa, f, r, sel, p);
    post<A_RD0 + C>(p.ev);
    if constexpr (C == 1 || C == 3) {
#pragma unroll
      for (int s = 0; s < 4; ++s) { p.x[s] += 6 * SLOT; p.w0[s] += 6 * SLOT; p.w1[s] += 6 * SLOT; PIN(p.x[s]); PIN(p.w0[s]); PIN(p.w1[s]); }
    }
    if constexpr (C == 5) {
#pragma unroll
      for (int s = 0; s < 4; ++s) { p.x[s] += p.xwrap; p.w0[s] -= 12 * SLOT; p.w1[s] -= 12 * SLOT; PIN(p.x[s]); PIN(p.w0[s]); PIN(p.w1[s]); }
    }
    SBAR();
    if (__builtin_expect(__builtin_amdgcn_readfirstlane(nxt) < 4u, 0)) pwait<L_LD0 + C + 1>(p.ev);
    p1_compute<VT, C + 1>(xa, f, r, sel, p STA);
  }
}
struct Ld { const char *xsrc, *w0src, *wqsrc, *fasrc; unsigned xvo0, xvo1, w0vo, wfvo, wqvo, w0rs, dst, xdst, xdst6, qdst; Ev ev; int ck[7]; };
template <int N, int E> DI void ld_piece(const Ld& k) {
  const unsigned dst = k.dst + p1_slot(N) + E * 1024;
  if constexpr (N < 21) {
    constexpr int c = N / 3, q = N % 3;
    if constexpr (q < 2) glds16(k.xsrc + (q * 16 + E * 8) * 896 + k.ck[c] * 128, E ? k.xvo1 : k.xvo0, (c < 6 ? k.xdst + p1_slot(3 * c) : k.xdst6) + (2 * q + E) * 1024);
    else glds16(k.w0src + (k.ck[c] * 64 + E * 8) * k.w0rs, k.w0vo, dst);
  } else if constexpr (N < 28) glds16(k.fasrc + (k.ck[N - 21] * 64 + E * 8) * 3584, k.wfvo, dst);
  else { constexpr int c = 2 * (N - 28) + E;
    glds16(k.wqsrc + k.ck[c < 7 ? c : 0] * (64 * 2688), k.wqvo, k.qdst + c * 4096); }
}
template <int N> DI void p1_stream(const Ld& k, bf16x8 (&xa)[28], const lptr (&xrd)[4], int xwrap STV) {
  if constexpr (N < 32) {
    if constexpr (N == 18 || (N > 19 && (N - 19) % 3 == 0)) { pwait<A_RD0 + (N - 18) / 3>(k.ev); pwait<L_LD0 + (N - 18) / 3 + 1>(k.ev); }
    ld_piece<N, 0>(k); ld_piece<N, 1>(k);
    if constexpr (N == 2) asm volatile("s_barrier" ::: "memory");
    constexpr int M = N - LOOK;
    if constexpr (M >= 2 && M <= 20 && M % 3 == 2) {
      constexpr int C = M / 3;
      asm volatile("s_waitcnt vmcnt(%0)" :: "i"(2 * LOOK) : "memory");
      post<L_LD0 + C>(k.ev);
      if (C == 0) STAMP(12);
      x_frags<C>(xa, xrd, xwrap); SBAR();
      if (C == 6) STAMP(1);
    } else if constexpr (M == 21) post<L_X6>(k.ev);
    else if constexpr (M == 27) { asm volatile("s_waitcnt vmcnt(%0)" :: "i"(2 * LOOK) : "memory"); post<L_FAL>(k.ev); }
    p1_stream<N + 1>(k, xa, xrd, xwrap STA);
  }
}
DI void cs_put(lptr lds, int area, int c, int t, int lane, const f32x4& cs) {
  if (lane < 16) { *(LAS float*)(lds + area + c * 512 + lane * 16 + t * 4) = cs[0]; *(LAS float*)(lds + area + c * 512 + (lane + 16) * 16 + t * 4) = cs[1]; }
}
DI float cs_get(lptr lds, int area, int c, int r32) { const f32x4 p = ldsf4(lds + area + c * 512 + r32 * 16); return sc1(sc1(p[0] + p[1]) + sc1(p[2] + p[3])); }
DI void lane_to_regs(lptr priv, int r32, int hh, float v, float (&o)[16]) {
  *(LAS float*)(priv + r32 * 4) = v;
#pragma unroll
  for (int i = 0; i < 4; ++i) { const f32x4 q = ldsf4(priv + (8 * i + 4 * hh) * 4); o[4 * i] = q[0]; o[4 * i + 1] = q[1]; o[4 * i + 2] = q[2]; o[4 * i + 3] = q[3]; }
}
typedef short s16x2 __attribute__((ext_vector_type(2)));
template <bool RELU> DI void epi_T(const f32x16& acc, const float (&cs)[16], float sc, float nmb, u32x4& o0, u32x4& o1) {
#pragma unroll
  for (int p = 0; p < 8; ++p) {
    const float v0 = sc1(sc1(__builtin_fmaf(cs[2 * p], nmb, acc[2 * p])) * sc), v1 = sc1(sc1(__builtin_fmaf(cs[2 * p + 1], nmb, acc[2 * p + 1])) * sc);
    unsigned w = cvtpk(v0, v1);
    if (RELU) w = __builtin_bit_cast(unsigned, __builtin_elementwise_max(__builtin_bit_cast(s16x2, w), (s16x2){0, 0}));
    if (p < 4) o0[p] = w; else o1[p - 4] = w;
  }
}
#define ST16(v, off) __builtin_amdgcn_raw_buffer_store_b128(v, rsw, off, 0, 16)
#define ABL_NOKV 0
#define LDAUX 16
#define LD16(off) __builtin_amdgcn_raw_buffer_load_b128(rsw, off, 0, LDAUX)

DI float set_ref(bf16x8& q1, float m, int hh) {
  const unsigned w = cvtpk(-__builtin_rintf(m), 0.f);
  u32x4 v = __builtin_bit_cast(u32x4, q1); v[2] = hh ? w : v[2]; q1 = __builtin_bit_cast(bf16x8, v);
  return -__uint_as_float(w << 16);
}
DI float tile_max(const f32x16& s) {
  float a = __builtin_fmaxf(__builtin_fmaxf(s[0], s[1]), s[2]), b = __builtin_fmaxf(__builtin_fmaxf(s[3], s[4]), s[5]);
#pragma unroll
  for (int r = 6; r < 14; r += 4) { a = __builtin_fmaxf(__builtin_fmaxf(a, s[r]), s[r + 1]); b = __builtin_fmaxf(__builtin_fmaxf(b, s[r + 2]), s[r + 3]); }
  a = __builtin_fmaxf(__builtin_fmaxf(a, s[14]), s[15]);
  return swap_max(__builtin_fmaxf(a, b));
}

template <int P, int ST> DI void f_steps(const Ev& ev, const bf16x8 (&xa)[28], WF (&f)[3], f32x16& a0, f32x16& a1, f32x4& c0, f32x4& c1, const bf16x8& sel, const lptr (&w0)[4], const lptr (&w1)[4]) {
  if constexpr (ST < 28) {
    constexpr int N = ST + 2, off = p1_slot((P ? 32 : 21) + ((N < 28 ? N : 0) >> 2)) - (P ? L_FB : L_FA);
    SBAR(); MM(false, a0, xa[ST], cat(f[ST % 3].l0, f[ST % 3].h0));
    if constexpr (N < 28) { f[N % 3].l0 = trd(w0[N & 3] + off); f[N % 3].h0 = trd(w0[N & 3] + off + 512); SBAR(); }
    MM(false, a1, xa[ST], cat(f[ST % 3].l1, f[ST % 3].h1));
    if constexpr (N < 28) { f[N % 3].l1 = trd(w1[N & 3] + off); f[N % 3].h1 = trd(w1[N & 3] + off + 512); SBAR(); }
    if constexpr ((ST & 3) == 0) { c0 = MFMA16(sel, cat(f[ST % 3].l0, f[ST % 3].h0), c0); c1 = MFMA16(sel, cat(f[ST % 3].l1, f[ST % 3].h1), c1); PIN(c0); PIN(c1); }
    if constexpr (P == 0 && ST == 6) { WAIT_VM(0); post<L_WQL>(ev); }
    f_steps<P, ST + 1>(ev, xa, f, a0, a1, c0, c1, sel, w0, w1);
  }
}
DI void wf_read(WF& f, lptr p0, lptr p1) { f.l0 = trd(p0); f.h0 = trd(p0 + 512); f.l1 = trd(p1); f.h1 = trd(p1 + 512); }

constexpr int g2_issued(int st) { int n = 28; for (int s = 0; s < st; ++s) { if (s < 14) n += 2; if (s < 24) n += 2; } return n; }
constexpr int g2_pos_h(int s) { return s < 14 ? 2 * s + 1 : g2_issued(s - 14) + 1; }
constexpr int g2_pos_w(int s) { return s < 12 ? -1 : g2_issued(s - 12) + (s - 12 < 14 ? 2 : 0) + 1; }
constexpr int g2_wait(int st) { const int a = g2_pos_h(st), b = st + 2 < 28 ? g2_pos_w(st + 2) : -1; const int n = g2_issued(st) - 1 - (a > b ? a : b); return n > 63 ? 63 : n; }
DI void ldg_sc1(u32x4& d, const char* sbase, unsigned voff) { asm volatile("global_load_dwordx4 %0, %1, %2 sc1" : "=v"(d) : "v"(voff), "s"(sbase) : "memory"); }
struct G2 { const char *hb0, *hb1, *w2s, *wos; unsigned lv, w2vo, ringdst; Ev ev; int t; lptr br0, br1; };
template <int ST, int E> DI void g2_w(const G2& g) {
  const unsigned dst = g.ringdst + (ST % 12) * 2048 + E * 1024;
  if constexpr (ST < 28) glds16(g.w2s + (ST * 16 + E * 8) * 896, g.w2vo, dst);
  else if constexpr (ST < 35) glds16(g.wos + ((ST - 28) * 16 + E * 8) * 896, g.w2vo, dst);
  else glds16(g.wos + (g.t == 3 ? 104 : 112 + E * 8) * 896, g.w2vo, dst);
}
template <int A, int B> DI void g2_ws(const G2& g) { if constexpr (A < B) { g2_w<A, 0>(g); g2_w<A, 1>(g); g2_ws<A + 1, B>(g); } }
template <int ST> DI void g2_steps(const G2& g, u32x4 (&hf)[14][2], WF (&f)[3], f32x16& a00, f32x16& a01, f32x16& a10, f32x16& a11) {
  if constexpr (ST < 28) {
    if constexpr (ST == G2_SPLIT) { __builtin_amdgcn_s_setprio(0); pwait_long<A_DONE>(g.ev); __builtin_amdgcn_s_setprio(GPRIO); }
    asm volatile("s_waitcnt vmcnt(%2)" : "+v"(hf[ST % 14][0]), "+v"(hf[ST % 14][1]) : "i"(g2_wait(ST)) : "memory");
    constexpr int N = ST + 2, off = (N % 12) * 2048;
    const bf16x8 h0 = asfrag(hf[ST % 14][0]), h1 = asfrag(hf[ST % 14][1]), w0 = cat(f[ST % 3].l0, f[ST % 3].h0), w1 = cat(f[ST % 3].l1, f[ST % 3].h1);
    SBAR(); MM(false, a00, h0, w0);
    if constexpr (N < 28) { f[N % 3].l0 = trd(g.br0 + off); f[N % 3].h0 = trd(g.br0 + off + 512); SBAR(); }
    MM(false, a01, h1, w0);
    if constexpr (N < 28) { f[N % 3].l1 = trd(g.br1 + off); f[N % 3].h1 = trd(g.br1 + off + 512); SBAR(); }
    MM(false, a10, h0, w1);
    MM(false, a11, h1, w1);
    if constexpr (ST < 14) { ldg_sc1(hf[ST][0], g.hb0 + (ST + 14) * 1024, g.lv); ldg_sc1(hf[ST][1], g.hb1 + (ST + 14) * 1024, g.lv); }
    if constexpr (ST < 24) { g2_w<ST + 12, 0>(g); g2_w<ST + 12, 1>(g); }
    g2_steps<ST + 1>(g, hf, f, a00, a01, a10, a11);
  }
}

__global__ void __launch_bounds__(512, 2) fused_block(const char* x, const char* wqkv, const char* wo, const char* w1, const char* w2, char* out, char* ws) {
  asm volatile("" : "+s"(x), "+s"(wqkv), "+s"(wo), "+s"(w1), "+s"(w2), "+s"(out), "+s"(ws));
  __shared__ __attribute__((aligned(16))) char smem[LDS_BYTES];
  u64 pc; asm volatile("s_getpc_b64 %0" : "=s"(pc));
  const lptr lds = (lptr)smem; const unsigned lds0 = (unsigned)(uintptr_t)lds;
  const int tid = threadIdx.x, lane = tid & 63, r32 = lane & 31, hh = lane >> 5;
  const int w = __builtin_amdgcn_readfirstlane(tid >> 6), t = w & 3, c = w >> 2;
  const int b = blockIdx.x, xg = b & 7, bi = b >> 3;
  const int j = (xg & 3) * 4 + (bi >> 3), rb = (xg >> 2) * 8 + (bi & 7);
  const int fme = (rb * 16 + j) * 128, rt = rb * 4 + t;
  STAMP(0); CSTAMP(30); VSTAMP(29, (unsigned)__builtin_amdgcn_s_getreg(20 | (3 << 11)) & 0xfu);
  const int rot = t, r7 = ROT7 ? (rb + 2 * j) % 7 : 0;
  const int q4 = (lane & 15) >> 2, p4 = lane & 3, g16 = (lane >> 4) & 1;
  const int pi_std = 4 * g16 + 2 * (p4 & 1) + (p4 >> 1);
  const lptr wrd0 = lds + (8 * hh + q4) * 128 + pi_std * 8;
  const lptr priv = lds + L_PRIV + w * 256;
  Ev ev = {lds0 + L_CNT, 1u}; PIN(ev.base); PIN(ev.one);
  const __amdgpu_buffer_rsrc_t rsw = __builtin_amdgcn_make_buffer_rsrc(ws, 0, WS_BYTES, 0x00020000);
#define COMMON_SETUP() \
  lptr xrd[4], wr0[4], wr1[4]; \
  _Pragma("unroll") for (int s = 0; s < 4; ++s) { xrd[s] = lds + (t * 32 + r32) * 128 + (((2 * (s ^ rot) + hh) ^ ((r32 >> 1) & 7)) << 4); \
    wr0[s] = wrd0 + ((q4 >> 1) << 6) + (s ^ rot) * 2048; wr1[s] = wrd0 + ((1 ^ (q4 >> 1)) << 6) + (s ^ rot) * 2048; } \
  const int xwrap = (t >> 1) ? -NSLOT * SLOT : 0; \
  bf16x8 sel; { const short one = ((lane & 15) == ((lane >> 4) & 1)) ? (short)0x3f80 : (short)0; sel = (bf16x8){one, one, one, one, one, one, one, one}; } \
  bf16x8 xa[28]; float rs, nmb
#define GET_TAG() PIN(tagraw); const u64 tag = (((u64)(unsigned)__builtin_amdgcn_readfirstlane((unsigned)(tagraw >> 32)) << 32) | (unsigned)__builtin_amdgcn_readfirstlane((unsigned)tagraw)) + 1

  if (c == 0) {
    unsigned cpf0;
    asm volatile("global_load_dword %0, %1, %2" : "=v"(cpf0) : "v"(min((unsigned)(bi * 16 + (tid & 15)) * 128u, (unsigned)CODE_BYTES - 128u)), "s"(pc) : "memory");
    unsigned tch = 0;
    u64 tagraw = __hip_atomic_load((GAS u64*)(ws + WS_EPOCH), RLX_AGENT);
    if (tid < N_EV) *(LAS unsigned*)(lds + L_CNT + 4 * tid) = 0u;
    BAR();
    const bool isK = j < 7, isV = j >= 7 && j < 14;
    bf16x8 qf[4][2]; u64 tag;
    {
      COMMON_SETUP();
      unsigned ad[4]; bool strad[4] = {false, false, false, false};
      if (isV) { const int rho = r32 >> 2, col0 = (j - 7) * 64 + 4 * ((rho & 4) + 2 * (rho & 1) + ((rho >> 1) & 1)) + (r32 & 3);
#pragma unroll
        for (int cc = 0; cc < 2; ++cc) { const int col = col0 + cc * 32, hd = col / 28, d = col - hd * 28;
          ad[cc] = WS_KV + hd * 262144 + rt * 4096 + 2048 + (hh * 32 + d) * 16; ad[2 + cc] = d >= 24 ? ad[cc] + 64 : 0x80000000u; strad[cc] = d == 24; }
      } else if (isK) {
#pragma unroll
        for (int s = 0; s < 4; ++s) { const int P = j * 8 + 2 * s + hh, hp = P / 7, rr = P - hp * 7, hd = 2 * hp + (rr > 3), sg = rr > 3 ? rr - 4 : rr;
          ad[s] = WS_KV + hd * 262144 + rt * 4096 + sg * 512 + r32 * 16; strad[s] = rr == 3; }
      } else {
#pragma unroll
        for (int s = 0; s < 4; ++s) ad[s] = WS_H + rt * 114688 + (j - 14) * 4096 + s * 1024 + lane * 16;
      }
      PIN(ad[0]); PIN(ad[1]); PIN(ad[2]); PIN(ad[3]);
      asm volatile("s_waitcnt vmcnt(0)" : "+v"(cpf0), "+v"(tch), "+v"(tagraw) :: "memory");
      *(LAS u64*)(lds + L_TAG) = tagraw;
      tag = (((u64)(unsigned)__builtin_amdgcn_readfirstlane((unsigned)(tagraw >> 32)) << 32) | (unsigned)__builtin_amdgcn_readfirstlane((unsigned)tagraw)) + 1;
      P1R r = {}; float cr0[16], cr1[16], cl0, cl1;
      { P1A p; p.xwrap = xwrap; p.ev = ev; WF f[4];
#pragma unroll
        for (int s = 0; s < 4; ++s) { p.x[s] = xrd[s]; p.w0[s] = wr0[s]; p.w1[s] = wr1[s]; }
        const lptr csw = lds + L_CS0 + lane * 16 + t * 4;
#define GS(st) do { r.s2 = MFMA32(xa[st], xa[st], r.s2); PIN(r.s2); r.s1 = MFMA16(sel, xa[st], r.s1); PIN(r.s1); SBAR(); } while (0)
#define MA(VT, st) MM(VT, r.a0, xa[st], cat(f[st & 3].l0, f[st & 3].h0))
#define MB(VT, st) MM(VT, r.a1, xa[st], cat(f[st & 3].l1, f[st & 3].h1))
#define WRD(s) do { wf_read(f[s], p.w0[s] + SLOT, p.w1[s] + SLOT); SBAR(); } while (0)
#define P1_TAIL(VT) do { \
        p1_compute<VT, 0>(xa, f, r, sel, p STA); \
        __builtin_amdgcn_s_setprio(3); \
        _Pragma("unroll") for (int s = 0; s < 4; ++s) xa[24 + s] = ldsb128(p.x[s] + 6 * SLOT); \
        SBAR(); GS(22); WRD(0); GS(23); WRD(1); GS(24); \
        r.c0 = MFMA16(sel, cat(f[0].l0, f[0].h0), r.c0); r.c1 = MFMA16(sel, cat(f[0].l1, f[0].h1), r.c1); PIN(r.c0); PIN(r.c1); SBAR(); \
        GS(25); GS(26); \
        if (lane < 16) { *(LAS float*)csw = r.c0[0]; *(LAS float*)(csw + 256) = r.c0[1]; *(LAS float*)(csw + 512) = r.c1[0]; *(LAS float*)(csw + 768) = r.c1[1]; } \
        post<A_CSX>(ev); SBAR(); \
        GS(27); MA(VT, 22); MB(VT, 22); WRD(2); MA(VT, 23); \
        if (lane < 16) { *(LAS float*)(priv + lane * 4) = r.s1[0]; *(LAS float*)(priv + 64 + lane * 4) = r.s1[1]; } \
        const float sx_ = *(LAS float*)(priv + r32 * 4); SBAR(); \
        MB(VT, 23); WRD(3); post<A_CS0>(ev); MA(VT, 24); \
        const float sq_ = gram_diag(r.s2); SBAR(); \
        MB(VT, 24); \
        pwait<A_CSX>(ev); \
        const f32x4 p0_ = ldsf4(lds + L_CS0 + r32 * 16), p1_ = ldsf4(lds + L_CS0 + 512 + r32 * 16); SBAR(); \
        MA(VT, 25); \
        row_norm(sx_, sq_, rs, nmb); PIN(rs); PIN(nmb); SBAR(); \
        MB(VT, 25); \
        cl0 = sc1(sc1(p0_[0] + p0_[1]) + sc1(p0_[2] + p0_[3])); cl1 = sc1(sc1(p1_[0] + p1_[1]) + sc1(p1_[2] + p1_[3])); \
        *(LAS float*)(lds + L_STAT + t * 256 + lane * 4) = hh ? nmb : rs; post<A_ST>(ev); SBAR();     \
        MA(VT, 26); \
        if constexpr (!VT) { lane_to_regs(priv, r32, hh, cl0, cr0); } SBAR(); \
        MA(VT, 27); \
        if constexpr (!VT) { lane_to_regs(priv + 128, r32, hh, cl1, cr1); } SBAR(); \
        MB(VT, 26); MB(VT, 27); } while (0)
        if (isV) P1_TAIL(true); else P1_TAIL(false); }
      STAMP(1);
      u32x4 o[4];
      if (isV) {
#pragma unroll
        for (int i = 0; i < 4; ++i) { const f32x4 rr = ldsf4(lds + L_STAT + t * 256 + (8 * i + 4 * hh) * 4), mm = ldsf4(lds + L_STAT + t * 256 + 128 + (8 * i + 4 * hh) * 4);
#pragma unroll
          for (int e = 0; e < 2; ++e) {
            const int n = 4 * i + 2 * e;
            o[i >> 1][2 * (i & 1) + e] = cvtpk(sc1(sc1(__builtin_fmaf(mm[2 * e], cl0, r.a0[n])) * rr[2 * e]), sc1(sc1(__builtin_fmaf(mm[2 * e + 1], cl0, r.a0[n + 1])) * rr[2 * e + 1]));
            o[2 + (i >> 1)][2 * (i & 1) + e] = cvtpk(sc1(sc1(__builtin_fmaf(mm[2 * e], cl1, r.a1[n])) * rr[2 * e]), sc1(sc1(__builtin_fmaf(mm[2 * e + 1], cl1, r.a1[n + 1])) * rr[2 * e + 1])); } }
        ST16(o[0], ad[0]); ST16(o[1], ad[0] + 1024); ST16(o[2], ad[1]); ST16(o[3], ad[1] + 1024);
#pragma unroll
        for (int cc = 0; cc < 2; ++cc) { const unsigned e = strad[cc] ? 0x3f803f80u : 0u; const u32x4 pad = {e, e, e, e}; ST16(pad, ad[2 + cc]); ST16(pad, ad[2 + cc] + 1024); }
      } else {
        if (isK) {
          epi_T<false>(r.a0, cr0, rs, nmb, o[0], o[1]);
#pragma unroll
          for (int s = 0; s < 2; ++s) { u32x4 v = o[s]; if (strad[s]) { v[2] = 0x3f803f80u; v[3] = 0x00003f80u; } ST16(v, ad[s]); }
          epi_T<false>(r.a1, cr1, rs, nmb, o[2], o[3]);
#pragma unroll
          for (int s = 2; s < 4; ++s) { u32x4 v = o[s]; if (strad[s]) { v[2] = 0x3f803f80u; v[3] = 0x00003f80u; } ST16(v, ad[s]); }
#pragma unroll
          for (int s = 0; s < 4; ++s) if (strad[s]) { const u32x4 e = {o[s][2], o[s][3], 0x3f803f80u, 0x00003f80u}; ST16(e, ad[s] + 262144); }
        } else {
          epi_T<true>(r.a0, cr0, rs, nmb, o[0], o[1]); epi_T<true>(r.a1, cr1, rs, nmb, o[2], o[3]);
#pragma unroll
          for (int s = 0; s < 4; ++s) ST16(o[s], ad[s]);
        }
      }
      STAMP(13);
      if (j >= 6) {
        pwait<A_CS0>(ev); pwait<L_X6>(ev);
        const unsigned vo = (16 * t + (lane >> 3)) * 3584 + (((lane & 7) ^ (((lane >> 4) & 1) << 2)) << 4);
#pragma unroll
        for (int i = 0; i < 14; ++i) { const int c7 = (i >> 1) + r7 >= 7 ? (i >> 1) + r7 - 7 : (i >> 1) + r7;
          glds16(w1 + (12 + j) * 128 + (c7 * 64 + (i & 1) * 8) * 3584, vo, lds0 + t * 2048 + p1_slot(32 + (i >> 1)) + (i & 1) * 1024); }
        asm volatile("s_waitcnt vmcnt(14)" ::: "memory");
      } else WAIT_VM(0);
      if (cnt_add(lds, C_KV) == 3u && lane == 0) __hip_atomic_store((GAS u64*)(ws + WS_FKV + fme), tag, RLX_AGENT);
      if (cnt_add(lds, C_H) == 7u && lane == 0) __hip_atomic_store((GAS u64*)(ws + WS_FH + fme), tag, RLX_AGENT);
      STAMP(2);
      __builtin_amdgcn_s_setprio(QPRIO);
      f32x16 a0, a1; f32x4 c0;
      const int rho = 4 * g16 + p4, sg = 2 * (rho >> 2) + (rho & 1), half = (rho >> 1) & 1;
      const int g = (j & 1) ? (sg < 3 ? 2 * sg + 1 + half : (half ? 7 : 0)) : 2 * sg + half;
      lptr qb[4];
#pragma unroll
      for (int s = 0; s < 4; ++s) { qb[s] = lds + L_WQ + (8 * hh + q4) * 64 + g * 8 + (s ^ rot) * 1024; PIN(qb[s]); }
      a0 = (f32x16){}; a1 = (f32x16){}; c0 = (f32x4){};
      pwait<L_WQL>(ev);
      s16x4 ql[6], qh[6];
#pragma unroll
      for (int st = 0; st < 4; ++st) { ql[st] = trd(qb[st]); qh[st] = trd(qb[st] + 256); }
#pragma unroll
      for (int st = 0; st < 28; ++st) {
        if (st == QSPLIT) {
          WAIT_VM(0); post<A_FB>(ev); }
        SBAR();
        if (st & 1) { a1 = MFMA32(cat(ql[st % 6], qh[st % 6]), xa[st], a1); PIN(a1); } else { a0 = MFMA32(cat(ql[st % 6], qh[st % 6]), xa[st], a0); PIN(a0); }
        SBAR();
        if (st + 4 < 28) { const int o_ = ((st + 4) >> 2) * 4096; ql[(st + 4) % 6] = trd(qb[st & 3] + o_); qh[(st + 4) % 6] = trd(qb[st & 3] + o_ + 256); }
        SBAR();
        c0 = MFMA16(sel, cat(ql[st % 6], qh[st % 6]), c0); PIN(c0);
      }
#pragma unroll
      for (int i = 0; i < 16; ++i) a0[i] = sc1(a0[i] + a1[i]);
      if (lane < 16) { *(LAS float*)(priv + lane * 4) = c0[0]; *(LAS float*)(priv + 64 + lane * 4) = c0[1]; }
      float cr[16];
#pragma unroll
      for (int i = 0; i < 4; ++i) { const f32x4 q = ldsf4(priv + (8 * i + 4 * hh) * 4); cr[4 * i] = q[0]; cr[4 * i + 1] = q[1]; cr[4 * i + 2] = q[2]; cr[4 * i + 3] = q[3]; }
      epi_T<false>(a0, cr, rs * QSCALE, nmb, o[0], o[1]);
      if (hh) { o[1][2] = 0u; o[1][3] = 0u; }
      *(LAS u32x4*)(lds + L_QX + t * 2048 + lane * 16) = o[0]; *(LAS u32x4*)(lds + L_QX + t * 2048 + 1024 + lane * 16) = o[1];
    }
    post<A_QW>(ev);
    const f32x16 zero = {};
    f32x16 o[4]; u32x4 kv[4][4], pA0 = {}, pA1 = {}, pB0 = {}, pB1 = {};
#pragma unroll
    for (int q = 0; q < 4; ++q) { o[q] = zero; PIN(o[q]); }
    kv[3][2] = (u32x4){}; kv[3][3] = (u32x4){}; PIN(kv[3][2]); PIN(kv[3][3]); PIN(pB0); PIN(pB1);
    STAMP(4);
    { const int c0 = (28 * j) >> 6, c1 = (28 * j + 27) >> 6, sel4 = lane >> 4;
      int jj = sel4 == 0 ? c0 : sel4 == 1 ? c1 : sel4 == 2 ? 7 + c0 : 7 + c1;
      if (j == 0 && (sel4 & 1)) jj = 14 + (sel4 >> 1);
      poll_flags<0>(ws + WS_FKV + ((lane & 15) * 16 + jj) * 128, tag); }
    STAMP(5);
    const unsigned kvb = WS_KV + j * 262144 + t * 65536 + lane * 16;
#pragma unroll
    for (int u = 0; u < 3; ++u)
#pragma unroll
      for (int e = 0; e < 4; ++e) kv[u][e] = LD16(kvb + u * 4096 + e * 1024);
    pwait<A_QW>(ev);
#pragma unroll
    for (int q = 0; q < 4; ++q) { const lptr a = lds + L_QX + ((q + t + 1) & 3) * 2048 + lane * 16; qf[q][0] = ldsb128(a); qf[q][1] = ldsb128(a + 1024); }
    float mref[4]; bool redo;
#pragma unroll
    for (int q = 0; q < 4; ++q) {
      f32x16 s = MFMA32(asfrag(kv[0][0]), qf[q][0], zero); s = MFMA32(asfrag(kv[0][1]), qf[q][1], s);
      mref[q] = set_ref(qf[q][1], tile_max(s) + REF_MARGIN, hh);
    }
    redo = !(__builtin_fmaxf(__builtin_fmaxf(__builtin_fabsf(mref[0]), __builtin_fabsf(mref[1])), __builtin_fmaxf(__builtin_fabsf(mref[2]), __builtin_fabsf(mref[3]))) < REF_LIMIT);
    __builtin_amdgcn_s_setprio(LPRIO);
    f32x16 sA, sB;
    sA = MFMA32(asfrag(kv[0][0]), qf[0][0], zero); sA = MFMA32(asfrag(kv[0][1]), qf[0][1], sA);
    PIN(sA); STAMP(6);
    auto area = [&](int dst, int e) -> lptr { const int n = dst * 3 + e; return lds + (n < 10 ? 8 * SLOT + n * 4096 : (n - 10) * 4096) + lane * 16; };
    auto put_o = [&](const f32x16& v, float m, int q) { const lptr a = area((q + t + 1) & 3, q);
      *(LAS f32x4*)a = (f32x4){v[0], v[1], v[2], v[3]}; *(LAS f32x4*)(a + 1024) = (f32x4){v[4], v[5], v[6], v[7]};
      *(LAS f32x4*)(a + 2048) = (f32x4){v[8], v[9], v[10], v[11]}; *(LAS f32x4*)(a + 3072) = (f32x4){v[12], hh ? m : v[13], v[14], v[15]}; };
#define PUT_O(q) put_o(o[q], mref[q], q)
#define EX4(S, i) do { S[i] = __builtin_amdgcn_exp2f(S[i]); S[i + 1] = __builtin_amdgcn_exp2f(S[i + 1]); S[i + 2] = __builtin_amdgcn_exp2f(S[i + 2]); S[i + 3] = __builtin_amdgcn_exp2f(S[i + 3]); } while (0)
#define UNIT(SC, SN, PC0, PC1, PP0, PP1, KN0, KN1, QN, QP, VV0, VV1, EXTRA) do { \
      SBAR(); if (QN >= 0) { SN = MFMA32(asfrag(KN0), qf[QN & 3][0], zero); PIN(SN); } SBAR(); EX4(SC, 0); PIN(SC); SBAR(); \
      if (QN >= 0) { SN = MFMA32(asfrag(KN1), qf[QN & 3][1], SN); PIN(SN); } SBAR(); EX4(SC, 4); PIN(SC); SBAR(); \
      o[QP] = MFMA32(asfrag(VV0), asfrag(PP0), o[QP]); PIN(o[QP]); SBAR(); EXTRA; EX4(SC, 8); PIN(SC); SBAR(); \
      o[QP] = MFMA32(asfrag(VV1), asfrag(PP1), o[QP]); PIN(o[QP]); SBAR(); EX4(SC, 12); SBAR(); \
      PC0 = (u32x4){cvtpk(SC[0], SC[1]), cvtpk(SC[2], SC[3]), cvtpk(SC[4], SC[5]), cvtpk(SC[6], SC[7])}; \
      PC1 = (u32x4){cvtpk(SC[8], SC[9]), cvtpk(SC[10], SC[11]), cvtpk(SC[12], SC[13]), cvtpk(SC[14], SC[15])}; PIN(PC0); PIN(PC1); SBAR(); } while (0)
#define LDKV(UL, e) kv[UL][e] = __builtin_amdgcn_raw_buffer_load_b128(rsw, kvb + (e) * 1024, nt, LDAUX)
#define KEYTILE(U, UN, UL, LOAD, QLAST, X2, X3) do { const int nt = (kt + (U) + 3) * 4096; \
      UNIT(sA, sB, pA0, pA1, pB0, pB1, kv[U][0], kv[U][1], 1, 3, kv[UL][2], kv[UL][3], if (LOAD) { LDKV(UL, 0); LDKV(UL, 1); }); \
      UNIT(sB, sA, pB0, pB1, pA0, pA1, kv[U][0], kv[U][1], 2, 0, kv[U][2], kv[U][3], if (LOAD) { LDKV(UL, 2); LDKV(UL, 3); }); \
      UNIT(sA, sB, pA0, pA1, pB0, pB1, kv[U][0], kv[U][1], 3, 1, kv[U][2], kv[U][3], X2); \
      UNIT(sB, sA, pB0, pB1, pA0, pA1, kv[UN][0], kv[UN][1], QLAST, 2, kv[U][2], kv[U][3], X3); \
    } while (0)
    for (int kt = 0; kt < 12; kt += 4) {
      LSTAMP(16 + (kt >> 1)); KEYTILE(0, 1, 3, true, 0, , ); KEYTILE(1, 2, 0, true, 0, , ); LSTAMP(17 + (kt >> 1)); KEYTILE(2, 3, 1, true, 0, , ); KEYTILE(3, 0, 2, true, 0, , );
      const float lm = __builtin_fmaxf(__builtin_fmaxf(o[0][12], o[1][12]), __builtin_fmaxf(o[2][12], o[3][12]));
      if (!NOCHECK && __builtin_expect((unsigned)(__ballot(lm > 1.099511627776e12f) >> 32) != 0u, 0)) {
        NRESC;
        o[3] = MFMA32(asfrag(kv[3][2]), asfrag(pB0), o[3]); o[3] = MFMA32(asfrag(kv[3][3]), asfrag(pB1), o[3]); pB0 = (u32x4){}; pB1 = (u32x4){};
#pragma unroll
        for (int q = 0; q < 4; ++q) { const float l = upper_bcast(o[q][12]); redo |= !(l < 1.2980742146337069e33f);
          const float mn = set_ref(qf[q][1], mref[q] + (float)max(__builtin_amdgcn_frexp_expf(l) + (int)REF_MARGIN, 0), hh);
          const int e = (int)(mn - mref[q]); mref[q] = mn; redo |= !(__builtin_fabsf(mn) < REF_LIMIT);
#pragma unroll
          for (int r = 0; r < 16; ++r) o[q][r] = __builtin_ldexpf(o[q][r], -e);
          if (q == 0) {
#pragma unroll
            for (int r = 0; r < 16; ++r) sA[r] -= (float)e; } } }
    }
    { const int kt = 12;
      if (__builtin_expect(__any(redo), 0)) { mref[0] = mref[1] = mref[2] = mref[3] = __builtin_nanf(""); }
      pwait<L_CSBP>(ev);
      LSTAMP(22); KEYTILE(0, 1, 3, true, 0, , ); KEYTILE(1, 2, 0, false, 0, , ); LSTAMP(23); KEYTILE(2, 3, 1, false, 0, , );
      post<A_DONE>(ev); __builtin_amdgcn_s_setprio(3);
      KEYTILE(3, 0, 2, false, -1, PUT_O(0), PUT_O(1)); }
    o[3] = MFMA32(asfrag(kv[3][2]), asfrag(pB0), o[3]); o[3] = MFMA32(asfrag(kv[3][3]), asfrag(pB1), o[3]); PIN(o[3]); SBAR();
    PUT_O(2);
    STAMP(7);
    VSTAMP(28, nresc + (__any(redo) ? 1000 : 0));
    auto combine = [&]() -> bool {
      f32x4 pp[3][4]; float ms[3]; f32x16 of;
#pragma unroll
      for (int e = 0; e < 3; ++e) { const lptr a = area(t, e); pp[e][3] = ldsf4(a + 3072); }
#pragma unroll
      for (int e = 0; e < 3; ++e) { const lptr a = area(t, e);
#pragma unroll
        for (int i = 0; i < 3; ++i) pp[e][i] = ldsf4(a + i * 1024);
        ms[e] = upper_bcast(pp[e][3][1]); }
      const float mx = __builtin_fmaxf(__builtin_fmaxf(mref[3], ms[0]), __builtin_fmaxf(ms[1], ms[2])) + 2.f;
      float f0 = __builtin_amdgcn_exp2f(mref[3] - mx), f1 = __builtin_amdgcn_exp2f(ms[0] - mx), f2 = __builtin_amdgcn_exp2f(ms[1] - mx), f3 = __builtin_amdgcn_exp2f(ms[2] - mx);
      const float l = upper_bcast(__builtin_fmaf(pp[2][3][0], f3, __builtin_fmaf(pp[1][3][0], f2, __builtin_fmaf(pp[0][3][0], f1, o[3][12] * f0))));
      const float inv = __builtin_amdgcn_rcpf(l);
      f0 = sc1(f0 * inv); f1 = sc1(f1 * inv); f2 = sc1(f2 * inv); f3 = sc1(f3 * inv);
#pragma unroll
      for (int r = 0; r < 16; ++r) of[r] = sc1(__builtin_fmaf(pp[2][r >> 2][r & 3], f3, sc1(__builtin_fmaf(pp[1][r >> 2][r & 3], f2, sc1(__builtin_fmaf(pp[0][r >> 2][r & 3], f1, sc1(o[3][r] * f0)))))));
      u32x4 e0, e1;
#pragma unroll
      for (int p = 0; p < 4; ++p) { e0[p] = cvtpk(of[2 * p], of[2 * p + 1]); e1[p] = cvtpk(of[8 + 2 * p], of[9 + 2 * p]); }
      if (hh) { e1[2] = 0u; e1[3] = 0u; }
      const unsigned a = WS_O + rt * 32768 + j * 2048 + lane * 16;
      ST16(e0, a); ST16(e1, a + 1024);
      unsigned ex = 0;
#pragma unroll
      for (int p = 0; p < 4; ++p) ex |= ((e0[p] & 0x7f807f80u) + 0x00800080u) | ((e1[p] & 0x7f807f80u) + 0x00800080u);
      return __any((ex & 0x80008000u) != 0u || !(l < 1.2676506002282294e30f));
    };
    post<A_CMB>(ev); STAMP(9); pwait<A_CMB>(ev);
    if (__builtin_expect(combine(), 0)) *(LAS unsigned*)(lds + L_CNT + 4 * C_BAD) = 1u;
    STAMP(10);
    WAIT_VM(0);
    const unsigned arr = cnt_add(lds, C_O);
    __builtin_amdgcn_s_setprio(0);
    if (arr != 3u) pwait_long<C_O>(ev);
    const bool bad = __builtin_amdgcn_readfirstlane(*(volatile LAS unsigned*)(lds + L_CNT + 4 * C_BAD)) != 0u;
    if (!bad && arr == 3u && lane == 0) __hip_atomic_store((GAS u64*)(ws + WS_FO + fme), tag, RLX_AGENT);
    if (__builtin_expect(bad, 0)) {
#pragma unroll
      for (int q = 0; q < 4; ++q) { set_ref(qf[q][1], 0.f, hh); o[q] = zero; mref[q] = -3.0e38f; }
#pragma nounroll
      for (int kt = 0; kt < 16; ++kt) {
        const u32x4 k0 = LD16(kvb + kt * 4096), k1 = LD16(kvb + kt * 4096 + 1024), v0 = LD16(kvb + kt * 4096 + 2048), v1 = LD16(kvb + kt * 4096 + 3072);
#pragma unroll
        for (int q = 0; q < 4; ++q) {
          f32x16 s = MFMA32(asfrag(k0), qf[q][0], zero); s = MFMA32(asfrag(k1), qf[q][1], s);
          const float mn = __builtin_fmaxf(mref[q], tile_max(s)), al = __builtin_amdgcn_exp2f(mref[q] - mn); mref[q] = mn;
#pragma unroll
          for (int r = 0; r < 16; ++r) { o[q][r] *= al; s[r] = __builtin_amdgcn_exp2f(s[r] - mn); }
          const u32x4 p0 = {cvtpk(s[0], s[1]), cvtpk(s[2], s[3]), cvtpk(s[4], s[5]), cvtpk(s[6], s[7])}, p1 = {cvtpk(s[8], s[9]), cvtpk(s[10], s[11]), cvtpk(s[12], s[13]), cvtpk(s[14], s[15])};
          o[q] = MFMA32(asfrag(v0), asfrag(p0), o[q]); o[q] = MFMA32(asfrag(v1), asfrag(p1), o[q]);
        }
      }
      PUT_O(0); PUT_O(1); PUT_O(2);
      post<A_CMB2>(ev); pwait<A_CMB2>(ev);
      combine();
      WAIT_VM(0);
      if (cnt_add(lds, C_O2) == 3u && lane == 0) __hip_atomic_store((GAS u64*)(ws + WS_FO + fme), tag, RLX_AGENT);
    }
    STAMP(8); CSTAMP(31); STAMP_OUT();
  } else {
    Ld k;
    k.xsrc = x + (rb * 128 + t * 32) * 896; k.xvo0 = (lane >> 3) * 896 + (((lane & 7) ^ (lane >> 4)) << 4); k.xvo1 = k.xvo0 ^ 64u;
    k.xdst = lds0 + t * 4096; k.xdst6 = lds0 + (t < 2 ? 18 * SLOT + t * 4096 : (t - 2) * 4096);
#pragma unroll
    for (int i = 0; i < 7; ++i) k.ck[i] = i + r7 >= 7 ? i + r7 - 7 : i + r7;
    { const int row = 16 * t + (lane >> 3); const unsigned piece = ((lane & 7) ^ (((lane >> 4) & 1) << 2)) << 4;
      k.w0rs = j < 14 ? 2688u : 3584u; k.w0src = j < 14 ? wqkv + 896 + j * 128 : w1 + (j - 14) * 128;
      k.fasrc = w1 + (2 + j) * 128; k.dst = lds0 + t * 2048;
      k.w0vo = row * k.w0rs + piece; k.wfvo = row * 3584 + piece;
      k.wqsrc = wqkv + j * 56; k.wqvo = (16 * t + (lane >> 2)) * 2688 + (lane & 3) * 16; k.qdst = lds0 + L_WQ + t * 1024; k.ev = ev; }
    {
      COMMON_SETUP();
      STAMP(14);
      p1_stream<0>(k, xa, xrd, xwrap STA);
      STAMP(15);
      __builtin_amdgcn_s_setprio(GPRIO);
#pragma unroll
      for (int s = 0; s < 4; ++s) { wr0[s] += L_FA; wr1[s] += L_FA; PIN(wr0[s]); PIN(wr1[s]); }
      WF f[3]; f32x16 a0 = {}, a1 = {}; f32x4 c0 = {}, c1 = {}; float cr[16]; u32x4 o[2];
      pwait<L_FAL>(ev);
      wf_read(f[0], wr0[0], wr1[0]); wf_read(f[1], wr0[1], wr1[1]);
      f_steps<0, 0>(ev, xa, f, a0, a1, c0, c1, sel, wr0, wr1);
      cs_put(lds, L_CSA, 0, t, lane, c0); cs_put(lds, L_CSA, 1, t, lane, c1);
      pwait<A_ST>(ev);
      rs = *(LAS float*)(lds + L_STAT + t * 256 + r32 * 4); nmb = *(LAS float*)(lds + L_STAT + t * 256 + 128 + r32 * 4);
      post<L_CSAP>(ev); pwait<L_CSAP>(ev);
      STAMP(2);
      __builtin_amdgcn_s_setprio(3);
      { const unsigned ad = WS_H + rt * 114688 + (2 + j) * 4096 + lane * 16;
        lane_to_regs(priv, r32, hh, cs_get(lds, L_CSA, 0, r32), cr); epi_T<true>(a0, cr, rs, nmb, o[0], o[1]); ST16(o[0], ad); ST16(o[1], ad + 1024);
        lane_to_regs(priv + 128, r32, hh, cs_get(lds, L_CSA, 1, r32), cr); epi_T<true>(a1, cr, rs, nmb, o[0], o[1]); ST16(o[0], ad + 2048); ST16(o[1], ad + 3072); }
      STAMP(3);
      if (j >= 6) {
#pragma unroll
        for (int s = 0; s < 4; ++s) { wr0[s] += L_FB - L_FA; wr1[s] += L_FB - L_FA; PIN(wr0[s]); PIN(wr1[s]); }
        a0 = (f32x16){}; a1 = (f32x16){}; c0 = (f32x4){}; c1 = (f32x4){};
        pwait<A_FB>(ev);
        wf_read(f[0], wr0[0], wr1[0]); wf_read(f[1], wr0[1], wr1[1]);
        f_steps<1, 0>(ev, xa, f, a0, a1, c0, c1, sel, wr0, wr1);
        cs_put(lds, L_CSB, 0, t, lane, c0); cs_put(lds, L_CSB, 1, t, lane, c1);
        post<L_CSBP>(ev); pwait<L_CSBP>(ev);
        const unsigned ad = WS_H + rt * 114688 + (12 + j) * 4096 + lane * 16;
        lane_to_regs(priv, r32, hh, cs_get(lds, L_CSB, 0, r32), cr); epi_T<true>(a0, cr, rs, nmb, o[0], o[1]); ST16(o[0], ad); ST16(o[1], ad + 1024);
        lane_to_regs(priv + 128, r32, hh, cs_get(lds, L_CSB, 1, r32), cr); epi_T<true>(a1, cr, rs, nmb, o[0], o[1]); ST16(o[0], ad + 2048); ST16(o[1], ad + 3072);
      } else post<L_CSBP>(ev);
    }
    __builtin_amdgcn_s_setprio(0);
    u64 tagraw = *(LAS u64*)(lds + L_TAG);
    GET_TAG();
    WAIT_VM(0);
    if (cnt_add(lds, C_H) == 7u && lane == 0) __hip_atomic_store((GAS u64*)(ws + WS_FH + fme), tag, RLX_AGENT);
    STAMP(4);
    const int cg = bi >> 2;
    if (cg < 7) {
      const int rt0 = 8 * xg + 2 * (bi & 3), rb3 = rt0 >> 2;
      const int ring = ring_of(t);
      const int ct = t >> 1, rtl = t & 1;
      G2 g; g.ev = ev; g.t = t; g.ringdst = lds0 + ring; g.lv = lane * 16;
      g.w2vo = (lane >> 3) * 896 + (((lane & 7) ^ (((lane >> 4) & 1) << 2)) << 4);
      g.w2s = w2 + t * 448 * 896 + cg * 128; g.wos = wo + t * 112 * 896 + cg * 128;
      g.hb0 = ws + WS_H + (rt0 + rtl) * 114688 + t * 28672; g.hb1 = ws + WS_H + (rt0 + (rtl ^ 1)) * 114688 + t * 28672;
      g.br0 = wrd0 + ring + ((ct ^ (q4 >> 1)) << 6); g.br1 = wrd0 + ring + ((ct ^ 1 ^ (q4 >> 1)) << 6);
      g2_ws<0, 12>(g);
      const int fb3 = rb3 * 16 + (lane & 15);
      __builtin_amdgcn_s_setprio(0);
      poll_flags<8>(ws + WS_FH + fb3 * 128, tag);
      __builtin_amdgcn_s_setprio(G2PRIO);
      WAIT_VM(0);
      STAMP(5);
      u32x4 hf[14][2];
#pragma unroll
      for (int s = 0; s < 14; ++s) { ldg_sc1(hf[s][0], g.hb0 + s * 1024, g.lv); ldg_sc1(hf[s][1], g.hb1 + s * 1024, g.lv); }
      f32x16 a00 = {}, a01 = {}, a10 = {}, a11 = {};
      PIN(g.br0); PIN(g.br1);
      { WF f[3]; wf_read(f[0], g.br0, g.br1); wf_read(f[1], g.br0 + 2048, g.br1 + 2048);
        g2_steps<0>(g, hf, f, a00, a01, a10, a11); }
      STAMP(6);
      WAIT_VM(0); post<L_WO>(ev);
      const size_t oo = (size_t)((rt0 + rtl) * 32 + r32) * 896 + cg * 128 + ct * 64 + hh * 16;
      const u32x4 xr0 = *(const GAS u32x4*)(x + oo), xr1 = *(const GAS u32x4*)(x + oo + 32);
      pwait<L_WO>(ev);
      bf16x8 wf[16][2];
#pragma unroll
      for (int g2 = 0; g2 < 2; ++g2) { const lptr o0 = lds + ring_of(2 * ct + g2) + SLOT + (4 * hh + q4) * 128 + pi_std * 8;
#pragma unroll
        for (int e = 0; e < 8; ++e)
#pragma unroll
          for (int cc = 0; cc < 2; ++cc) wf[8 * g2 + e][cc] = trfrag(o0 + ((cc ^ ct ^ (q4 >> 1)) << 6) + ((e >> 1) * 28 + (e & 1) * 16) * 128, 1024); }
      post<L_WOR>(ev); pwait<L_WOR>(ev);
      auto put = [&](const f32x16& a, int e) { const lptr m = lds + ring + e * 4096 + lane * 16;
#pragma unroll
        for (int i = 0; i < 4; ++i) *(LAS f32x4*)(m + i * 1024) = (f32x4){a[4 * i], a[4 * i + 1], a[4 * i + 2], a[4 * i + 3]}; };
      auto add = [&](f32x16& a, int src, int e) { const lptr m = lds + ring_of(src) + e * 4096 + lane * 16;
#pragma unroll
        for (int i = 0; i < 4; ++i) { const f32x4 v = ldsf4(m + i * 1024);
#pragma unroll
          for (int n = 0; n < 4; ++n) a[4 * i + n] = sc1(a[4 * i + n] + v[n]); } };
      put(a11, 3); put(a10, 2); put(a01, 1);
      post<L_RED>(ev); pwait<L_RED>(ev);
      add(a00, t ^ 1, 1); add(a00, t ^ 2, 2); add(a00, t ^ 3, 3);
#pragma unroll
      for (int p = 0; p < 4; ++p) { a00[2 * p] = sc1(a00[2 * p] + bflo(xr0[p])); a00[2 * p + 1] = sc1(a00[2 * p + 1] + bfhi(xr0[p]));
        a00[8 + 2 * p] = sc1(a00[8 + 2 * p] + bflo(xr1[p])); a00[9 + 2 * p] = sc1(a00[9 + 2 * p] + bfhi(xr1[p])); }
      a10 = (f32x16){}; PIN(a00); PIN(a10);
      STAMP(11);
      { const char* fl = ws + WS_FO + (rb3 * 16 + ct * 8 + (lane & 7)) * 128; const unsigned ob = WS_O + (rt0 + rtl) * 32768 + ct * 16384 + lane * 16;
        unsigned ready = 0; u32x4 of[8][2];
#pragma unroll
        for (int h = 0; h < 8 + OLAG; ++h) {
          if (h < 8) {
            if (__builtin_expect(!((ready >> h) & 1u), 0)) ready = poll_bit(fl, tag, h);
            if (h == 7) STAMP(7);
            of[h][0] = LD16(ob + h * 2048); of[h][1] = LD16(ob + h * 2048 + 1024); }
          if (h >= OLAG) {
#pragma unroll
            for (int s = 0; s < 2; ++s) { const bf16x8 o_ = asfrag(of[h - OLAG][s]); a10 = MFMA32(wf[2 * (h - OLAG) + s][1], o_, a10); a00 = MFMA32(wf[2 * (h - OLAG) + s][0], o_, a00); }
            PIN(a10); PIN(a00); }
          SBAR(); } }
      STAMP(9);
      put(a10, 0);
      post<L_FIN>(ev);
      STAMP(10); pwait<L_FIN>(ev);
      add(a00, t ^ 2, 0);
      u32x4 r0, r1;
#pragma unroll
      for (int p = 0; p < 4; ++p) { r0[p] = cvtpk(a00[2 * p], a00[2 * p + 1]); r1[p] = cvtpk(a00[8 + 2 * p], a00[9 + 2 * p]); }
      *(GAS u32x4*)(out + oo) = r0; *(GAS u32x4*)(out + oo + 32) = r1;
      if (b == 0 && tid == 256) __hip_atomic_store((GAS u64*)(ws + WS_EPOCH), tag, RLX_AGENT);
      if (tid == 256) __hip_atomic_store((GAS u64*)(ws + 0x40000 + blockIdx.x * 8), (u64)__builtin_amdgcn_s_memrealtime(), RLX_AGENT);
    }
    STAMP_OUT();
  }
}

__global__ void post_k(char* ws) { const u64 t0 = __builtin_amdgcn_s_memrealtime(); u64 mx = 0;
  for (int i = 0; i < 4; ++i) { const int b = threadIdx.x * 4 + i; const u64 v = (b >> 5) == 7 ? 0 : __hip_atomic_load((GAS u64*)(ws + 0x40000 + b * 8), RLX_AGENT); mx = v > mx ? v : mx; }
  for (int o = 32; o; o >>= 1) { const u64 c = __shfl_xor(mx, o); mx = c > mx ? c : mx; }
  u64 X = (t0 - mx) * MULT; X = X > 6000 ? 6000 : X; const u64 now = __builtin_amdgcn_s_memrealtime(); while (__builtin_amdgcn_s_memrealtime() - now < X) __builtin_amdgcn_s_sleep(2); }
extern "C" void kernel_launch(void* const* d_in, const int* in_sizes, int n_in, void* d_out, int out_size, void* d_ws, size_t ws_size, hipStream_t stream) {
  fused_block<<<256, 512, 0, stream>>>((const char*)d_in[0], (const char*)d_in[1], (const char*)d_in[2], (const char*)d_in[4], (const char*)d_in[5], (char*)d_out, (char*)d_ws + WS_BASE);
  post_k<<<1, 64, 0, stream>>>((char*)d_ws);
}
```

```cpp
#define METRIC 0
#include <hip/hip_runtime.h>
#include <stdint.h>

typedef short bf16x8 __attribute__((ext_vector_type(8)));
typedef short s16x4 __attribute__((ext_vector_type(4)));
typedef float f32x16 __attribute__((ext_vector_type(16)));
typedef float f32x4 __attribute__((ext_vector_type(4)));
typedef float f32x2 __attribute__((ext_vector_type(2)));
typedef __bf16 bf16x2v __attribute__((ext_vector_type(2)));
typedef unsigned u32x4 __attribute__((ext_vector_type(4)));
typedef unsigned long long u64;
#define LAS __attribute__((address_space(3)))
#define GAS __attribute__((address_space(1)))
#define DI __device__ __forceinline__
#define MFMA32(a, b, c) __builtin_amdgcn_mfma_f32_32x32x16_bf16(a, b, c, 0, 0, 0)
#define MFMA16(a, b, c) __builtin_amdgcn_mfma_f32_16x16x32_bf16(a, b, c, 0, 0, 0)
#define SBAR() __builtin_amdgcn_sched_barrier(0)
#define PIN(x) asm volatile("" : "+v"(x))
#define RLX_AGENT __ATOMIC_RELAXED, __HIP_MEMORY_SCOPE_AGENT
#define WAIT_BAR(N) asm volatile("s_waitcnt vmcnt(" #N ") lgkmcnt(0)\n\ts_barrier" ::: "memory")
#define WAIT_VM(N) asm volatile("s_waitcnt vmcnt(" #N ")" ::: "memory")
#define WAIT_LGKM0() asm volatile("s_waitcnt lgkmcnt(0)" ::: "memory")

constexpr unsigned WS_EPOCH = 0;
constexpr unsigned WS_FKV = 4096, WS_FH = WS_FKV + 32768, WS_FO = WS_FH + 32768;
constexpr unsigned WS_KV = 1u << 20;
constexpr unsigned WS_H = 5u << 20;
constexpr unsigned WS_O = 13u << 20;
constexpr unsigned WS_BYTES = 15u << 20;
constexpr int SLOT = 8192, NSLOT = 19;
constexpr int L_MISC = NSLOT * SLOT;
constexpr int L_STAT = L_MISC;
constexpr int L_CS0 = L_MISC + 2048, L_CSA = L_CS0 + 1024, L_CSB = L_CSA + 1024;
constexpr int L_PRIV = L_MISC + 5120;
constexpr int L_CNT = L_MISC + 7168;
constexpr int L_TAG = L_MISC + 7424;
constexpr int LDS_BYTES = 163840;
constexpr int L_FA = 2 * SLOT, L_WQ = 9 * SLOT, L_FB = 13 * SLOT, L_QX = SLOT;
constexpr int ring_of(int t) { return (t < 2 ? 2 + 3 * t : 7 + 3 * t) * SLOT; }
constexpr int p1_slot(int item) { return (item % NSLOT) * SLOT; }
enum { C_KV, C_H, C_O, C_O2, C_BAD,
       A_RD0, A_CS0 = A_RD0 + 6, A_CSX, A_ST, A_FB, A_QW, A_DONE, A_CMB, A_CMB2,
       L_LD0, L_X6 = L_LD0 + 7, L_FAL, L_WQL, L_CSAP, L_CSBP, L_WO, L_WOR, L_RED, L_FIN, N_EV };

#define WS_BASE 0
#define TOUCH 0
#define ROT7 0
#define MAPA 8
#define G2_SPLIT 14
#define CODE_BYTES 55296
constexpr float QSCALE = 0.18898223650461362f * 1.4426950408889634f;
constexpr float REF_MARGIN = 80.f;
constexpr float REF_LIMIT = 4096.f;
constexpr unsigned SPIN_CAP = 1u << 21;

DI unsigned cvtpk(float lo, float hi) { f32x2 v = {lo, hi}; return __builtin_bit_cast(unsigned, __builtin_convertvector(v, bf16x2v)); }
DI float sc1(float x) { asm("" : "+v"(x)); return x; }
DI float bf16r(float x) { return __uint_as_float(cvtpk(x, 0.f) << 16); }
DI float bflo(unsigned v) { return __uint_as_float(v << 16); }
DI float bfhi(unsigned v) { return __uint_as_float(v & 0xffff0000u); }
DI float swap_add(float v) { auto r = __builtin_amdgcn_permlane32_swap(__float_as_uint(v), __float_as_uint(v), false, false); return __uint_as_float(r[0]) + __uint_as_float(r[1]); }
DI float swap_max(float v) { auto r = __builtin_amdgcn_permlane32_swap(__float_as_uint(v), __float_as_uint(v), false, false); return __builtin_fmaxf(__uint_as_float(r[0]), __uint_as_float(r[1])); }
DI float upper_bcast(float v) { auto r = __builtin_amdgcn_permlane32_swap(__float_as_uint(v), __float_as_uint(v), false, false); return __uint_as_float(r[1]); }
DI void glds16(const void* sbase, unsigned voff, unsigned ldsdst) {
  unsigned keep;
  asm volatile("s_mov_b32 %0, m0\n\ts_mov_b32 m0, %3\n\ts_nop 0\n\tglobal_load_lds_dwordx4 %1, %2\n\ts_mov_b32 m0, %0"
               : "=&s"(keep) : "v"(voff), "s"(sbase), "s"(ldsdst) : "memory");
}
typedef LAS char* lptr;
typedef short v4i16_t __attribute__((ext_vector_type(4)));
DI s16x4 trd(lptr p) { return __builtin_bit_cast(s16x4, __builtin_amdgcn_ds_read_tr16_b64_v4i16((LAS v4i16_t*)p)); }
DI bf16x8 trfrag(lptr p, int second) { s16x4 lo = trd(p), hi = trd(p + second); return __builtin_shufflevector(lo, hi, 0, 1, 2, 3, 4, 5, 6, 7); }
DI bf16x8 ldsb128(lptr p) { return *(LAS bf16x8*)p; }
DI f32x4 ldsf4(lptr p) { return *(LAS f32x4*)p; }
DI bf16x8 asfrag(u32x4 v) { return __builtin_bit_cast(bf16x8, v); }
struct Ev { unsigned base, one; };
template <int E> DI void post(const Ev& e) {
  asm volatile("s_mov_b64 exec, 1\n\tds_add_u32 %0, %1 offset:%2\n\ts_mov_b64 exec, -1" :: "v"(e.base), "v"(e.one), "i"(4 * E) : "memory"); }
template <int E> DI void pwait(const Ev& e) {
  unsigned a;
  asm volatile("1:\n\tds_read_b32 %0, %1 offset:%2\n\ts_waitcnt lgkmcnt(0)\n\tv_cmp_gt_u32 vcc, 4, %0\n\ts_cbranch_vccnz 1b"
               : "=&v"(a) : "v"(e.base), "i"(4 * E) : "vcc", "memory"); }
template <int E> DI void pwait_long(const Ev& e) {
  unsigned a;
  asm volatile("1:\n\ts_sleep 8\n\tds_read_b32 %0, %1 offset:%2\n\ts_waitcnt lgkmcnt(0)\n\tv_cmp_gt_u32 vcc, 4, %0\n\ts_cbranch_vccnz 1b"
               : "=&v"(a) : "v"(e.base), "i"(4 * E) : "vcc", "memory"); }
DI unsigned cnt_add(lptr lds, int c) { unsigned r = 0; if ((threadIdx.x & 63) == 0) r = __hip_atomic_fetch_add((LAS unsigned*)(lds + L_CNT + 4 * c), 1u, __ATOMIC_RELAXED, __HIP_MEMORY_SCOPE_WORKGROUP);
  return __builtin_amdgcn_readfirstlane(r); }
template <int SLEEP> DI void poll_flags(const char* flag, u64 tag) {
  u64 v; unsigned n = SPIN_CAP;
  asm volatile("1:\n\tglobal_load_dwordx2 %0, %2, off sc1\n\ts_sub_u32 %1, %1, 1\n\ts_cbranch_scc1 2f\n\ts_sleep %4\n\ts_waitcnt vmcnt(0)\n\t"
               "v_cmp_ne_u64 vcc, %3, %0\n\ts_cbranch_vccnz 1b\n2:\n\ts_waitcnt vmcnt(0)"
               : "=&v"(v), "+s"(n) : "v"(flag), "s"(tag), "i"(SLEEP) : "vcc", "scc", "memory");
}
DI unsigned poll_bit(const char* flag, u64 tag, int h) {
  u64 v, m; unsigned n = SPIN_CAP;
  asm volatile("1:\n\tglobal_load_dwordx2 %0, %3, off sc1\n\ts_sub_u32 %2, %2, 1\n\ts_cbranch_scc1 2f\n\ts_waitcnt vmcnt(0)\n\t"
               "v_cmp_eq_u64 %1, %4, %0\n\ts_nop 3\n\ts_bitcmp0_b64 %1, %5\n\ts_cbranch_scc1 1b\n2:\n\ts_waitcnt vmcnt(0)"
               : "=&v"(v), "=&s"(m), "+s"(n) : "v"(flag), "s"(tag), "i"(h) : "scc", "memory");
  return (unsigned)m;
}
#define STAMP(i) do {} while (0)
#define CSTAMP(i) do {} while (0)
#define VSTAMP(i, v) do {} while (0)
#define STAMP_OUT() do {} while (0)
#define NRESC
#define STV
#define STA
#define FSTAMP(i) do {} while (0)
#define LSTAMP(i) do {} while (0)
#define BAR() asm volatile("s_waitcnt lgkmcnt(0)\n\ts_barrier" ::: "memory")

#define QPRIO 3
#define LPRIO 2
#define GPRIO 1
#define G2PRIO 0
#define NOCHECK 0
#define QSPLIT 8
#define OLAG 4
#define FBPRIO 3
#define LOOK 4
DI float pick(u64 m, float hi, float lo) { float r; asm("v_cndmask_b32 %0, %1, %2, %3" : "=v"(r) : "v"(lo), "v"(hi), "s"(m)); return r; }
DI float gram_diag(const f32x16& g) {
  const u64 m1 = 0xaaaaaaaaaaaaaaaaull, m2 = 0xccccccccccccccccull, m4 = 0xf0f0f0f0f0f0f0f0ull, m8 = 0xff00ff00ff00ff00ull, m16 = 0xffff0000ffff0000ull;
  const float b0 = pick(m2, pick(m1, g[3], g[2]), pick(m1, g[1], g[0])), b1 = pick(m2, pick(m1, g[7], g[6]), pick(m1, g[5], g[4]));
  const float b2 = pick(m2, pick(m1, g[11], g[10]), pick(m1, g[9], g[8])), b3 = pick(m2, pick(m1, g[15], g[14]), pick(m1, g[13], g[12]));
  float d = pick(m16, pick(m8, b3, b2), pick(m8, b1, b0));
  asm volatile("s_nop 1" : "+v"(d));
  auto r = __builtin_amdgcn_permlane32_swap(__float_as_uint(d), __float_as_uint(d), false, false);
  return pick(m4, __uint_as_float(r[1]), __uint_as_float(r[0]));
}
template <int C> DI void x_frags(bf16x8 (&xa)[28], const lptr (&xrd)[4], int xwrap) {
#pragma unroll
  for (int s = 0; s < 4; ++s) xa[4 * C + s] = ldsb128(xrd[s] + p1_slot(3 * C) + (C == 6 ? xwrap : 0));
}
DI void row_norm(float s1, float s2, float& rs, float& nmb) {
  const float mean = sc1(s1 * (1.f / 448.f)), var = __builtin_fmaxf(sc1(s2 * (1.f / 448.f)) - mean * mean, 0.f);
  const float sd = bf16r(__builtin_amdgcn_sqrtf(bf16r(bf16r(var) + 1e-5f))); rs = __builtin_amdgcn_rcpf(sd); nmb = -bf16r(mean);
}
struct P1A { lptr x[4], w0[4], w1[4]; int xwrap; Ev ev; u64 c12; };
struct WF { s16x4 l0, h0, l1, h1; };
DI bf16x8 cat(const s16x4& lo, const s16x4& hi) { return __builtin_shufflevector(lo, hi, 0, 1, 2, 3, 4, 5, 6, 7); }
struct P1R { f32x16 a0, a1; f32x4 c0, c1, s1; f32x16 s2; };
#define MM(VT, acc, xf, wf) do { if constexpr (VT) acc = MFMA32(xf, wf, acc); else acc = MFMA32(wf, xf, acc); PIN(acc); SBAR(); } while (0)
template <bool VT, int ST, int C, int S> DI void p1_group(bf16x8 (&xa)[28], WF (&f)[4], P1R& r, const bf16x8& sel, const P1A& p) {
  constexpr int xo = p1_slot(3 * C) - (C >> 1) * 6 * SLOT, wo = p1_slot(3 * C + 2) - (C >> 1) * 6 * SLOT;
  if constexpr (ST >= 0) MM(VT, r.a0, xa[ST], cat(f[ST & 3].l0, f[ST & 3].h0));
  xa[4 * C + S] = ldsb128(p.x[S] + xo); f[S].l0 = trd(p.w0[S] + wo); SBAR();
  if constexpr (ST >= 0) MM(VT, r.a1, xa[ST], cat(f[ST & 3].l1, f[ST & 3].h1));
  f[S].h0 = trd(p.w0[S] + wo + 512); f[S].l1 = trd(p.w1[S] + wo); SBAR();
  if constexpr (ST >= 0) { r.s2 = MFMA32(xa[ST], xa[ST], r.s2); PIN(r.s2); SBAR(); }
  f[S].h1 = trd(p.w1[S] + wo + 512); SBAR();
  if constexpr (ST >= 0) { r.s1 = MFMA16(sel, xa[ST], r.s1); PIN(r.s1);
    if constexpr ((ST & 3) == 0) { r.c0 = MFMA16(sel, cat(f[0].l0, f[0].h0), r.c0); r.c1 = MFMA16(sel, cat(f[0].l1, f[0].h1), r.c1); PIN(r.c0); PIN(r.c1); }
    SBAR(); }
}
template <bool VT, int C> DI void p1_compute(bf16x8 (&xa)[28], WF (&f)[4], P1R& r, const bf16x8& sel, P1A& p STV) {
  if constexpr (C == 0) pwait<L_LD0>(p.ev);
  if constexpr (C == 0 && METRIC == 4) p.c12 = __builtin_amdgcn_s_memtime();
  if (C == 0) STAMP(12);
  FSTAMP(15 + C);
  if constexpr (C < 6) {
    p1_group<VT, 4 * C - 2, C, 0>(xa, f, r, sel, p); p1_group<VT, 4 * C - 1, C, 1>(xa, f, r, sel, p); p1_group<VT, 4 * C, C, 2>(xa, f, r, sel, p);
    const unsigned nxt = *(volatile LAS unsigned*)(uintptr_t)(p.ev.base + 4 * (L_LD0 + C + 1));
    p1_group<VT, 4 * C + 1, C, 3>(xa, f, r, sel, p);
    post<A_RD0 + C>(p.ev);
    if constexpr (C == 1 || C == 3) {
#pragma unroll
      for (int s = 0; s < 4; ++s) { p.x[s] += 6 * SLOT; p.w0[s] += 6 * SLOT; p.w1[s] += 6 * SLOT; PIN(p.x[s]); PIN(p.w0[s]); PIN(p.w1[s]); }
    }
    if constexpr (C == 5) {
#pragma unroll
      for (int s = 0; s < 4; ++s) { p.x[s] += p.xwrap; p.w0[s] -= 12 * SLOT; p.w1[s] -= 12 * SLOT; PIN(p.x[s]); PIN(p.w0[s]); PIN(p.w1[s]); }
    }
    SBAR();
    if (__builtin_expect(__builtin_amdgcn_readfirstlane(nxt) < 4u, 0)) pwait<L_LD0 + C + 1>(p.ev);
    p1_compute<VT, C + 1>(xa, f, r, sel, p STA);
  }
}
struct Ld { const char *xsrc, *w0src, *wqsrc, *fasrc; unsigned xvo0, xvo1, w0vo, wfvo, wqvo, w0rs, dst, xdst, xdst6, qdst; Ev ev; int ck[7]; };
template <int N, int E> DI void ld_piece(const Ld& k) {
  const unsigned dst = k.dst + p1_slot(N) + E * 1024;
  if constexpr (N < 21) {
    constexpr int c = N / 3, q = N % 3;
    if constexpr (q < 2) glds16(k.xsrc + (q * 16 + E * 8) * 896 + k.ck[c] * 128, E ? k.xvo1 : k.xvo0, (c < 6 ? k.xdst + p1_slot(3 * c) : k.xdst6) + (2 * q + E) * 1024);
    else glds16(k.w0src + (k.ck[c] * 64 + E * 8) * k.w0rs, k.w0vo, dst);
  } else if constexpr (N < 28) glds16(k.fasrc + (k.ck[N - 21] * 64 + E * 8) * 3584, k.wfvo, dst);
  else { constexpr int c = 2 * (N - 28) + E;
    glds16(k.wqsrc + k.ck[c < 7 ? c : 0] * (64 * 2688), k.wqvo, k.qdst + c * 4096); }
}
template <int N> DI void p1_stream(const Ld& k, bf16x8 (&xa)[28], const lptr (&xrd)[4], int xwrap STV) {
  if constexpr (N < 32) {
    if constexpr (N == 18 || (N > 19 && (N - 19) % 3 == 0)) { pwait<A_RD0 + (N - 18) / 3>(k.ev); pwait<L_LD0 + (N - 18) / 3 + 1>(k.ev); }
    ld_piece<N, 0>(k); ld_piece<N, 1>(k);
    if constexpr (N == 2) asm volatile("s_barrier" ::: "memory");
    constexpr int M = N - LOOK;
    if constexpr (M >= 2 && M <= 20 && M % 3 == 2) {
      constexpr int C = M / 3;
      asm volatile("s_waitcnt vmcnt(%0)" :: "i"(2 * LOOK) : "memory");
      post<L_LD0 + C>(k.ev);
      if (C == 0) STAMP(12);
      x_frags<C>(xa, xrd, xwrap); SBAR();
      if (C == 6) STAMP(1);
    } else if constexpr (M == 21) post<L_X6>(k.ev);
    else if constexpr (M == 27) { asm volatile("s_waitcnt vmcnt(%0)" :: "i"(2 * LOOK) : "memory"); post<L_FAL>(k.ev); }
    p1_stream<N + 1>(k, xa, xrd, xwrap STA);
  }
}
DI void cs_put(lptr lds, int area, int c, int t, int lane, const f32x4& cs) {
  if (lane < 16) { *(LAS float*)(lds + area + c * 512 + lane * 16 + t * 4) = cs[0]; *(LAS float*)(lds + area + c * 512 + (lane + 16) * 16 + t * 4) = cs[1]; }
}
DI float cs_get(lptr lds, int area, int c, int r32) { const f32x4 p = ldsf4(lds + area + c * 512 + r32 * 16); return sc1(sc1(p[0] + p[1]) + sc1(p[2] + p[3])); }
DI void lane_to_regs(lptr priv, int r32, int hh, float v, float (&o)[16]) {
  *(LAS float*)(priv + r32 * 4) = v;
#pragma unroll
  for (int i = 0; i < 4; ++i) { const f32x4 q = ldsf4(priv + (8 * i + 4 * hh) * 4); o[4 * i] = q[0]; o[4 * i + 1] = q[1]; o[4 * i + 2] = q[2]; o[4 * i + 3] = q[3]; }
}
typedef short s16x2 __attribute__((ext_vector_type(2)));
template <bool RELU> DI void epi_T(const f32x16& acc, const float (&cs)[16], float sc, float nmb, u32x4& o0, u32x4& o1) {
#pragma unroll
  for (int p = 0; p < 8; ++p) {
    const float v0 = sc1(sc1(__builtin_fmaf(cs[2 * p], nmb, acc[2 * p])) * sc), v1 = sc1(sc1(__builtin_fmaf(cs[2 * p + 1], nmb, acc[2 * p + 1])) * sc);
    unsigned w = cvtpk(v0, v1);
    if (RELU) w = __builtin_bit_cast(unsigned, __builtin_elementwise_max(__builtin_bit_cast(s16x2, w), (s16x2){0, 0}));
    if (p < 4) o0[p] = w; else o1[p - 4] = w;
  }
}
#define ST16(v, off) __builtin_amdgcn_raw_buffer_store_b128(v, rsw, off, 0, 16)
#define ABL_NOKV 0
#define LDAUX 16
#define LD16(off) __builtin_amdgcn_raw_buffer_load_b128(rsw, off, 0, LDAUX)

DI float set_ref(bf16x8& q1, float m, int hh) {
  const unsigned w = cvtpk(-__builtin_rintf(m), 0.f);
  u32x4 v = __builtin_bit_cast(u32x4, q1); v[2] = hh ? w : v[2]; q1 = __builtin_bit_cast(bf16x8, v);
  return -__uint_as_float(w << 16);
}
DI float tile_max(const f32x16& s) {
  float a = __builtin_fmaxf(__builtin_fmaxf(s[0], s[1]), s[2]), b = __builtin_fmaxf(__builtin_fmaxf(s[3], s[4]), s[5]);
#pragma unroll
  for (int r = 6; r < 14; r += 4) { a = __builtin_fmaxf(__builtin_fmaxf(a, s[r]), s[r + 1]); b = __builtin_fmaxf(__builtin_fmaxf(b, s[r + 2]), s[r + 3]); }
  a = __builtin_fmaxf(__builtin_fmaxf(a, s[14]), s[15]);
  return swap_max(__builtin_fmaxf(a, b));
}

template <int P, int ST> DI void f_steps(const Ev& ev, const bf16x8 (&xa)[28], WF (&f)[3], f32x16& a0, f32x16& a1, f32x4& c0, f32x4& c1, const bf16x8& sel, const lptr (&w0)[4], const lptr (&w1)[4]) {
  if constexpr (ST < 28) {
    constexpr int N = ST + 2, off = p1_slot((P ? 32 : 21) + ((N < 28 ? N : 0) >> 2)) - (P ? L_FB : L_FA);
    SBAR(); MM(false, a0, xa[ST], cat(f[ST % 3].l0, f[ST % 3].h0));
    if constexpr (N < 28) { f[N % 3].l0 = trd(w0[N & 3] + off); f[N % 3].h0 = trd(w0[N & 3] + off + 512); SBAR(); }
    MM(false, a1, xa[ST], cat(f[ST % 3].l1, f[ST % 3].h1));
    if constexpr (N < 28) { f[N % 3].l1 = trd(w1[N & 3] + off); f[N % 3].h1 = trd(w1[N & 3] + off + 512); SBAR(); }
    if constexpr ((ST & 3) == 0) { c0 = MFMA16(sel, cat(f[ST % 3].l0, f[ST % 3].h0), c0); c1 = MFMA16(sel, cat(f[ST % 3].l1, f[ST % 3].h1), c1); PIN(c0); PIN(c1); }
    if constexpr (P == 0 && ST == 6) { WAIT_VM(0); post<L_WQL>(ev); }
    f_steps<P, ST + 1>(ev, xa, f, a0, a1, c0, c1, sel, w0, w1);
  }
}
DI void wf_read(WF& f, lptr p0, lptr p1) { f.l0 = trd(p0); f.h0 = trd(p0 + 512); f.l1 = trd(p1); f.h1 = trd(p1 + 512); }

constexpr int g2_issued(int st) { int n = 28; for (int s = 0; s < st; ++s) { if (s < 14) n += 2; if (s < 24) n += 2; } return n; }
constexpr int g2_pos_h(int s) { return s < 14 ? 2 * s + 1 : g2_issued(s - 14) + 1; }
constexpr int g2_pos_w(int s) { return s < 12 ? -1 : g2_issued(s - 12) + (s - 12 < 14 ? 2 : 0) + 1; }
constexpr int g2_wait(int st) { const int a = g2_pos_h(st), b = st + 2 < 28 ? g2_pos_w(st + 2) : -1; const int n = g2_issued(st) - 1 - (a > b ? a : b); return n > 63 ? 63 : n; }
DI void ldg_sc1(u32x4& d, const char* sbase, unsigned voff) { asm volatile("global_load_dwordx4 %0, %1, %2 sc1" : "=v"(d) : "v"(voff), "s"(sbase) : "memory"); }
struct G2 { const char *hb0, *hb1, *w2s, *wos; unsigned lv, w2vo, ringdst; Ev ev; int t; lptr br0, br1; };
template <int ST, int E> DI void g2_w(const G2& g) {
  const unsigned dst = g.ringdst + (ST % 12) * 2048 + E * 1024;
  if constexpr (ST < 28) glds16(g.w2s + (ST * 16 + E * 8) * 896, g.w2vo, dst);
  else if constexpr (ST < 35) glds16(g.wos + ((ST - 28) * 16 + E * 8) * 896, g.w2vo, dst);
  else glds16(g.wos + (g.t == 3 ? 104 : 112 + E * 8) * 896, g.w2vo, dst);
}
template <int A, int B> DI void g2_ws(const G2& g) { if constexpr (A < B) { g2_w<A, 0>(g); g2_w<A, 1>(g); g2_ws<A + 1, B>(g); } }
template <int ST> DI void g2_steps(const G2& g, u32x4 (&hf)[14][2], WF (&f)[3], f32x16& a00, f32x16& a01, f32x16& a10, f32x16& a11) {
  if constexpr (ST < 28) {
    if constexpr (ST == G2_SPLIT) { __builtin_amdgcn_s_setprio(0); pwait_long<A_DONE>(g.ev); __builtin_amdgcn_s_setprio(GPRIO); }
    asm volatile("s_waitcnt vmcnt(%2)" : "+v"(hf[ST % 14][0]), "+v"(hf[ST % 14][1]) : "i"(g2_wait(ST)) : "memory");
    constexpr int N = ST + 2, off = (N % 12) * 2048;
    const bf16x8 h0 = asfrag(hf[ST % 14][0]), h1 = asfrag(hf[ST % 14][1]), w0 = cat(f[ST % 3].l0, f[ST % 3].h0), w1 = cat(f[ST % 3].l1, f[ST % 3].h1);
    SBAR(); MM(false, a00, h0, w0);
    if constexpr (N < 28) { f[N % 3].l0 = trd(g.br0 + off); f[N % 3].h0 = trd(g.br0 + off + 512); SBAR(); }
    MM(false, a01, h1, w0);
    if constexpr (N < 28) { f[N % 3].l1 = trd(g.br1 + off); f[N % 3].h1 = trd(g.br1 + off + 512); SBAR(); }
    MM(false, a10, h0, w1);
    MM(false, a11, h1, w1);
    if constexpr (ST < 14) { ldg_sc1(hf[ST][0], g.hb0 + (ST + 14) * 1024, g.lv); ldg_sc1(hf[ST][1], g.hb1 + (ST + 14) * 1024, g.lv); }
    if constexpr (ST < 24) { g2_w<ST + 12, 0>(g); g2_w<ST + 12, 1>(g); }
    g2_steps<ST + 1>(g, hf, f, a00, a01, a10, a11);
  }
}

__global__ void __launch_bounds__(512, 2) fused_block(const char* x, const char* wqkv, const char* wo, const char* w1, const char* w2, char* out, char* ws) {
  asm volatile("" : "+s"(x), "+s"(wqkv), "+s"(wo), "+s"(w1), "+s"(w2), "+s"(out), "+s"(ws));
  __shared__ __attribute__((aligned(16))) char smem[LDS_BYTES];
  u64 pc; asm volatile("s_getpc_b64 %0" : "=s"(pc));
  const lptr lds = (lptr)smem; const unsigned lds0 = (unsigned)(uintptr_t)lds;
  const int tid = threadIdx.x, lane = tid & 63, r32 = lane & 31, hh = lane >> 5;
  const int w = __builtin_amdgcn_readfirstlane(tid >> 6), t = w & 3, c = w >> 2;
  const int b = blockIdx.x, xg = b & 7, bi = b >> 3;
  const int j = (xg & 3) * 4 + (bi >> 3), rb = (xg >> 2) * 8 + (bi & 7);
  const int fme = (rb * 16 + j) * 128, rt = rb * 4 + t;
  STAMP(0); CSTAMP(30); VSTAMP(29, (unsigned)__builtin_amdgcn_s_getreg(20 | (3 << 11)) & 0xfu);
  const u64 c0_ = __builtin_amdgcn_s_memtime(); u64 cx_ = c0_;
  const int rot = t, r7 = ROT7 ? (rb + 2 * j) % 7 : 0;
  const int q4 = (lane & 15) >> 2, p4 = lane & 3, g16 = (lane >> 4) & 1;
  const int pi_std = 4 * g16 + 2 * (p4 & 1) + (p4 >> 1);
  const lptr wrd0 = lds + (8 * hh + q4) * 128 + pi_std * 8;
  const lptr priv = lds + L_PRIV + w * 256;
  Ev ev = {lds0 + L_CNT, 1u}; PIN(ev.base); PIN(ev.one);
  const __amdgpu_buffer_rsrc_t rsw = __builtin_amdgcn_make_buffer_rsrc(ws, 0, WS_BYTES, 0x00020000);
#define COMMON_SETUP() \
  lptr xrd[4], wr0[4], wr1[4]; \
  _Pragma("unroll") for (int s = 0; s < 4; ++s) { xrd[s] = lds + (t * 32 + r32) * 128 + (((2 * (s ^ rot) + hh) ^ ((r32 >> 1) & 7)) << 4); \
    wr0[s] = wrd0 + ((q4 >> 1) << 6) + (s ^ rot) * 2048; wr1[s] = wrd0 + ((1 ^ (q4 >> 1)) << 6) + (s ^ rot) * 2048; } \
  const int xwrap = (t >> 1) ? -NSLOT * SLOT : 0; \
  bf16x8 sel; { const short one = ((lane & 15) == ((lane >> 4) & 1)) ? (short)0x3f80 : (short)0; sel = (bf16x8){one, one, one, one, one, one, one, one}; } \
  bf16x8 xa[28]; float rs, nmb
#define GET_TAG() PIN(tagraw); const u64 tag = (((u64)(unsigned)__builtin_amdgcn_readfirstlane((unsigned)(tagraw >> 32)) << 32) | (unsigned)__builtin_amdgcn_readfirstlane((unsigned)tagraw)) + 1

  if (c == 0) {
    unsigned cpf0;
    asm volatile("global_load_dword %0, %1, %2" : "=v"(cpf0) : "v"(min((unsigned)(bi * 16 + (tid & 15)) * 128u, (unsigned)CODE_BYTES - 128u)), "s"(pc) : "memory");
    unsigned tch = 0;
    u64 tagraw = __hip_atomic_load((GAS u64*)(ws + WS_EPOCH), RLX_AGENT);
    if (tid < N_EV) *(LAS unsigned*)(lds + L_CNT + 4 * tid) = 0u;
    BAR();
    const bool isK = j < 7, isV = j >= 7 && j < 14;
    bf16x8 qf[4][2]; u64 tag;
    {
      COMMON_SETUP();
      unsigned ad[4]; bool strad[4] = {false, false, false, false};
      if (isV) { const int rho = r32 >> 2, col0 = (j - 7) * 64 + 4 * ((rho & 4) + 2 * (rho & 1) + ((rho >> 1) & 1)) + (r32 & 3);
#pragma unroll
        for (int cc = 0; cc < 2; ++cc) { const int col = col0 + cc * 32, hd = col / 28, d = col - hd * 28;
          ad[cc] = WS_KV + hd * 262144 + rt * 4096 + 2048 + (hh * 32 + d) * 16; ad[2 + cc] = d >= 24 ? ad[cc] + 64 : 0x80000000u; strad[cc] = d == 24; }
      } else if (isK) {
#pragma unroll
        for (int s = 0; s < 4; ++s) { const int P = j * 8 + 2 * s + hh, hp = P / 7, rr = P - hp * 7, hd = 2 * hp + (rr > 3), sg = rr > 3 ? rr - 4 : rr;
          ad[s] = WS_KV + hd * 262144 + rt * 4096 + sg * 512 + r32 * 16; strad[s] = rr == 3; }
      } else {
#pragma unroll
        for (int s = 0; s < 4; ++s) ad[s] = WS_H + rt * 114688 + (j - 14) * 4096 + s * 1024 + lane * 16;
      }
      PIN(ad[0]); PIN(ad[1]); PIN(ad[2]); PIN(ad[3]);
      asm volatile("s_waitcnt vmcnt(0)" : "+v"(cpf0), "+v"(tch), "+v"(tagraw) :: "memory");
      *(LAS u64*)(lds + L_TAG) = tagraw;
      tag = (((u64)(unsigned)__builtin_amdgcn_readfirstlane((unsigned)(tagraw >> 32)) << 32) | (unsigned)__builtin_amdgcn_readfirstlane((unsigned)tagraw)) + 1;
      P1R r = {}; float cr0[16], cr1[16], cl0, cl1;
      { P1A p; p.xwrap = xwrap; p.ev = ev; WF f[4];
#pragma unroll
        for (int s = 0; s < 4; ++s) { p.x[s] = xrd[s]; p.w0[s] = wr0[s]; p.w1[s] = wr1[s]; }
        const lptr csw = lds + L_CS0 + lane * 16 + t * 4;
#define GS(st) do { r.s2 = MFMA32(xa[st], xa[st], r.s2); PIN(r.s2); r.s1 = MFMA16(sel, xa[st], r.s1); PIN(r.s1); SBAR(); } while (0)
#define MA(VT, st) MM(VT, r.a0, xa[st], cat(f[st & 3].l0, f[st & 3].h0))
#define MB(VT, st) MM(VT, r.a1, xa[st], cat(f[st & 3].l1, f[st & 3].h1))
#define WRD(s) do { wf_read(f[s], p.w0[s] + SLOT, p.w1[s] + SLOT); SBAR(); } while (0)
#define P1_TAIL(VT) do { \
        p1_compute<VT, 0>(xa, f, r, sel, p STA); \
        __builtin_amdgcn_s_setprio(3); \
        _Pragma("unroll") for (int s = 0; s < 4; ++s) xa[24 + s] = ldsb128(p.x[s] + 6 * SLOT); \
        SBAR(); GS(22); WRD(0); GS(23); WRD(1); GS(24); \
        r.c0 = MFMA16(sel, cat(f[0].l0, f[0].h0), r.c0); r.c1 = MFMA16(sel, cat(f[0].l1, f[0].h1), r.c1); PIN(r.c0); PIN(r.c1); SBAR(); \
        GS(25); GS(26); \
        if (lane < 16) { *(LAS float*)csw = r.c0[0]; *(LAS float*)(csw + 256) = r.c0[1]; *(LAS float*)(csw + 512) = r.c1[0]; *(LAS float*)(csw + 768) = r.c1[1]; } \
        post<A_CSX>(ev); SBAR(); \
        GS(27); MA(VT, 22); MB(VT, 22); WRD(2); MA(VT, 23); \
        if (lane < 16) { *(LAS float*)(priv + lane * 4) = r.s1[0]; *(LAS float*)(priv + 64 + lane * 4) = r.s1[1]; } \
        const float sx_ = *(LAS float*)(priv + r32 * 4); SBAR(); \
        MB(VT, 23); WRD(3); post<A_CS0>(ev); MA(VT, 24); \
        const float sq_ = gram_diag(r.s2); SBAR(); \
        MB(VT, 24); \
        pwait<A_CSX>(ev); \
        const f32x4 p0_ = ldsf4(lds + L_CS0 + r32 * 16), p1_ = ldsf4(lds + L_CS0 + 512 + r32 * 16); SBAR(); \
        MA(VT, 25); \
        row_norm(sx_, sq_, rs, nmb); PIN(rs); PIN(nmb); SBAR(); \
        MB(VT, 25); \
        cl0 = sc1(sc1(p0_[0] + p0_[1]) + sc1(p0_[2] + p0_[3])); cl1 = sc1(sc1(p1_[0] + p1_[1]) + sc1(p1_[2] + p1_[3])); \
        *(LAS float*)(lds + L_STAT + t * 256 + lane * 4) = hh ? nmb : rs; post<A_ST>(ev); SBAR();     \
        MA(VT, 26); \
        if constexpr (!VT) { lane_to_regs(priv, r32, hh, cl0, cr0); } SBAR(); \
        MA(VT, 27); \
        if constexpr (!VT) { lane_to_regs(priv + 128, r32, hh, cl1, cr1); } SBAR(); \
        MB(VT, 26); MB(VT, 27); } while (0)
        if (isV) P1_TAIL(true); else P1_TAIL(false); if (METRIC == 4) cx_ = p.c12; }
      STAMP(1);
      u32x4 o[4];
      if (isV) {
#pragma unroll
        for (int i = 0; i < 4; ++i) { const f32x4 rr = ldsf4(lds + L_STAT + t * 256 + (8 * i + 4 * hh) * 4), mm = ldsf4(lds + L_STAT + t * 256 + 128 + (8 * i + 4 * hh) * 4);
#pragma unroll
          for (int e = 0; e < 2; ++e) {
            const int n = 4 * i + 2 * e;
            o[i >> 1][2 * (i & 1) + e] = cvtpk(sc1(sc1(__builtin_fmaf(mm[2 * e], cl0, r.a0[n])) * rr[2 * e]), sc1(sc1(__builtin_fmaf(mm[2 * e + 1], cl0, r.a0[n + 1])) * rr[2 * e + 1]));
            o[2 + (i >> 1)][2 * (i & 1) + e] = cvtpk(sc1(sc1(__builtin_fmaf(mm[2 * e], cl1, r.a1[n])) * rr[2 * e]), sc1(sc1(__builtin_fmaf(mm[2 * e + 1], cl1, r.a1[n + 1])) * rr[2 * e + 1])); } }
        ST16(o[0], ad[0]); ST16(o[1], ad[0] + 1024); ST16(o[2], ad[1]); ST16(o[3], ad[1] + 1024);
#pragma unroll
        for (int cc = 0; cc < 2; ++cc) { const unsigned e = strad[cc] ? 0x3f803f80u : 0u; const u32x4 pad = {e, e, e, e}; ST16(pad, ad[2 + cc]); ST16(pad, ad[2 + cc] + 1024); }
      } else {
        if (isK) {
          epi_T<false>(r.a0, cr0, rs, nmb, o[0], o[1]);
#pragma unroll
          for (int s = 0; s < 2; ++s) { u32x4 v = o[s]; if (strad[s]) { v[2] = 0x3f803f80u; v[3] = 0x00003f80u; } ST16(v, ad[s]); }
          epi_T<false>(r.a1, cr1, rs, nmb, o[2], o[3]);
#pragma unroll
          for (int s = 2; s < 4; ++s) { u32x4 v = o[s]; if (strad[s]) { v[2] = 0x3f803f80u; v[3] = 0x00003f80u; } ST16(v, ad[s]); }
#pragma unroll
          for (int s = 0; s < 4; ++s) if (strad[s]) { const u32x4 e = {o[s][2], o[s][3], 0x3f803f80u, 0x00003f80u}; ST16(e, ad[s] + 262144); }
        } else {
          epi_T<true>(r.a0, cr0, rs, nmb, o[0], o[1]); epi_T<true>(r.a1, cr1, rs, nmb, o[2], o[3]);
#pragma unroll
          for (int s = 0; s < 4; ++s) ST16(o[s], ad[s]);
        }
      }
      if (METRIC == 1) cx_ = __builtin_amdgcn_s_memtime();
      if (j >= 6) {
        pwait<A_CS0>(ev); pwait<L_X6>(ev);
        const unsigned vo = (16 * t + (lane >> 3)) * 3584 + (((lane & 7) ^ (((lane >> 4) & 1) << 2)) << 4);
#pragma unroll
        for (int i = 0; i < 14; ++i) { const int c7 = (i >> 1) + r7 >= 7 ? (i >> 1) + r7 - 7 : (i >> 1) + r7;
          glds16(w1 + (12 + j) * 128 + (c7 * 64 + (i & 1) * 8) * 3584, vo, lds0 + t * 2048 + p1_slot(32 + (i >> 1)) + (i & 1) * 1024); }
        asm volatile("s_waitcnt vmcnt(14)" ::: "memory");
      } else WAIT_VM(0);
      if (cnt_add(lds, C_KV) == 3u && lane == 0) __hip_atomic_store((GAS u64*)(ws + WS_FKV + fme), tag, RLX_AGENT);
      if (cnt_add(lds, C_H) == 7u && lane == 0) __hip_atomic_store((GAS u64*)(ws + WS_FH + fme), tag, RLX_AGENT);
      STAMP(2);
      __builtin_amdgcn_s_setprio(QPRIO);
      f32x16 a0, a1; f32x4 c0;
      const int rho = 4 * g16 + p4, sg = 2 * (rho >> 2) + (rho & 1), half = (rho >> 1) & 1;
      const int g = (j & 1) ? (sg < 3 ? 2 * sg + 1 + half : (half ? 7 : 0)) : 2 * sg + half;
      lptr qb[4];
#pragma unroll
      for (int s = 0; s < 4; ++s) { qb[s] = lds + L_WQ + (8 * hh + q4) * 64 + g * 8 + (s ^ rot) * 1024; PIN(qb[s]); }
      a0 = (f32x16){}; a1 = (f32x16){}; c0 = (f32x4){};
      pwait<L_WQL>(ev);
      s16x4 ql[6], qh[6];
#pragma unroll
      for (int st = 0; st < 4; ++st) { ql[st] = trd(qb[st]); qh[st] = trd(qb[st] + 256); }
#pragma unroll
      for (int st = 0; st < 28; ++st) {
        if (st == QSPLIT) {
          WAIT_VM(0); post<A_FB>(ev); }
        SBAR();
        if (st & 1) { a1 = MFMA32(cat(ql[st % 6], qh[st % 6]), xa[st], a1); PIN(a1); } else { a0 = MFMA32(cat(ql[st % 6], qh[st % 6]), xa[st], a0); PIN(a0); }
        SBAR();
        if (st + 4 < 28) { const int o_ = ((st + 4) >> 2) * 4096; ql[(st + 4) % 6] = trd(qb[st & 3] + o_); qh[(st + 4) % 6] = trd(qb[st & 3] + o_ + 256); }
        SBAR();
        c0 = MFMA16(sel, cat(ql[st % 6], qh[st % 6]), c0); PIN(c0);
      }
#pragma unroll
      for (int i = 0; i < 16; ++i) a0[i] = sc1(a0[i] + a1[i]);
      if (lane < 16) { *(LAS float*)(priv + lane * 4) = c0[0]; *(LAS float*)(priv + 64 + lane * 4) = c0[1]; }
      float cr[16];
#pragma unroll
      for (int i = 0; i < 4; ++i) { const f32x4 q = ldsf4(priv + (8 * i + 4 * hh) * 4); cr[4 * i] = q[0]; cr[4 * i + 1] = q[1]; cr[4 * i + 2] = q[2]; cr[4 * i + 3] = q[3]; }
      epi_T<false>(a0, cr, rs * QSCALE, nmb, o[0], o[1]);
      if (hh) { o[1][2] = 0u; o[1][3] = 0u; }
      *(LAS u32x4*)(lds + L_QX + t * 2048 + lane * 16) = o[0]; *(LAS u32x4*)(lds + L_QX + t * 2048 + 1024 + lane * 16) = o[1];
    }
    post<A_QW>(ev);
    const f32x16 zero = {};
    f32x16 o[4]; u32x4 kv[4][4], pA0 = {}, pA1 = {}, pB0 = {}, pB1 = {};
#pragma unroll
    for (int q = 0; q < 4; ++q) { o[q] = zero; PIN(o[q]); }
    kv[3][2] = (u32x4){}; kv[3][3] = (u32x4){}; PIN(kv[3][2]); PIN(kv[3][3]); PIN(pB0); PIN(pB1);
    STAMP(4);
    { const int c0 = (28 * j) >> 6, c1 = (28 * j + 27) >> 6, sel4 = lane >> 4;
      int jj = sel4 == 0 ? c0 : sel4 == 1 ? c1 : sel4 == 2 ? 7 + c0 : 7 + c1;
      if (j == 0 && (sel4 & 1)) jj = 14 + (sel4 >> 1);
      poll_flags<0>(ws + WS_FKV + ((lane & 15) * 16 + jj) * 128, tag); }
    if (METRIC == 2) cx_ = __builtin_amdgcn_s_memtime();
    const unsigned kvb = WS_KV + j * 262144 + t * 65536 + lane * 16;
#pragma unroll
    for (int u = 0; u < 3; ++u)
#pragma unroll
      for (int e = 0; e < 4; ++e) kv[u][e] = LD16(kvb + u * 4096 + e * 1024);
    pwait<A_QW>(ev);
#pragma unroll
    for (int q = 0; q < 4; ++q) { const lptr a = lds + L_QX + ((q + t + 1) & 3) * 2048 + lane * 16; qf[q][0] = ldsb128(a); qf[q][1] = ldsb128(a + 1024); }
    float mref[4]; bool redo;
#pragma unroll
    for (int q = 0; q < 4; ++q) {
      f32x16 s = MFMA32(asfrag(kv[0][0]), qf[q][0], zero); s = MFMA32(asfrag(kv[0][1]), qf[q][1], s);
      mref[q] = set_ref(qf[q][1], tile_max(s) + REF_MARGIN, hh);
    }
    redo = !(__builtin_fmaxf(__builtin_fmaxf(__builtin_fabsf(mref[0]), __builtin_fabsf(mref[1])), __builtin_fmaxf(__builtin_fabsf(mref[2]), __builtin_fabsf(mref[3]))) < REF_LIMIT);
    __builtin_amdgcn_s_setprio(LPRIO);
    f32x16 sA, sB;
    sA = MFMA32(asfrag(kv[0][0]), qf[0][0], zero); sA = MFMA32(asfrag(kv[0][1]), qf[0][1], sA);
    PIN(sA); STAMP(6);
    auto area = [&](int dst, int e) -> lptr { const int n = dst * 3 + e; return lds + (n < 10 ? 8 * SLOT + n * 4096 : (n - 10) * 4096) + lane * 16; };
    auto put_o = [&](const f32x16& v, float m, int q) { const lptr a = area((q + t + 1) & 3, q);
      *(LAS f32x4*)a = (f32x4){v[0], v[1], v[2], v[3]}; *(LAS f32x4*)(a + 1024) = (f32x4){v[4], v[5], v[6], v[7]};
      *(LAS f32x4*)(a + 2048) = (f32x4){v[8], v[9], v[10], v[11]}; *(LAS f32x4*)(a + 3072) = (f32x4){v[12], hh ? m : v[13], v[14], v[15]}; };
#define PUT_O(q) put_o(o[q], mref[q], q)
#define EX4(S, i) do { S[i] = __builtin_amdgcn_exp2f(S[i]); S[i + 1] = __builtin_amdgcn_exp2f(S[i + 1]); S[i + 2] = __builtin_amdgcn_exp2f(S[i + 2]); S[i + 3] = __builtin_amdgcn_exp2f(S[i + 3]); } while (0)
#define UNIT(SC, SN, PC0, PC1, PP0, PP1, KN0, KN1, QN, QP, VV0, VV1, EXTRA) do { \
      SBAR(); if (QN >= 0) { SN = MFMA32(asfrag(KN0), qf[QN & 3][0], zero); PIN(SN); } SBAR(); EX4(SC, 0); PIN(SC); SBAR(); \
      if (QN >= 0) { SN = MFMA32(asfrag(KN1), qf[QN & 3][1], SN); PIN(SN); } SBAR(); EX4(SC, 4); PIN(SC); SBAR(); \
      o[QP] = MFMA32(asfrag(VV0), asfrag(PP0), o[QP]); PIN(o[QP]); SBAR(); EXTRA; EX4(SC, 8); PIN(SC); SBAR(); \
      o[QP] = MFMA32(asfrag(VV1), asfrag(PP1), o[QP]); PIN(o[QP]); SBAR(); EX4(SC, 12); SBAR(); \
      PC0 = (u32x4){cvtpk(SC[0], SC[1]), cvtpk(SC[2], SC[3]), cvtpk(SC[4], SC[5]), cvtpk(SC[6], SC[7])}; \
      PC1 = (u32x4){cvtpk(SC[8], SC[9]), cvtpk(SC[10], SC[11]), cvtpk(SC[12], SC[13]), cvtpk(SC[14], SC[15])}; PIN(PC0); PIN(PC1); SBAR(); } while (0)
    const float lthr = hh ? 16777216.f : __builtin_inff();
#define CHECK3 do { float m_; asm volatile("v_max3_f32 %0, %2, %3, %4\n\tv_cmp_lt_f32 %1, %5, %0" : "=&v"(m_), "=s"(g0) : "v"(o[1][12]), "v"(o[2][12]), "v"(o[3][12]), "v"(lthr)); } while (0)
#define CHECK1 asm volatile("v_cmp_lt_f32 %0, %2, %1" : "=s"(g1) : "v"(o[0][12]), "v"(lthr))
#define LDKV(UL, e) kv[UL][e] = __builtin_amdgcn_raw_buffer_load_b128(rsw, kvb + (e) * 1024, nt, LDAUX)
#define KEYTILE(U, UN, UL, LOAD, QLAST, X1, X2, X3) do { const int nt = (kt + (U) + 3) * 4096; \
      UNIT(sA, sB, pA0, pA1, pB0, pB1, kv[U][0], kv[U][1], 1, 3, kv[UL][2], kv[UL][3], if (LOAD) { LDKV(UL, 0); LDKV(UL, 1); }); \
      UNIT(sB, sA, pB0, pB1, pA0, pA1, kv[U][0], kv[U][1], 2, 0, kv[U][2], kv[U][3], if (LOAD) { LDKV(UL, 2); LDKV(UL, 3); } X1); \
      UNIT(sA, sB, pA0, pA1, pB0, pB1, kv[U][0], kv[U][1], 3, 1, kv[U][2], kv[U][3], X2); \
      UNIT(sB, sA, pB0, pB1, pA0, pA1, kv[UN][0], kv[UN][1], QLAST, 2, kv[U][2], kv[U][3], X3); \
    } while (0)
    for (int kt = 0; kt < 12; kt += 4) {
      LSTAMP(16 + (kt >> 1)); KEYTILE(0, 1, 3, true, 0, , , ); KEYTILE(1, 2, 0, true, 0, , , ); LSTAMP(17 + (kt >> 1)); KEYTILE(2, 3, 1, true, 0, , , ); u64 g0, g1;
      KEYTILE(3, 0, 2, true, 0, CHECK3, CHECK1, );
      if (!NOCHECK && __builtin_expect((g0 | g1) != 0ull, 0)) {
        NRESC;
        o[3] = MFMA32(asfrag(kv[3][2]), asfrag(pB0), o[3]); o[3] = MFMA32(asfrag(kv[3][3]), asfrag(pB1), o[3]); pB0 = (u32x4){}; pB1 = (u32x4){};
#pragma unroll
        for (int q = 0; q < 4; ++q) { const float l = upper_bcast(o[q][12]); redo |= !(l < 1.2676506002282294e30f);
          const float mn = set_ref(qf[q][1], mref[q] + (float)max(__builtin_amdgcn_frexp_expf(l) + (int)REF_MARGIN, 0), hh);
          const int e = (int)(mn - mref[q]); mref[q] = mn; redo |= !(__builtin_fabsf(mn) < REF_LIMIT);
#pragma unroll
          for (int r = 0; r < 16; ++r) o[q][r] = __builtin_ldexpf(o[q][r], -e);
          if (q == 0) {
#pragma unroll
            for (int r = 0; r < 16; ++r) sA[r] -= (float)e; } } }
    }
    { const int kt = 12;
      if (__builtin_expect(__any(redo), 0)) { mref[0] = mref[1] = mref[2] = mref[3] = __builtin_nanf(""); }
      pwait<L_CSBP>(ev);
      LSTAMP(22); KEYTILE(0, 1, 3, true, 0, , , ); KEYTILE(1, 2, 0, false, 0, , , ); LSTAMP(23); KEYTILE(2, 3, 1, false, 0, , , );
      post<A_DONE>(ev); __builtin_amdgcn_s_setprio(3);
      KEYTILE(3, 0, 2, false, -1, , PUT_O(0), PUT_O(1)); }
    o[3] = MFMA32(asfrag(kv[3][2]), asfrag(pB0), o[3]); o[3] = MFMA32(asfrag(kv[3][3]), asfrag(pB1), o[3]); PIN(o[3]); SBAR();
    PUT_O(2);
    if (METRIC == 3) cx_ = __builtin_amdgcn_s_memtime();
    VSTAMP(28, nresc + (__any(redo) ? 1000 : 0));
    auto combine = [&]() -> bool {
      f32x4 pp[3][4]; f32x16 of;
#pragma unroll
      for (int e = 0; e < 3; ++e) { const lptr a = area(t, e); pp[e][3] = ldsf4(a + 3072); }
#pragma unroll
      for (int e = 0; e < 3; ++e) { const lptr a = area(t, e);
#pragma unroll
        for (int i = 0; i < 3; ++i) pp[e][i] = ldsf4(a + i * 1024); }
      const float l0 = o[3][12], l1 = pp[0][3][0], l2 = pp[1][3][0], l3 = pp[2][3][0], m1 = pp[0][3][1], m2 = pp[1][3][1], m3 = pp[2][3][1];
      const float mx = __builtin_fmaxf(__builtin_fmaxf(mref[3] + (float)__builtin_amdgcn_frexp_expf(l0), m1 + (float)__builtin_amdgcn_frexp_expf(l1)),
                                       __builtin_fmaxf(m2 + (float)__builtin_amdgcn_frexp_expf(l2), m3 + (float)__builtin_amdgcn_frexp_expf(l3)));
      float f0 = __builtin_amdgcn_exp2f(mref[3] - mx), f1 = __builtin_amdgcn_exp2f(m1 - mx), f2 = __builtin_amdgcn_exp2f(m2 - mx), f3 = __builtin_amdgcn_exp2f(m3 - mx);
      const float inv = __builtin_amdgcn_rcpf(__builtin_fmaf(l3, f3, __builtin_fmaf(l2, f2, __builtin_fmaf(l1, f1, l0 * f0))));
      f0 = upper_bcast(f0 * inv); f1 = upper_bcast(f1 * inv); f2 = upper_bcast(f2 * inv); f3 = upper_bcast(f3 * inv);
      const bool big = hh && !(__builtin_fmaxf(__builtin_fmaxf(l0, l1), __builtin_fmaxf(l2, l3)) < 1.2676506002282294e30f);
#pragma unroll
      for (int r = 0; r < 16; ++r) of[r] = sc1(__builtin_fmaf(pp[2][r >> 2][r & 3], f3, sc1(__builtin_fmaf(pp[1][r >> 2][r & 3], f2, sc1(__builtin_fmaf(pp[0][r >> 2][r & 3], f1, sc1(o[3][r] * f0)))))));
      u32x4 e0, e1;
#pragma unroll
      for (int p = 0; p < 4; ++p) { e0[p] = cvtpk(of[2 * p], of[2 * p + 1]); e1[p] = cvtpk(of[8 + 2 * p], of[9 + 2 * p]); }
      if (hh) { e1[2] = 0u; e1[3] = 0u; }
      const unsigned a = WS_O + rt * 32768 + j * 2048 + lane * 16;
      ST16(e0, a); ST16(e1, a + 1024);
      unsigned ex = 0;
#pragma unroll
      for (int p = 0; p < 4; ++p) ex |= ((e0[p] & 0x7f807f80u) + 0x00800080u) | ((e1[p] & 0x7f807f80u) + 0x00800080u);
      return __any((ex & 0x80008000u) != 0u || big);
    };
    post<A_CMB>(ev); STAMP(9); pwait<A_CMB>(ev);
    if (__builtin_expect(combine(), 0)) *(LAS unsigned*)(lds + L_CNT + 4 * C_BAD) = 1u;
    STAMP(10);
    WAIT_VM(0);
    const unsigned arr = cnt_add(lds, C_O);
    __builtin_amdgcn_s_setprio(0);
    if (arr != 3u) pwait_long<C_O>(ev);
    const bool bad = __builtin_amdgcn_readfirstlane(*(volatile LAS unsigned*)(lds + L_CNT + 4 * C_BAD)) != 0u;
    if (!bad && arr == 3u && lane == 0) __hip_atomic_store((GAS u64*)(ws + WS_FO + fme), tag, RLX_AGENT);
    if (__builtin_expect(bad, 0)) {
#pragma unroll
      for (int q = 0; q < 4; ++q) { set_ref(qf[q][1], 0.f, hh); o[q] = zero; mref[q] = -3.0e38f; }
#pragma nounroll
      for (int kt = 0; kt < 16; ++kt) {
        const u32x4 k0 = LD16(kvb + kt * 4096), k1 = LD16(kvb + kt * 4096 + 1024), v0 = LD16(kvb + kt * 4096 + 2048), v1 = LD16(kvb + kt * 4096 + 3072);
#pragma unroll
        for (int q = 0; q < 4; ++q) {
          f32x16 s = MFMA32(asfrag(k0), qf[q][0], zero); s = MFMA32(asfrag(k1), qf[q][1], s);
          const float mn = __builtin_fmaxf(mref[q], tile_max(s)), al = __builtin_amdgcn_exp2f(mref[q] - mn); mref[q] = mn;
#pragma unroll
          for (int r = 0; r < 16; ++r) { o[q][r] *= al; s[r] = __builtin_amdgcn_exp2f(s[r] - mn); }
          const u32x4 p0 = {cvtpk(s[0], s[1]), cvtpk(s[2], s[3]), cvtpk(s[4], s[5]), cvtpk(s[6], s[7])}, p1 = {cvtpk(s[8], s[9]), cvtpk(s[10], s[11]), cvtpk(s[12], s[13]), cvtpk(s[14], s[15])};
          o[q] = MFMA32(asfrag(v0), asfrag(p0), o[q]); o[q] = MFMA32(asfrag(v1), asfrag(p1), o[q]);
        }
      }
      PUT_O(0); PUT_O(1); PUT_O(2);
      post<A_CMB2>(ev); pwait<A_CMB2>(ev);
      combine();
      WAIT_VM(0);
      if (cnt_add(lds, C_O2) == 3u && lane == 0) __hip_atomic_store((GAS u64*)(ws + WS_FO + fme), tag, RLX_AGENT);
    }
    if (METRIC == 5) cx_ = __builtin_amdgcn_s_memtime();
    { const u64 now = __builtin_amdgcn_s_memrealtime(); const u64 X = 1000 + (cx_ - c0_) / 20; while (__builtin_amdgcn_s_memrealtime() - now < X) __builtin_amdgcn_s_sleep(2); }

  } else {
    Ld k;
    k.xsrc = x + (rb * 128 + t * 32) * 896; k.xvo0 = (lane >> 3) * 896 + (((lane & 7) ^ (lane >> 4)) << 4); k.xvo1 = k.xvo0 ^ 64u;
    k.xdst = lds0 + t * 4096; k.xdst6 = lds0 + (t < 2 ? 18 * SLOT + t * 4096 : (t - 2) * 4096);
#pragma unroll
    for (int i = 0; i < 7; ++i) k.ck[i] = i + r7 >= 7 ? i + r7 - 7 : i + r7;
    { const int row = 16 * t + (lane >> 3); const unsigned piece = ((lane & 7) ^ (((lane >> 4) & 1) << 2)) << 4;
      k.w0rs = j < 14 ? 2688u : 3584u; k.w0src = j < 14 ? wqkv + 896 + j * 128 : w1 + (j - 14) * 128;
      k.fasrc = w1 + (2 + j) * 128; k.dst = lds0 + t * 2048;
      k.w0vo = row * k.w0rs + piece; k.wfvo = row * 3584 + piece;
      k.wqsrc = wqkv + j * 56; k.wqvo = (16 * t + (lane >> 2)) * 2688 + (lane & 3) * 16; k.qdst = lds0 + L_WQ + t * 1024; k.ev = ev; }
    {
      COMMON_SETUP();
      STAMP(14);
      p1_stream<0>(k, xa, xrd, xwrap STA);
      STAMP(15);
      __builtin_amdgcn_s_setprio(GPRIO);
#pragma unroll
      for (int s = 0; s < 4; ++s) { wr0[s] += L_FA; wr1[s] += L_FA; PIN(wr0[s]); PIN(wr1[s]); }
      WF f[3]; f32x16 a0 = {}, a1 = {}; f32x4 c0 = {}, c1 = {}; float cr[16]; u32x4 o[2];
      pwait<L_FAL>(ev);
      wf_read(f[0], wr0[0], wr1[0]); wf_read(f[1], wr0[1], wr1[1]);
      f_steps<0, 0>(ev, xa, f, a0, a1, c0, c1, sel, wr0, wr1);
      cs_put(lds, L_CSA, 0, t, lane, c0); cs_put(lds, L_CSA, 1, t, lane, c1);
      pwait<A_ST>(ev);
      rs = *(LAS float*)(lds + L_STAT + t * 256 + r32 * 4); nmb = *(LAS float*)(lds + L_STAT + t * 256 + 128 + r32 * 4);
      post<L_CSAP>(ev); pwait<L_CSAP>(ev);
      STAMP(2);
      __builtin_amdgcn_s_setprio(FBPRIO);
      { const unsigned ad = WS_H + rt * 114688 + (2 + j) * 4096 + lane * 16;
        lane_to_regs(priv, r32, hh, cs_get(lds, L_CSA, 0, r32), cr); epi_T<true>(a0, cr, rs, nmb, o[0], o[1]); ST16(o[0], ad); ST16(o[1], ad + 1024);
        lane_to_regs(priv + 128, r32, hh, cs_get(lds, L_CSA, 1, r32), cr); epi_T<true>(a1, cr, rs, nmb, o[0], o[1]); ST16(o[0], ad + 2048); ST16(o[1], ad + 3072); }
      STAMP(3);
      if (j >= 6) {
#pragma unroll
        for (int s = 0; s < 4; ++s) { wr0[s] += L_FB - L_FA; wr1[s] += L_FB - L_FA; PIN(wr0[s]); PIN(wr1[s]); }
        a0 = (f32x16){}; a1 = (f32x16){}; c0 = (f32x4){}; c1 = (f32x4){};
        pwait<A_FB>(ev);
        wf_read(f[0], wr0[0], wr1[0]); wf_read(f[1], wr0[1], wr1[1]);
        f_steps<1, 0>(ev, xa, f, a0, a1, c0, c1, sel, wr0, wr1);
        cs_put(lds, L_CSB, 0, t, lane, c0); cs_put(lds, L_CSB, 1, t, lane, c1);
        post<L_CSBP>(ev); pwait<L_CSBP>(ev);
        const unsigned ad = WS_H + rt * 114688 + (12 + j) * 4096 + lane * 16;
        lane_to_regs(priv, r32, hh, cs_get(lds, L_CSB, 0, r32), cr); epi_T<true>(a0, cr, rs, nmb, o[0], o[1]); ST16(o[0], ad); ST16(o[1], ad + 1024);
        lane_to_regs(priv + 128, r32, hh, cs_get(lds, L_CSB, 1, r32), cr); epi_T<true>(a1, cr, rs, nmb, o[0], o[1]); ST16(o[0], ad + 2048); ST16(o[1], ad + 3072);
      } else post<L_CSBP>(ev);
    }
    __builtin_amdgcn_s_setprio(0);
    u64 tagraw = *(LAS u64*)(lds + L_TAG);
    GET_TAG();
    WAIT_VM(0);
    if (cnt_add(lds, C_H) == 7u && lane == 0) __hip_atomic_store((GAS u64*)(ws + WS_FH + fme), tag, RLX_AGENT);
    STAMP(4);
    const int cg = bi >> 2;
    if (cg < 7) {
      const int rt0 = 8 * xg + 2 * (bi & 3), rb3 = rt0 >> 2;
      const int ring = ring_of(t);
      const int ct = t >> 1, rtl = t & 1;
      G2 g; g.ev = ev; g.t = t; g.ringdst = lds0 + ring; g.lv = lane * 16;
      g.w2vo = (lane >> 3) * 896 + (((lane & 7) ^ (((lane >> 4) & 1) << 2)) << 4);
      g.w2s = w2 + t * 448 * 896 + cg * 128; g.wos = wo + t * 112 * 896 + cg * 128;
      g.hb0 = ws + WS_H + (rt0 + rtl) * 114688 + t * 28672; g.hb1 = ws + WS_H + (rt0 + (rtl ^ 1)) * 114688 + t * 28672;
      g.br0 = wrd0 + ring + ((ct ^ (q4 >> 1)) << 6); g.br1 = wrd0 + ring + ((ct ^ 1 ^ (q4 >> 1)) << 6);
      g2_ws<0, 12>(g);
      const int fb3 = rb3 * 16 + (lane & 15);
      __builtin_amdgcn_s_setprio(0);
      poll_flags<8>(ws + WS_FH + fb3 * 128, tag);
      __builtin_amdgcn_s_setprio(G2PRIO);
      WAIT_VM(0);
      STAMP(5);
      u32x4 hf[14][2];
#pragma unroll
      for (int s = 0; s < 14; ++s) { ldg_sc1(hf[s][0], g.hb0 + s * 1024, g.lv); ldg_sc1(hf[s][1], g.hb1 + s * 1024, g.lv); }
      f32x16 a00 = {}, a01 = {}, a10 = {}, a11 = {};
      PIN(g.br0); PIN(g.br1);
      { WF f[3]; wf_read(f[0], g.br0, g.br1); wf_read(f[1], g.br0 + 2048, g.br1 + 2048);
        g2_steps<0>(g, hf, f, a00, a01, a10, a11); }
      STAMP(6);
      WAIT_VM(0); post<L_WO>(ev);
      const size_t oo = (size_t)((rt0 + rtl) * 32 + r32) * 896 + cg * 128 + ct * 64 + hh * 16;
      const u32x4 xr0 = *(const GAS u32x4*)(x + oo), xr1 = *(const GAS u32x4*)(x + oo + 32);
      pwait<L_WO>(ev);
      bf16x8 wf[16][2];
#pragma unroll
      for (int g2 = 0; g2 < 2; ++g2) { const lptr o0 = lds + ring_of(2 * ct + g2) + SLOT + (4 * hh + q4) * 128 + pi_std * 8;
#pragma unroll
        for (int e = 0; e < 8; ++e)
#pragma unroll
          for (int cc = 0; cc < 2; ++cc) wf[8 * g2 + e][cc] = trfrag(o0 + ((cc ^ ct ^ (q4 >> 1)) << 6) + ((e >> 1) * 28 + (e & 1) * 16) * 128, 1024); }
      post<L_WOR>(ev); pwait<L_WOR>(ev);
      auto put = [&](const f32x16& a, int e) { const lptr m = lds + ring + e * 4096 + lane * 16;
#pragma unroll
        for (int i = 0; i < 4; ++i) *(LAS f32x4*)(m + i * 1024) = (f32x4){a[4 * i], a[4 * i + 1], a[4 * i + 2], a[4 * i + 3]}; };
      auto add = [&](f32x16& a, int src, int e) { const lptr m = lds + ring_of(src) + e * 4096 + lane * 16;
#pragma unroll
        for (int i = 0; i < 4; ++i) { const f32x4 v = ldsf4(m + i * 1024);
#pragma unroll
          for (int n = 0; n < 4; ++n) a[4 * i + n] = sc1(a[4 * i + n] + v[n]); } };
      put(a11, 3); put(a10, 2); put(a01, 1);
      post<L_RED>(ev); pwait<L_RED>(ev);
      add(a00, t ^ 1, 1); add(a00, t ^ 2, 2); add(a00, t ^ 3, 3);
#pragma unroll
      for (int p = 0; p < 4; ++p) { a00[2 * p] = sc1(a00[2 * p] + bflo(xr0[p])); a00[2 * p + 1] = sc1(a00[2 * p + 1] + bfhi(xr0[p]));
        a00[8 + 2 * p] = sc1(a00[8 + 2 * p] + bflo(xr1[p])); a00[9 + 2 * p] = sc1(a00[9 + 2 * p] + bfhi(xr1[p])); }
      a10 = (f32x16){}; PIN(a00); PIN(a10);
      STAMP(11);
      { const char* fl = ws + WS_FO + (rb3 * 16 + ct * 8 + (lane & 7)) * 128; const unsigned ob = WS_O + (rt0 + rtl) * 32768 + ct * 16384 + lane * 16;
        unsigned ready = 0; u32x4 of[8][2];
#pragma unroll
        for (int h = 0; h < 8 + OLAG; ++h) {
          if (h < 8) {
            if (__builtin_expect(!((ready >> h) & 1u), 0)) ready = poll_bit(fl, tag, h);
            if (h == 7) STAMP(7);
            of[h][0] = LD16(ob + h * 2048); of[h][1] = LD16(ob + h * 2048 + 1024); }
          if (h >= OLAG) {
#pragma unroll
            for (int s = 0; s < 2; ++s) { const bf16x8 o_ = asfrag(of[h - OLAG][s]); a10 = MFMA32(wf[2 * (h - OLAG) + s][1], o_, a10); a00 = MFMA32(wf[2 * (h - OLAG) + s][0], o_, a00); }
            PIN(a10); PIN(a00); }
          SBAR(); } }
      STAMP(9);
      put(a10, 0);
      post<L_FIN>(ev);
      STAMP(10); pwait<L_FIN>(ev);
      add(a00, t ^ 2, 0);
      u32x4 r0, r1;
#pragma unroll
      for (int p = 0; p < 4; ++p) { r0[p] = cvtpk(a00[2 * p], a00[2 * p + 1]); r1[p] = cvtpk(a00[8 + 2 * p], a00[9 + 2 * p]); }
      *(GAS u32x4*)(out + oo) = r0; *(GAS u32x4*)(out + oo + 32) = r1;
      if (b == 0 && tid == 256) __hip_atomic_store((GAS u64*)(ws + WS_EPOCH), tag, RLX_AGENT);
      STAMP(8);
    }
    STAMP_OUT();
  }
}

extern "C" void kernel_launch(void* const* d_in, const int* in_sizes, int n_in, void* d_out, int out_size, void* d_ws, size_t ws_size, hipStream_t stream) {
  fused_block<<<256, 512, 0, stream>>>((const char*)d_in[0], (const char*)d_in[1], (const char*)d_in[2], (const char*)d_in[4], (const char*)d_in[5], (char*)d_out, (char*)d_ws + WS_BASE);
}
```

```cpp
#include <hip/hip_runtime.h>
#include <stdint.h>

typedef short bf16x8 __attribute__((ext_vector_type(8)));
typedef short s16x4 __attribute__((ext_vector_type(4)));
typedef float f32x16 __attribute__((ext_vector_type(16)));
typedef float f32x4 __attribute__((ext_vector_type(4)));
typedef float f32x2 __attribute__((ext_vector_type(2)));
typedef __bf16 bf16x2v __attribute__((ext_vector_type(2)));
typedef unsigned u32x4 __attribute__((ext_vector_type(4)));
typedef unsigned long long u64;
#define LAS __attribute__((address_space(3)))
#define GAS __attribute__((address_space(1)))
#define DI __device__ __forceinline__
#define MFMA32(a, b, c) __builtin_amdgcn_mfma_f32_32x32x16_bf16(a, b, c, 0, 0, 0)
#define MFMA16(a, b, c) __builtin_amdgcn_mfma_f32_16x16x32_bf16(a, b, c, 0, 0, 0)
#define SBAR() __builtin_amdgcn_sched_barrier(0)
#define PIN(x) asm volatile("" : "+v"(x))
#define RLX_AGENT __ATOMIC_RELAXED, __HIP_MEMORY_SCOPE_AGENT
#define WAIT_BAR(N) asm volatile("s_waitcnt vmcnt(" #N ") lgkmcnt(0)\n\ts_barrier" ::: "memory")
#define WAIT_VM(N) asm volatile("s_waitcnt vmcnt(" #N ")" ::: "memory")
#define WAIT_LGKM0() asm volatile("s_waitcnt lgkmcnt(0)" ::: "memory")

constexpr unsigned WS_EPOCH = 0;
constexpr unsigned WS_FKV = 4096, WS_FH = WS_FKV + 32768, WS_FO = WS_FH + 32768;
constexpr unsigned WS_KV = 1u << 20;
constexpr unsigned WS_H = 5u << 20;
constexpr unsigned WS_O = 13u << 20;
constexpr unsigned WS_BYTES = 15u << 20;
constexpr int SLOT = 8192, NSLOT = 19;
constexpr int L_MISC = NSLOT * SLOT;
constexpr int L_STAT = L_MISC;
constexpr int L_CS0 = L_MISC + 2048, L_CSA = L_CS0 + 1024, L_CSB = L_CSA + 1024;
constexpr int L_PRIV = L_MISC + 5120;
constexpr int L_CNT = L_MISC + 7168;
constexpr int L_TAG = L_MISC + 7424;
constexpr int LDS_BYTES = 163840;
constexpr int L_FA = 2 * SLOT, L_WQ = 9 * SLOT, L_FB = 13 * SLOT, L_QX = SLOT;
constexpr int ring_of(int t) { return (t < 2 ? 2 + 3 * t : 7 + 3 * t) * SLOT; }
constexpr int p1_slot(int item) { return (item % NSLOT) * SLOT; }
enum { C_KV, C_H, C_O, C_O2, C_BAD,
       A_RD0, A_CS0 = A_RD0 + 6, A_CSX, A_ST, A_FB, A_QW, A_DONE, A_CMB, A_CMB2,
       L_LD0, L_X6 = L_LD0 + 7, L_FAL, L_WQL, L_CSAP, L_CSBP, L_WO, L_WOR, L_RED, L_FIN, N_EV };

#define WS_BASE 0
#define TOUCH 0
#define ROT7 0
#define MAPA 8
#define G2_SPLIT 14
#define CODE_BYTES 55296
constexpr float QSCALE = 0.18898223650461362f * 1.4426950408889634f;
constexpr float REF_MARGIN = 80.f;
constexpr float REF_LIMIT = 4096.f;
constexpr unsigned SPIN_CAP = 1u << 21;

DI unsigned cvtpk(float lo, float hi) { f32x2 v = {lo, hi}; return __builtin_bit_cast(unsigned, __builtin_convertvector(v, bf16x2v)); }
DI float sc1(float x) { asm("" : "+v"(x)); return x; }
DI float bf16r(float x) { return __uint_as_float(cvtpk(x, 0.f) << 16); }
DI float bflo(unsigned v) { return __uint_as_float(v << 16); }
DI float bfhi(unsigned v) { return __uint_as_float(v & 0xffff0000u); }
DI float swap_add(float v) { auto r = __builtin_amdgcn_permlane32_swap(__float_as_uint(v), __float_as_uint(v), false, false); return __uint_as_float(r[0]) + __uint_as_float(r[1]); }
DI float swap_max(float v) { auto r = __builtin_amdgcn_permlane32_swap(__float_as_uint(v), __float_as_uint(v), false, false); return __builtin_fmaxf(__uint_as_float(r[0]), __uint_as_float(r[1])); }
DI float upper_bcast(float v) { auto r = __builtin_amdgcn_permlane32_swap(__float_as_uint(v), __float_as_uint(v), false, false); return __uint_as_float(r[1]); }
DI void glds16(const void* sbase, unsigned voff, unsigned ldsdst) {
  unsigned keep;
  asm volatile("s_mov_b32 %0, m0\n\ts_mov_b32 m0, %3\n\ts_nop 0\n\tglobal_load_lds_dwordx4 %1, %2\n\ts_mov_b32 m0, %0"
               : "=&s"(keep) : "v"(voff), "s"(sbase), "s"(ldsdst) : "memory");
}
typedef LAS char* lptr;
typedef short v4i16_t __attribute__((ext_vector_type(4)));
DI s16x4 trd(lptr p) { return __builtin_bit_cast(s16x4, __builtin_amdgcn_ds_read_tr16_b64_v4i16((LAS v4i16_t*)p)); }
DI bf16x8 trfrag(lptr p, int second) { s16x4 lo = trd(p), hi = trd(p + second); return __builtin_shufflevector(lo, hi, 0, 1, 2, 3, 4, 5, 6, 7); }
DI bf16x8 ldsb128(lptr p) { return *(LAS bf16x8*)p; }
DI f32x4 ldsf4(lptr p) { return *(LAS f32x4*)p; }
DI bf16x8 asfrag(u32x4 v) { return __builtin_bit_cast(bf16x8, v); }
struct Ev { unsigned base, one; };
template <int E> DI void post(const Ev& e) {
  asm volatile("s_mov_b64 exec, 1\n\tds_add_u32 %0, %1 offset:%2\n\ts_mov_b64 exec, -1" :: "v"(e.base), "v"(e.one), "i"(4 * E) : "memory"); }
template <int E> DI void pwait(const Ev& e) {
  unsigned a;
  asm volatile("1:\n\tds_read_b32 %0, %1 offset:%2\n\ts_waitcnt lgkmcnt(0)\n\tv_cmp_gt_u32 vcc, 4, %0\n\ts_cbranch_vccnz 1b"
               : "=&v"(a) : "v"(e.base), "i"(4 * E) : "vcc", "memory"); }
template <int E> DI void pwait_long(const Ev& e) {
  unsigned a;
  asm volatile("1:\n\ts_sleep 8\n\tds_read_b32 %0, %1 offset:%2\n\ts_waitcnt lgkmcnt(0)\n\tv_cmp_gt_u32 vcc, 4, %0\n\ts_cbranch_vccnz 1b"
               : "=&v"(a) : "v"(e.base), "i"(4 * E) : "vcc", "memory"); }
DI unsigned cnt_add(lptr lds, int c) { unsigned r = 0; if ((threadIdx.x & 63) == 0) r = __hip_atomic_fetch_add((LAS unsigned*)(lds + L_CNT + 4 * c), 1u, __ATOMIC_RELAXED, __HIP_MEMORY_SCOPE_WORKGROUP);
  return __builtin_amdgcn_readfirstlane(r); }
template <int SLEEP> DI void poll_flags(const char* flag, u64 tag) {
  u64 v; unsigned n = SPIN_CAP;
  asm volatile("1:\n\tglobal_load_dwordx2 %0, %2, off sc1\n\ts_sub_u32 %1, %1, 1\n\ts_cbranch_scc1 2f\n\ts_sleep %4\n\ts_waitcnt vmcnt(0)\n\t"
               "v_cmp_ne_u64 vcc, %3, %0\n\ts_cbranch_vccnz 1b\n2:\n\ts_waitcnt vmcnt(0)"
               : "=&v"(v), "+s"(n) : "v"(flag), "s"(tag), "i"(SLEEP) : "vcc", "scc", "memory");
}
DI unsigned poll_bit(const char* flag, u64 tag, int h) {
  u64 v, m; unsigned n = SPIN_CAP;
  asm volatile("1:\n\tglobal_load_dwordx2 %0, %3, off sc1\n\ts_sub_u32 %2, %2, 1\n\ts_cbranch_scc1 2f\n\ts_waitcnt vmcnt(0)\n\t"
               "v_cmp_eq_u64 %1, %4, %0\n\ts_nop 3\n\ts_bitcmp0_b64 %1, %5\n\ts_cbranch_scc1 1b\n2:\n\ts_waitcnt vmcnt(0)"
               : "=&v"(v), "=&s"(m), "+s"(n) : "v"(flag), "s"(tag), "i"(h) : "scc", "memory");
  return (unsigned)m;
}
#define STAMP(i) do {} while (0)
#define CSTAMP(i) do {} while (0)
#define VSTAMP(i, v) do {} while (0)
#define STAMP_OUT() do {} while (0)
#define NRESC
#define STV
#define STA
#define FSTAMP(i) do {} while (0)
#define LSTAMP(i) do {} while (0)
#define BAR() asm volatile("s_waitcnt lgkmcnt(0)\n\ts_barrier" ::: "memory")

#define QPRIO 3
#define LPRIO 2
#define GPRIO 1
#define G2PRIO 0
#define NOCHECK 0
#define QSPLIT 8
#define OLAG 4
#define FBPRIO 3
#define PRETOUCH 0
#define LOOK 4
DI float pick(u64 m, float hi, float lo) { float r; asm("v_cndmask_b32 %0, %1, %2, %3" : "=v"(r) : "v"(lo), "v"(hi), "s"(m)); return r; }
DI float gram_diag(const f32x16& g) {
  const u64 m1 = 0xaaaaaaaaaaaaaaaaull, m2 = 0xccccccccccccccccull, m4 = 0xf0f0f0f0f0f0f0f0ull, m8 = 0xff00ff00ff00ff00ull, m16 = 0xffff0000ffff0000ull;
  const float b0 = pick(m2, pick(m1, g[3], g[2]), pick(m1, g[1], g[0])), b1 = pick(m2, pick(m1, g[7], g[6]), pick(m1, g[5], g[4]));
  const float b2 = pick(m2, pick(m1, g[11], g[10]), pick(m1, g[9], g[8])), b3 = pick(m2, pick(m1, g[15], g[14]), pick(m1, g[13], g[12]));
  float d = pick(m16, pick(m8, b3, b2), pick(m8, b1, b0));
  asm volatile("s_nop 1" : "+v"(d));
  auto r = __builtin_amdgcn_permlane32_swap(__float_as_uint(d), __float_as_uint(d), false, false);
  return pick(m4, __uint_as_float(r[1]), __uint_as_float(r[0]));
}
template <int C> DI void x_frags(bf16x8 (&xa)[28], const lptr (&xrd)[4], int xwrap) {
#pragma unroll
  for (int s = 0; s < 4; ++s) xa[4 * C + s] = ldsb128(xrd[s] + p1_slot(3 * C) + (C == 6 ? xwrap : 0));
}
DI void row_norm(float s1, float s2, float& rs, float& nmb) {
  const float mean = sc1(s1 * (1.f / 448.f)), var = __builtin_fmaxf(sc1(s2 * (1.f / 448.f)) - mean * mean, 0.f);
  const float sd = bf16r(__builtin_amdgcn_sqrtf(bf16r(bf16r(var) + 1e-5f))); rs = __builtin_amdgcn_rcpf(sd); nmb = -bf16r(mean);
}
struct P1A { lptr x[4], w0[4], w1[4]; int xwrap; Ev ev; };
struct WF { s16x4 l0, h0, l1, h1; };
DI bf16x8 cat(const s16x4& lo, const s16x4& hi) { return __builtin_shufflevector(lo, hi, 0, 1, 2, 3, 4, 5, 6, 7); }
struct P1R { f32x16 a0, a1; f32x4 c0, c1, s1; f32x16 s2; };
#define MM(VT, acc, xf, wf) do { if constexpr (VT) acc = MFMA32(xf, wf, acc); else acc = MFMA32(wf, xf, acc); PIN(acc); SBAR(); } while (0)
template <bool VT, int ST, int C, int S> DI void p1_group(bf16x8 (&xa)[28], WF (&f)[4], P1R& r, const bf16x8& sel, const P1A& p) {
  constexpr int xo = p1_slot(3 * C) - (C >> 1) * 6 * SLOT, wo = p1_slot(3 * C + 2) - (C >> 1) * 6 * SLOT;
  if constexpr (ST >= 0) MM(VT, r.a0, xa[ST], cat(f[ST & 3].l0, f[ST & 3].h0));
  xa[4 * C + S] = ldsb128(p.x[S] + xo); f[S].l0 = trd(p.w0[S] + wo); SBAR();
  if constexpr (ST >= 0) MM(VT, r.a1, xa[ST], cat(f[ST & 3].l1, f[ST & 3].h1));
  f[S].h0 = trd(p.w0[S] + wo + 512); f[S].l1 = trd(p.w1[S] + wo); SBAR();
  if constexpr (ST >= 0) { r.s2 = MFMA32(xa[ST], xa[ST], r.s2); PIN(r.s2); SBAR(); }
  f[S].h1 = trd(p.w1[S] + wo + 512); SBAR();
  if constexpr (ST >= 0) { r.s1 = MFMA16(sel, xa[ST], r.s1); PIN(r.s1);
    if constexpr ((ST & 3) == 0) { r.c0 = MFMA16(sel, cat(f[0].l0, f[0].h0), r.c0); r.c1 = MFMA16(sel, cat(f[0].l1, f[0].h1), r.c1); PIN(r.c0); PIN(r.c1); }
    SBAR(); }
}
template <bool VT, int C> DI void p1_compute(bf16x8 (&xa)[28], WF (&f)[4], P1R& r, const bf16x8& sel, P1A& p STV) {
  if constexpr (C == 0) pwait<L_LD0>(p.ev);
  if (C == 0) STAMP(12);
  FSTAMP(15 + C);
  if constexpr (C < 6) {
    p1_group<VT, 4 * C - 2, C, 0>(xa, f, r, sel, p); p1_group<VT, 4 * C - 1, C, 1>(xa, f, r, sel, p); p1_group<VT, 4 * C, C, 2>(xa, f, r, sel, p);
    const unsigned nxt = *(volatile LAS unsigned*)(uintptr_t)(p.ev.base + 4 * (L_LD0 + C + 1));
    p1_group<VT, 4 * C + 1, C, 3>(xa, f, r, sel, p);
    post<A_RD0 + C>(p.ev);
    if constexpr (C == 1 || C == 3) {
#pragma unroll
      for (int s = 0; s < 4; ++s) { p.x[s] += 6 * SLOT; p.w0[s] += 6 * SLOT; p.w1[s] += 6 * SLOT; PIN(p.x[s]); PIN(p.w0[s]); PIN(p.w1[s]); }
    }
    if constexpr (C == 5) {
#pragma unroll
      for (int s = 0; s < 4; ++s) { p.x[s] += p.xwrap; p.w0[s] -= 12 * SLOT; p.w1[s] -= 12 * SLOT; PIN(p.x[s]); PIN(p.w0[s]); PIN(p.w1[s]); }
    }
    SBAR();
    if (__builtin_expect(__builtin_amdgcn_readfirstlane(nxt) < 4u, 0)) pwait<L_LD0 + C + 1>(p.ev);
    p1_compute<VT, C + 1>(xa, f, r, sel, p STA);
  }
}
struct Ld { const char *xsrc, *w0src, *wqsrc, *fasrc; unsigned xvo0, xvo1, w0vo, wfvo, wqvo, w0rs, dst, xdst, xdst6, qdst; Ev ev; int ck[7]; };
template <int N, int E> DI void ld_piece(const Ld& k) {
  const unsigned dst = k.dst + p1_slot(N) + E * 1024;
  if constexpr (N < 21) {
    constexpr int c = N / 3, q = N % 3;
    if constexpr (q < 2) glds16(k.xsrc + (q * 16 + E * 8) * 896 + k.ck[c] * 128, E ? k.xvo1 : k.xvo0, (c < 6 ? k.xdst + p1_slot(3 * c) : k.xdst6) + (2 * q + E) * 1024);
    else glds16(k.w0src + (k.ck[c] * 64 + E * 8) * k.w0rs, k.w0vo, dst);
  } else if constexpr (N < 28) glds16(k.fasrc + (k.ck[N - 21] * 64 + E * 8) * 3584, k.wfvo, dst);
  else { constexpr int c = 2 * (N - 28) + E;
    glds16(k.wqsrc + k.ck[c < 7 ? c : 0] * (64 * 2688), k.wqvo, k.qdst + c * 4096); }
}
template <int N> DI void p1_stream(const Ld& k, bf16x8 (&xa)[28], const lptr (&xrd)[4], int xwrap STV) {
  if constexpr (N < 32) {
    if constexpr (N == 18 || (N > 19 && (N - 19) % 3 == 0)) { pwait<A_RD0 + (N - 18) / 3>(k.ev); pwait<L_LD0 + (N - 18) / 3 + 1>(k.ev); }
    ld_piece<N, 0>(k); ld_piece<N, 1>(k);
    if constexpr (N == 2) asm volatile("s_barrier" ::: "memory");
    constexpr int M = N - LOOK;
    if constexpr (M >= 2 && M <= 20 && M % 3 == 2) {
      constexpr int C = M / 3;
      asm volatile("s_waitcnt vmcnt(%0)" :: "i"(2 * LOOK) : "memory");
      post<L_LD0 + C>(k.ev);
      if (C == 0) STAMP(12);
      x_frags<C>(xa, xrd, xwrap); SBAR();
      if (C == 6) STAMP(1);
    } else if constexpr (M == 21) post<L_X6>(k.ev);
    else if constexpr (M == 27) { asm volatile("s_waitcnt vmcnt(%0)" :: "i"(2 * LOOK) : "memory"); post<L_FAL>(k.ev); }
    p1_stream<N + 1>(k, xa, xrd, xwrap STA);
  }
}
DI void cs_put(lptr lds, int area, int c, int t, int lane, const f32x4& cs) {
  if (lane < 16) { *(LAS float*)(lds + area + c * 512 + lane * 16 + t * 4) = cs[0]; *(LAS float*)(lds + area + c * 512 + (lane + 16) * 16 + t * 4) = cs[1]; }
}
DI float cs_get(lptr lds, int area, int c, int r32) { const f32x4 p = ldsf4(lds + area + c * 512 + r32 * 16); return sc1(sc1(p[0] + p[1]) + sc1(p[2] + p[3])); }
DI void lane_to_regs(lptr priv, int r32, int hh, float v, float (&o)[16]) {
  *(LAS float*)(priv + r32 * 4) = v;
#pragma unroll
  for (int i = 0; i < 4; ++i) { const f32x4 q = ldsf4(priv + (8 * i + 4 * hh) * 4); o[4 * i] = q[0]; o[4 * i + 1] = q[1]; o[4 * i + 2] = q[2]; o[4 * i + 3] = q[3]; }
}
typedef short s16x2 __attribute__((ext_vector_type(2)));
template <bool RELU> DI void epi_T(const f32x16& acc, const float (&cs)[16], float sc, float nmb, u32x4& o0, u32x4& o1) {
#pragma unroll
  for (int p = 0; p < 8; ++p) {
    const float v0 = sc1(sc1(__builtin_fmaf(cs[2 * p], nmb, acc[2 * p])) * sc), v1 = sc1(sc1(__builtin_fmaf(cs[2 * p + 1], nmb, acc[2 * p + 1])) * sc);
    unsigned w = cvtpk(v0, v1);
    if (RELU) w = __builtin_bit_cast(unsigned, __builtin_elementwise_max(__builtin_bit_cast(s16x2, w), (s16x2){0, 0}));
    if (p < 4) o0[p] = w; else o1[p - 4] = w;
  }
}
#define ST16(v, off) __builtin_amdgcn_raw_buffer_store_b128(v, rsw, off, 0, 16)
#define ABL_NOKV 0
#define LDAUX 16
#define LD16(off) __builtin_amdgcn_raw_buffer_load_b128(rsw, off, 0, LDAUX)

DI float set_ref(bf16x8& q1, float m, int hh) {
  const unsigned w = cvtpk(-__builtin_rintf(m), 0.f);
  u32x4 v = __builtin_bit_cast(u32x4, q1); v[2] = hh ? w : v[2]; q1 = __builtin_bit_cast(bf16x8, v);
  return -__uint_as_float(w << 16);
}
DI float tile_max(const f32x16& s) {
  float a = __builtin_fmaxf(__builtin_fmaxf(s[0], s[1]), s[2]), b = __builtin_fmaxf(__builtin_fmaxf(s[3], s[4]), s[5]);
#pragma unroll
  for (int r = 6; r < 14; r += 4) { a = __builtin_fmaxf(__builtin_fmaxf(a, s[r]), s[r + 1]); b = __builtin_fmaxf(__builtin_fmaxf(b, s[r + 2]), s[r + 3]); }
  a = __builtin_fmaxf(__builtin_fmaxf(a, s[14]), s[15]);
  return swap_max(__builtin_fmaxf(a, b));
}

template <int P, int ST> DI void f_steps(const Ev& ev, const bf16x8 (&xa)[28], WF (&f)[3], f32x16& a0, f32x16& a1, f32x4& c0, f32x4& c1, const bf16x8& sel, const lptr (&w0)[4], const lptr (&w1)[4]) {
  if constexpr (ST < 28) {
    constexpr int N = ST + 2, off = p1_slot((P ? 32 : 21) + ((N < 28 ? N : 0) >> 2)) - (P ? L_FB : L_FA);
    SBAR(); MM(false, a0, xa[ST], cat(f[ST % 3].l0, f[ST % 3].h0));
    if constexpr (N < 28) { f[N % 3].l0 = trd(w0[N & 3] + off); f[N % 3].h0 = trd(w0[N & 3] + off + 512); SBAR(); }
    MM(false, a1, xa[ST], cat(f[ST % 3].l1, f[ST % 3].h1));
    if constexpr (N < 28) { f[N % 3].l1 = trd(w1[N & 3] + off); f[N % 3].h1 = trd(w1[N & 3] + off + 512); SBAR(); }
    if constexpr ((ST & 3) == 0) { c0 = MFMA16(sel, cat(f[ST % 3].l0, f[ST % 3].h0), c0); c1 = MFMA16(sel, cat(f[ST % 3].l1, f[ST % 3].h1), c1); PIN(c0); PIN(c1); }
    if constexpr (P == 0 && ST == 6) { WAIT_VM(0); post<L_WQL>(ev); }
    f_steps<P, ST + 1>(ev, xa, f, a0, a1, c0, c1, sel, w0, w1);
  }
}
DI void wf_read(WF& f, lptr p0, lptr p1) { f.l0 = trd(p0); f.h0 = trd(p0 + 512); f.l1 = trd(p1); f.h1 = trd(p1 + 512); }

constexpr int g2_issued(int st) { int n = 28; for (int s = 0; s < st; ++s) { if (s < 14) n += 2; if (s < 24) n += 2; } return n; }
constexpr int g2_pos_h(int s) { return s < 14 ? 2 * s + 1 : g2_issued(s - 14) + 1; }
constexpr int g2_pos_w(int s) { return s < 12 ? -1 : g2_issued(s - 12) + (s - 12 < 14 ? 2 : 0) + 1; }
constexpr int g2_wait(int st) { const int a = g2_pos_h(st), b = st + 2 < 28 ? g2_pos_w(st + 2) : -1; const int n = g2_issued(st) - 1 - (a > b ? a : b); return n > 63 ? 63 : n; }
DI void ldg_sc1(u32x4& d, const char* sbase, unsigned voff) { asm volatile("global_load_dwordx4 %0, %1, %2 sc1" : "=v"(d) : "v"(voff), "s"(sbase) : "memory"); }
struct G2 { const char *hb0, *hb1, *w2s, *wos; unsigned lv, w2vo, ringdst; Ev ev; int t; lptr br0, br1; };
template <int ST, int E> DI void g2_w(const G2& g) {
  const unsigned dst = g.ringdst + (ST % 12) * 2048 + E * 1024;
  if constexpr (ST < 28) glds16(g.w2s + (ST * 16 + E * 8) * 896, g.w2vo, dst);
  else if constexpr (ST < 35) glds16(g.wos + ((ST - 28) * 16 + E * 8) * 896, g.w2vo, dst);
  else glds16(g.wos + (g.t == 3 ? 104 : 112 + E * 8) * 896, g.w2vo, dst);
}
template <int A, int B> DI void g2_ws(const G2& g) { if constexpr (A < B) { g2_w<A, 0>(g); g2_w<A, 1>(g); g2_ws<A + 1, B>(g); } }
template <int ST> DI void g2_steps(const G2& g, u32x4 (&hf)[14][2], WF (&f)[3], f32x16& a00, f32x16& a01, f32x16& a10, f32x16& a11) {
  if constexpr (ST < 28) {
    if constexpr (ST == G2_SPLIT) { __builtin_amdgcn_s_setprio(0); pwait_long<A_DONE>(g.ev); __builtin_amdgcn_s_setprio(GPRIO); }
    asm volatile("s_waitcnt vmcnt(%2)" : "+v"(hf[ST % 14][0]), "+v"(hf[ST % 14][1]) : "i"(g2_wait(ST)) : "memory");
    constexpr int N = ST + 2, off = (N % 12) * 2048;
    const bf16x8 h0 = asfrag(hf[ST % 14][0]), h1 = asfrag(hf[ST % 14][1]), w0 = cat(f[ST % 3].l0, f[ST % 3].h0), w1 = cat(f[ST % 3].l1, f[ST % 3].h1);
    SBAR(); MM(false, a00, h0, w0);
    if constexpr (N < 28) { f[N % 3].l0 = trd(g.br0 + off); f[N % 3].h0 = trd(g.br0 + off + 512); SBAR(); }
    MM(false, a01, h1, w0);
    if constexpr (N < 28) { f[N % 3].l1 = trd(g.br1 + off); f[N % 3].h1 = trd(g.br1 + off + 512); SBAR(); }
    MM(false, a10, h0, w1);
    MM(false, a11, h1, w1);
    if constexpr (ST < 14) { ldg_sc1(hf[ST][0], g.hb0 + (ST + 14) * 1024, g.lv); ldg_sc1(hf[ST][1], g.hb1 + (ST + 14) * 1024, g.lv); }
    if constexpr (ST < 24) { g2_w<ST + 12, 0>(g); g2_w<ST + 12, 1>(g); }
    g2_steps<ST + 1>(g, hf, f, a00, a01, a10, a11);
  }
}

__global__ void __launch_bounds__(512, 2) fused_block(const char* x, const char* wqkv, const char* wo, const char* w1, const char* w2, char* out, char* ws) {
  asm volatile("" : "+s"(x), "+s"(wqkv), "+s"(wo), "+s"(w1), "+s"(w2), "+s"(out), "+s"(ws));
  __shared__ __attribute__((aligned(16))) char smem[LDS_BYTES];
  u64 pc; asm volatile("s_getpc_b64 %0" : "=s"(pc));
  const lptr lds = (lptr)smem; const unsigned lds0 = (unsigned)(uintptr_t)lds;
  const int tid = threadIdx.x, lane = tid & 63, r32 = lane & 31, hh = lane >> 5;
  const int w = __builtin_amdgcn_readfirstlane(tid >> 6), t = w & 3, c = w >> 2;
  const int b = blockIdx.x, xg = b & 7, bi = b >> 3;
  const int j = (xg & 3) * 4 + (bi >> 3), rb = (xg >> 2) * 8 + (bi & 7);
  const int fme = (rb * 16 + j) * 128, rt = rb * 4 + t;
  STAMP(0); CSTAMP(30); VSTAMP(29, (unsigned)__builtin_amdgcn_s_getreg(20 | (3 << 11)) & 0xfu);
  const int rot = t, r7 = ROT7 ? (rb + 2 * j) % 7 : 0;
  const int q4 = (lane & 15) >> 2, p4 = lane & 3, g16 = (lane >> 4) & 1;
  const int pi_std = 4 * g16 + 2 * (p4 & 1) + (p4 >> 1);
  const lptr wrd0 = lds + (8 * hh + q4) * 128 + pi_std * 8;
  const lptr priv = lds + L_PRIV + w * 256;
  Ev ev = {lds0 + L_CNT, 1u}; PIN(ev.base); PIN(ev.one);
  const __amdgpu_buffer_rsrc_t rsw = __builtin_amdgcn_make_buffer_rsrc(ws, 0, WS_BYTES, 0x00020000);
#define COMMON_SETUP() \
  lptr xrd[4], wr0[4], wr1[4]; \
  _Pragma("unroll") for (int s = 0; s < 4; ++s) { xrd[s] = lds + (t * 32 + r32) * 128 + (((2 * (s ^ rot) + hh) ^ ((r32 >> 1) & 7)) << 4); \
    wr0[s] = wrd0 + ((q4 >> 1) << 6) + (s ^ rot) * 2048; wr1[s] = wrd0 + ((1 ^ (q4 >> 1)) << 6) + (s ^ rot) * 2048; } \
  const int xwrap = (t >> 1) ? -NSLOT * SLOT : 0; \
  bf16x8 sel; { const short one = ((lane & 15) == ((lane >> 4) & 1)) ? (short)0x3f80 : (short)0; sel = (bf16x8){one, one, one, one, one, one, one, one}; } \
  bf16x8 xa[28]; float rs, nmb
#define GET_TAG() PIN(tagraw); const u64 tag = (((u64)(unsigned)__builtin_amdgcn_readfirstlane((unsigned)(tagraw >> 32)) << 32) | (unsigned)__builtin_amdgcn_readfirstlane((unsigned)tagraw)) + 1

  if (c == 0) {
    unsigned cpf0;
    asm volatile("global_load_dword %0, %1, %2" : "=v"(cpf0) : "v"(min((unsigned)(bi * 16 + (tid & 15)) * 128u, (unsigned)CODE_BYTES - 128u)), "s"(pc) : "memory");
    unsigned tch = 0;
    u64 tagraw = __hip_atomic_load((GAS u64*)(ws + WS_EPOCH), RLX_AGENT);
    if (tid < N_EV) *(LAS unsigned*)(lds + L_CNT + 4 * tid) = 0u;
    BAR();
    const bool isK = j < 7, isV = j >= 7 && j < 14;
    bf16x8 qf[4][2]; u64 tag;
    {
      COMMON_SETUP();
      unsigned ad[4]; bool strad[4] = {false, false, false, false};
      if (isV) { const int rho = r32 >> 2, col0 = (j - 7) * 64 + 4 * ((rho & 4) + 2 * (rho & 1) + ((rho >> 1) & 1)) + (r32 & 3);
#pragma unroll
        for (int cc = 0; cc < 2; ++cc) { const int col = col0 + cc * 32, hd = col / 28, d = col - hd * 28;
          ad[cc] = WS_KV + hd * 262144 + rt * 4096 + 2048 + (hh * 32 + d) * 16; ad[2 + cc] = d >= 24 ? ad[cc] + 64 : 0x80000000u; strad[cc] = d == 24; }
      } else if (isK) {
#pragma unroll
        for (int s = 0; s < 4; ++s) { const int P = j * 8 + 2 * s + hh, hp = P / 7, rr = P - hp * 7, hd = 2 * hp + (rr > 3), sg = rr > 3 ? rr - 4 : rr;
          ad[s] = WS_KV + hd * 262144 + rt * 4096 + sg * 512 + r32 * 16; strad[s] = rr == 3; }
      } else {
#pragma unroll
        for (int s = 0; s < 4; ++s) ad[s] = WS_H + rt * 114688 + (j - 14) * 4096 + s * 1024 + lane * 16;
      }
      PIN(ad[0]); PIN(ad[1]); PIN(ad[2]); PIN(ad[3]);
      asm volatile("s_waitcnt vmcnt(0)" : "+v"(cpf0), "+v"(tch), "+v"(tagraw) :: "memory");
      *(LAS u64*)(lds + L_TAG) = tagraw;
      tag = (((u64)(unsigned)__builtin_amdgcn_readfirstlane((unsigned)(tagraw >> 32)) << 32) | (unsigned)__builtin_amdgcn_readfirstlane((unsigned)tagraw)) + 1;
      P1R r = {}; float cr0[16], cr1[16], cl0, cl1;
      { P1A p; p.xwrap = xwrap; p.ev = ev; WF f[4];
#pragma unroll
        for (int s = 0; s < 4; ++s) { p.x[s] = xrd[s]; p.w0[s] = wr0[s]; p.w1[s] = wr1[s]; }
        const lptr csw = lds + L_CS0 + lane * 16 + t * 4;
#define GS(st) do { r.s2 = MFMA32(xa[st], xa[st], r.s2); PIN(r.s2); r.s1 = MFMA16(sel, xa[st], r.s1); PIN(r.s1); SBAR(); } while (0)
#define MA(VT, st) MM(VT, r.a0, xa[st], cat(f[st & 3].l0, f[st & 3].h0))
#define MB(VT, st) MM(VT, r.a1, xa[st], cat(f[st & 3].l1, f[st & 3].h1))
#define WRD(s) do { wf_read(f[s], p.w0[s] + SLOT, p.w1[s] + SLOT); SBAR(); } while (0)
#define P1_TAIL(VT) do { \
        p1_compute<VT, 0>(xa, f, r, sel, p STA); \
        __builtin_amdgcn_s_setprio(3); \
        _Pragma("unroll") for (int s = 0; s < 4; ++s) xa[24 + s] = ldsb128(p.x[s] + 6 * SLOT); \
        SBAR(); GS(22); WRD(0); GS(23); WRD(1); GS(24); \
        r.c0 = MFMA16(sel, cat(f[0].l0, f[0].h0), r.c0); r.c1 = MFMA16(sel, cat(f[0].l1, f[0].h1), r.c1); PIN(r.c0); PIN(r.c1); SBAR(); \
        GS(25); GS(26); \
        if (lane < 16) { *(LAS float*)csw = r.c0[0]; *(LAS float*)(csw + 256) = r.c0[1]; *(LAS float*)(csw + 512) = r.c1[0]; *(LAS float*)(csw + 768) = r.c1[1]; } \
        post<A_CSX>(ev); SBAR(); \
        GS(27); MA(VT, 22); MB(VT, 22); WRD(2); MA(VT, 23); \
        if (lane < 16) { *(LAS float*)(priv + lane * 4) = r.s1[0]; *(LAS float*)(priv + 64 + lane * 4) = r.s1[1]; } \
        const float sx_ = *(LAS float*)(priv + r32 * 4); SBAR(); \
        MB(VT, 23); WRD(3); post<A_CS0>(ev); MA(VT, 24); \
        const float sq_ = gram_diag(r.s2); SBAR(); \
        MB(VT, 24); \
        pwait<A_CSX>(ev); \
        const f32x4 p0_ = ldsf4(lds + L_CS0 + r32 * 16), p1_ = ldsf4(lds + L_CS0 + 512 + r32 * 16); SBAR(); \
        MA(VT, 25); \
        row_norm(sx_, sq_, rs, nmb); PIN(rs); PIN(nmb); SBAR(); \
        MB(VT, 25); \
        cl0 = sc1(sc1(p0_[0] + p0_[1]) + sc1(p0_[2] + p0_[3])); cl1 = sc1(sc1(p1_[0] + p1_[1]) + sc1(p1_[2] + p1_[3])); \
        *(LAS float*)(lds + L_STAT + t * 256 + lane * 4) = hh ? nmb : rs; post<A_ST>(ev); SBAR();     \
        MA(VT, 26); \
        if constexpr (!VT) { lane_to_regs(priv, r32, hh, cl0, cr0); } SBAR(); \
        MA(VT, 27); \
        if constexpr (!VT) { lane_to_regs(priv + 128, r32, hh, cl1, cr1); } SBAR(); \
        MB(VT, 26); MB(VT, 27); } while (0)
        if (isV) P1_TAIL(true); else P1_TAIL(false); }
      STAMP(1);
      u32x4 o[4];
      if (isV) {
#pragma unroll
        for (int i = 0; i < 4; ++i) { const f32x4 rr = ldsf4(lds + L_STAT + t * 256 + (8 * i + 4 * hh) * 4), mm = ldsf4(lds + L_STAT + t * 256 + 128 + (8 * i + 4 * hh) * 4);
#pragma unroll
          for (int e = 0; e < 2; ++e) {
            const int n = 4 * i + 2 * e;
            o[i >> 1][2 * (i & 1) + e] = cvtpk(sc1(sc1(__builtin_fmaf(mm[2 * e], cl0, r.a0[n])) * rr[2 * e]), sc1(sc1(__builtin_fmaf(mm[2 * e + 1], cl0, r.a0[n + 1])) * rr[2 * e + 1]));
            o[2 + (i >> 1)][2 * (i & 1) + e] = cvtpk(sc1(sc1(__builtin_fmaf(mm[2 * e], cl1, r.a1[n])) * rr[2 * e]), sc1(sc1(__builtin_fmaf(mm[2 * e + 1], cl1, r.a1[n + 1])) * rr[2 * e + 1])); } }
        ST16(o[0], ad[0]); ST16(o[1], ad[0] + 1024); ST16(o[2], ad[1]); ST16(o[3], ad[1] + 1024);
#pragma unroll
        for (int cc = 0; cc < 2; ++cc) { const unsigned e = strad[cc] ? 0x3f803f80u : 0u; const u32x4 pad = {e, e, e, e}; ST16(pad, ad[2 + cc]); ST16(pad, ad[2 + cc] + 1024); }
      } else {
        if (isK) {
          epi_T<false>(r.a0, cr0, rs, nmb, o[0], o[1]);
#pragma unroll
          for (int s = 0; s < 2; ++s) { u32x4 v = o[s]; if (strad[s]) { v[2] = 0x3f803f80u; v[3] = 0x00003f80u; } ST16(v, ad[s]); }
          epi_T<false>(r.a1, cr1, rs, nmb, o[2], o[3]);
#pragma unroll
          for (int s = 2; s < 4; ++s) { u32x4 v = o[s]; if (strad[s]) { v[2] = 0x3f803f80u; v[3] = 0x00003f80u; } ST16(v, ad[s]); }
#pragma unroll
          for (int s = 0; s < 4; ++s) if (strad[s]) { const u32x4 e = {o[s][2], o[s][3], 0x3f803f80u, 0x00003f80u}; ST16(e, ad[s] + 262144); }
        } else {
          epi_T<true>(r.a0, cr0, rs, nmb, o[0], o[1]); epi_T<true>(r.a1, cr1, rs, nmb, o[2], o[3]);
#pragma unroll
          for (int s = 0; s < 4; ++s) ST16(o[s], ad[s]);
        }
      }
      STAMP(13);
      if (j >= 6) {
        pwait<A_CS0>(ev); pwait<L_X6>(ev);
        const unsigned vo = (16 * t + (lane >> 3)) * 3584 + (((lane & 7) ^ (((lane >> 4) & 1) << 2)) << 4);
#pragma unroll
        for (int i = 0; i < 14; ++i) { const int c7 = (i >> 1) + r7 >= 7 ? (i >> 1) + r7 - 7 : (i >> 1) + r7;
          glds16(w1 + (12 + j) * 128 + (c7 * 64 + (i & 1) * 8) * 3584, vo, lds0 + t * 2048 + p1_slot(32 + (i >> 1)) + (i & 1) * 1024); }
        asm volatile("s_waitcnt vmcnt(14)" ::: "memory");
      } else WAIT_VM(0);
      if (cnt_add(lds, C_KV) == 3u && lane == 0) __hip_atomic_store((GAS u64*)(ws + WS_FKV + fme), tag, RLX_AGENT);
      if (cnt_add(lds, C_H) == 7u && lane == 0) __hip_atomic_store((GAS u64*)(ws + WS_FH + fme), tag, RLX_AGENT);
      STAMP(2);
      __builtin_amdgcn_s_setprio(QPRIO);
      f32x16 a0, a1; f32x4 c0;
      const int rho = 4 * g16 + p4, sg = 2 * (rho >> 2) + (rho & 1), half = (rho >> 1) & 1;
      const int g = (j & 1) ? (sg < 3 ? 2 * sg + 1 + half : (half ? 7 : 0)) : 2 * sg + half;
      lptr qb[4];
#pragma unroll
      for (int s = 0; s < 4; ++s) { qb[s] = lds + L_WQ + (8 * hh + q4) * 64 + g * 8 + (s ^ rot) * 1024; PIN(qb[s]); }
      a0 = (f32x16){}; a1 = (f32x16){}; c0 = (f32x4){};
      pwait<L_WQL>(ev);
      s16x4 ql[6], qh[6];
#pragma unroll
      for (int st = 0; st < 4; ++st) { ql[st] = trd(qb[st]); qh[st] = trd(qb[st] + 256); }
#pragma unroll
      for (int st = 0; st < 28; ++st) {
        if (st == QSPLIT) {
          WAIT_VM(0); post<A_FB>(ev); }
        SBAR();
        if (st & 1) { a1 = MFMA32(cat(ql[st % 6], qh[st % 6]), xa[st], a1); PIN(a1); } else { a0 = MFMA32(cat(ql[st % 6], qh[st % 6]), xa[st], a0); PIN(a0); }
        SBAR();
        if (st + 4 < 28) { const int o_ = ((st + 4) >> 2) * 4096; ql[(st + 4) % 6] = trd(qb[st & 3] + o_); qh[(st + 4) % 6] = trd(qb[st & 3] + o_ + 256); }
        SBAR();
        c0 = MFMA16(sel, cat(ql[st % 6], qh[st % 6]), c0); PIN(c0);
      }
#pragma unroll
      for (int i = 0; i < 16; ++i) a0[i] = sc1(a0[i] + a1[i]);
      if (lane < 16) { *(LAS float*)(priv + lane * 4) = c0[0]; *(LAS float*)(priv + 64 + lane * 4) = c0[1]; }
      float cr[16];
#pragma unroll
      for (int i = 0; i < 4; ++i) { const f32x4 q = ldsf4(priv + (8 * i + 4 * hh) * 4); cr[4 * i] = q[0]; cr[4 * i + 1] = q[1]; cr[4 * i + 2] = q[2]; cr[4 * i + 3] = q[3]; }
      epi_T<false>(a0, cr, rs * QSCALE, nmb, o[0], o[1]);
      if (hh) { o[1][2] = 0u; o[1][3] = 0u; }
      *(LAS u32x4*)(lds + L_QX + t * 2048 + lane * 16) = o[0]; *(LAS u32x4*)(lds + L_QX + t * 2048 + 1024 + lane * 16) = o[1];
    }
    post<A_QW>(ev);
    const f32x16 zero = {};
    f32x16 o[4]; u32x4 kv[4][4], pA0 = {}, pA1 = {}, pB0 = {}, pB1 = {};
#pragma unroll
    for (int q = 0; q < 4; ++q) { o[q] = zero; PIN(o[q]); }
    kv[3][2] = (u32x4){}; kv[3][3] = (u32x4){}; PIN(kv[3][2]); PIN(kv[3][3]); PIN(pB0); PIN(pB1);
    STAMP(4);
    { const int c0 = (28 * j) >> 6, c1 = (28 * j + 27) >> 6, sel4 = lane >> 4;
      int jj = sel4 == 0 ? c0 : sel4 == 1 ? c1 : sel4 == 2 ? 7 + c0 : 7 + c1;
      if (j == 0 && (sel4 & 1)) jj = 14 + (sel4 >> 1);
      poll_flags<0>(ws + WS_FKV + ((lane & 15) * 16 + jj) * 128, tag); }
    STAMP(5);
    const unsigned kvb = WS_KV + j * 262144 + t * 65536 + lane * 16;
#pragma unroll
    for (int u = 0; u < 3; ++u)
#pragma unroll
      for (int e = 0; e < 4; ++e) kv[u][e] = LD16(kvb + u * 4096 + e * 1024);
    pwait<A_QW>(ev);
#pragma unroll
    for (int q = 0; q < 4; ++q) { const lptr a = lds + L_QX + ((q + t + 1) & 3) * 2048 + lane * 16; qf[q][0] = ldsb128(a); qf[q][1] = ldsb128(a + 1024); }
    float mref[4]; bool redo;
#pragma unroll
    for (int q = 0; q < 4; ++q) {
      f32x16 s = MFMA32(asfrag(kv[0][0]), qf[q][0], zero); s = MFMA32(asfrag(kv[0][1]), qf[q][1], s);
      mref[q] = set_ref(qf[q][1], tile_max(s) + REF_MARGIN, hh);
    }
    redo = !(__builtin_fmaxf(__builtin_fmaxf(__builtin_fabsf(mref[0]), __builtin_fabsf(mref[1])), __builtin_fmaxf(__builtin_fabsf(mref[2]), __builtin_fabsf(mref[3]))) < REF_LIMIT);
    __builtin_amdgcn_s_setprio(LPRIO);
    f32x16 sA, sB;
    sA = MFMA32(asfrag(kv[0][0]), qf[0][0], zero); sA = MFMA32(asfrag(kv[0][1]), qf[0][1], sA);
    PIN(sA); STAMP(6);
    auto area = [&](int dst, int e) -> lptr { const int n = dst * 3 + e; return lds + (n < 10 ? 8 * SLOT + n * 4096 : (n - 10) * 4096) + lane * 16; };
    auto put_o = [&](const f32x16& v, float m, int q) { const lptr a = area((q + t + 1) & 3, q);
      *(LAS f32x4*)a = (f32x4){v[0], v[1], v[2], v[3]}; *(LAS f32x4*)(a + 1024) = (f32x4){v[4], v[5], v[6], v[7]};
      *(LAS f32x4*)(a + 2048) = (f32x4){v[8], v[9], v[10], v[11]}; *(LAS f32x4*)(a + 3072) = (f32x4){v[12], hh ? m : v[13], v[14], v[15]}; };
#define PUT_O(q) put_o(o[q], mref[q], q)
#define EX4(S, i) do { S[i] = __builtin_amdgcn_exp2f(S[i]); S[i + 1] = __builtin_amdgcn_exp2f(S[i + 1]); S[i + 2] = __builtin_amdgcn_exp2f(S[i + 2]); S[i + 3] = __builtin_amdgcn_exp2f(S[i + 3]); } while (0)
#define UNIT(SC, SN, PC0, PC1, PP0, PP1, KN0, KN1, QN, QP, VV0, VV1, EXTRA) do { \
      SBAR(); if (QN >= 0) { SN = MFMA32(asfrag(KN0), qf[QN & 3][0], zero); PIN(SN); } SBAR(); EX4(SC, 0); PIN(SC); SBAR(); \
      if (QN >= 0) { SN = MFMA32(asfrag(KN1), qf[QN & 3][1], SN); PIN(SN); } SBAR(); EX4(SC, 4); PIN(SC); SBAR(); \
      o[QP] = MFMA32(asfrag(VV0), asfrag(PP0), o[QP]); PIN(o[QP]); SBAR(); EXTRA; EX4(SC, 8); PIN(SC); SBAR(); \
      o[QP] = MFMA32(asfrag(VV1), asfrag(PP1), o[QP]); PIN(o[QP]); SBAR(); EX4(SC, 12); SBAR(); \
      PC0 = (u32x4){cvtpk(SC[0], SC[1]), cvtpk(SC[2], SC[3]), cvtpk(SC[4], SC[5]), cvtpk(SC[6], SC[7])}; \
      PC1 = (u32x4){cvtpk(SC[8], SC[9]), cvtpk(SC[10], SC[11]), cvtpk(SC[12], SC[13]), cvtpk(SC[14], SC[15])}; PIN(PC0); PIN(PC1); SBAR(); } while (0)
    const float lthr = hh ? 16777216.f : __builtin_inff();
#define CHECK3 do { float m_; asm volatile("v_max3_f32 %0, %2, %3, %4\n\tv_cmp_lt_f32 %1, %5, %0" : "=&v"(m_), "=s"(g0) : "v"(o[1][12]), "v"(o[2][12]), "v"(o[3][12]), "v"(lthr)); } while (0)
#define CHECK1 asm volatile("v_cmp_lt_f32 %0, %2, %1" : "=s"(g1) : "v"(o[0][12]), "v"(lthr))
#define LDKV(UL, e) kv[UL][e] = __builtin_amdgcn_raw_buffer_load_b128(rsw, kvb + (e) * 1024, nt, LDAUX)
#define KEYTILE(U, UN, UL, LOAD, QLAST, X1, X2, X3) do { const int nt = (kt + (U) + 3) * 4096; \
      UNIT(sA, sB, pA0, pA1, pB0, pB1, kv[U][0], kv[U][1], 1, 3, kv[UL][2], kv[UL][3], if (LOAD) { LDKV(UL, 0); LDKV(UL, 1); }); \
      UNIT(sB, sA, pB0, pB1, pA0, pA1, kv[U][0], kv[U][1], 2, 0, kv[U][2], kv[U][3], if (LOAD) { LDKV(UL, 2); LDKV(UL, 3); } X1); \
      UNIT(sA, sB, pA0, pA1, pB0, pB1, kv[U][0], kv[U][1], 3, 1, kv[U][2], kv[U][3], X2); \
      UNIT(sB, sA, pB0, pB1, pA0, pA1, kv[UN][0], kv[UN][1], QLAST, 2, kv[U][2], kv[U][3], X3); \
    } while (0)
    for (int kt = 0; kt < 12; kt += 4) {
      LSTAMP(16 + (kt >> 1)); KEYTILE(0, 1, 3, true, 0, , , ); KEYTILE(1, 2, 0, true, 0, , , ); LSTAMP(17 + (kt >> 1)); KEYTILE(2, 3, 1, true, 0, , , ); u64 g0, g1;
      KEYTILE(3, 0, 2, true, 0, CHECK3, CHECK1, );
      if (!NOCHECK && __builtin_expect((g0 | g1) != 0ull, 0)) {
        NRESC;
        o[3] = MFMA32(asfrag(kv[3][2]), asfrag(pB0), o[3]); o[3] = MFMA32(asfrag(kv[3][3]), asfrag(pB1), o[3]); pB0 = (u32x4){}; pB1 = (u32x4){};
#pragma unroll
        for (int q = 0; q < 4; ++q) { const float l = upper_bcast(o[q][12]); redo |= !(l < 1.2676506002282294e30f);
          const float mn = set_ref(qf[q][1], mref[q] + (float)max(__builtin_amdgcn_frexp_expf(l) + (int)REF_MARGIN, 0), hh);
          const int e = (int)(mn - mref[q]); mref[q] = mn; redo |= !(__builtin_fabsf(mn) < REF_LIMIT);
#pragma unroll
          for (int r = 0; r < 16; ++r) o[q][r] = __builtin_ldexpf(o[q][r], -e);
          if (q == 0) {
#pragma unroll
            for (int r = 0; r < 16; ++r) sA[r] -= (float)e; } } }
    }
    { const int kt = 12;
      if (__builtin_expect(__any(redo), 0)) { mref[0] = mref[1] = mref[2] = mref[3] = __builtin_nanf(""); }
      pwait<L_CSBP>(ev);
      LSTAMP(22); KEYTILE(0, 1, 3, true, 0, , , ); KEYTILE(1, 2, 0, false, 0, , , ); LSTAMP(23); KEYTILE(2, 3, 1, false, 0, , , );
      post<A_DONE>(ev); __builtin_amdgcn_s_setprio(3);
      KEYTILE(3, 0, 2, false, -1, , PUT_O(0), PUT_O(1)); }
    o[3] = MFMA32(asfrag(kv[3][2]), asfrag(pB0), o[3]); o[3] = MFMA32(asfrag(kv[3][3]), asfrag(pB1), o[3]); PIN(o[3]); SBAR();
    PUT_O(2);
    STAMP(7);
    VSTAMP(28, nresc + (__any(redo) ? 1000 : 0));
    auto combine = [&]() -> bool {
      f32x4 pp[3][4]; f32x16 of;
#pragma unroll
      for (int e = 0; e < 3; ++e) { const lptr a = area(t, e); pp[e][3] = ldsf4(a + 3072); }
#pragma unroll
      for (int e = 0; e < 3; ++e) { const lptr a = area(t, e);
#pragma unroll
        for (int i = 0; i < 3; ++i) pp[e][i] = ldsf4(a + i * 1024); }
      const float l0 = o[3][12], l1 = pp[0][3][0], l2 = pp[1][3][0], l3 = pp[2][3][0], m1 = pp[0][3][1], m2 = pp[1][3][1], m3 = pp[2][3][1];
      const float mx = __builtin_fmaxf(__builtin_fmaxf(mref[3] + (float)__builtin_amdgcn_frexp_expf(l0), m1 + (float)__builtin_amdgcn_frexp_expf(l1)),
                                       __builtin_fmaxf(m2 + (float)__builtin_amdgcn_frexp_expf(l2), m3 + (float)__builtin_amdgcn_frexp_expf(l3)));
      float f0 = __builtin_amdgcn_exp2f(mref[3] - mx), f1 = __builtin_amdgcn_exp2f(m1 - mx), f2 = __builtin_amdgcn_exp2f(m2 - mx), f3 = __builtin_amdgcn_exp2f(m3 - mx);
      const float inv = __builtin_amdgcn_rcpf(__builtin_fmaf(l3, f3, __builtin_fmaf(l2, f2, __builtin_fmaf(l1, f1, l0 * f0))));
      f0 = upper_bcast(f0 * inv); f1 = upper_bcast(f1 * inv); f2 = upper_bcast(f2 * inv); f3 = upper_bcast(f3 * inv);
      const bool big = hh && !(__builtin_fmaxf(__builtin_fmaxf(l0, l1), __builtin_fmaxf(l2, l3)) < 1.2676506002282294e30f);
#pragma unroll
      for (int r = 0; r < 16; ++r) of[r] = sc1(__builtin_fmaf(pp[2][r >> 2][r & 3], f3, sc1(__builtin_fmaf(pp[1][r >> 2][r & 3], f2, sc1(__builtin_fmaf(pp[0][r >> 2][r & 3], f1, sc1(o[3][r] * f0)))))));
      u32x4 e0, e1;
#pragma unroll
      for (int p = 0; p < 4; ++p) { e0[p] = cvtpk(of[2 * p], of[2 * p + 1]); e1[p] = cvtpk(of[8 + 2 * p], of[9 + 2 * p]); }
      if (hh) { e1[2] = 0u; e1[3] = 0u; }
      const unsigned a = WS_O + rt * 32768 + j * 2048 + lane * 16;
      ST16(e0, a); ST16(e1, a + 1024);
      unsigned ex = 0;
#pragma unroll
      for (int p = 0; p < 4; ++p) ex |= ((e0[p] & 0x7f807f80u) + 0x00800080u) | ((e1[p] & 0x7f807f80u) + 0x00800080u);
      return __any((ex & 0x80008000u) != 0u || big);
    };
    post<A_CMB>(ev); STAMP(9); pwait<A_CMB>(ev);
    if (__builtin_expect(combine(), 0)) *(LAS unsigned*)(lds + L_CNT + 4 * C_BAD) = 1u;
    STAMP(10);
    WAIT_VM(0);
    const unsigned arr = cnt_add(lds, C_O);
    __builtin_amdgcn_s_setprio(0);
    if (arr != 3u) pwait_long<C_O>(ev);
    const bool bad = __builtin_amdgcn_readfirstlane(*(volatile LAS unsigned*)(lds + L_CNT + 4 * C_BAD)) != 0u;
    if (!bad && arr == 3u && lane == 0) __hip_atomic_store((GAS u64*)(ws + WS_FO + fme), tag, RLX_AGENT);
    if (__builtin_expect(bad, 0)) {
#pragma unroll
      for (int q = 0; q < 4; ++q) { set_ref(qf[q][1], 0.f, hh); o[q] = zero; mref[q] = -3.0e38f; }
#pragma nounroll
      for (int kt = 0; kt < 16; ++kt) {
        const u32x4 k0 = LD16(kvb + kt * 4096), k1 = LD16(kvb + kt * 4096 + 1024), v0 = LD16(kvb + kt * 4096 + 2048), v1 = LD16(kvb + kt * 4096 + 3072);
#pragma unroll
        for (int q = 0; q < 4; ++q) {
          f32x16 s = MFMA32(asfrag(k0), qf[q][0], zero); s = MFMA32(asfrag(k1), qf[q][1], s);
          const float mn = __builtin_fmaxf(mref[q], tile_max(s)), al = __builtin_amdgcn_exp2f(mref[q] - mn); mref[q] = mn;
#pragma unroll
          for (int r = 0; r < 16; ++r) { o[q][r] *= al; s[r] = __builtin_amdgcn_exp2f(s[r] - mn); }
          const u32x4 p0 = {cvtpk(s[0], s[1]), cvtpk(s[2], s[3]), cvtpk(s[4], s[5]), cvtpk(s[6], s[7])}, p1 = {cvtpk(s[8], s[9]), cvtpk(s[10], s[11]), cvtpk(s[12], s[13]), cvtpk(s[14], s[15])};
          o[q] = MFMA32(asfrag(v0), asfrag(p0), o[q]); o[q] = MFMA32(asfrag(v1), asfrag(p1), o[q]);
        }
      }
      PUT_O(0); PUT_O(1); PUT_O(2);
      post<A_CMB2>(ev); pwait<A_CMB2>(ev);
      combine();
      WAIT_VM(0);
      if (cnt_add(lds, C_O2) == 3u && lane == 0) __hip_atomic_store((GAS u64*)(ws + WS_FO + fme), tag, RLX_AGENT);
    }
    STAMP(8); CSTAMP(31); STAMP_OUT();
  } else {
    Ld k;
    k.xsrc = x + (rb * 128 + t * 32) * 896; k.xvo0 = (lane >> 3) * 896 + (((lane & 7) ^ (lane >> 4)) << 4); k.xvo1 = k.xvo0 ^ 64u;
    k.xdst = lds0 + t * 4096; k.xdst6 = lds0 + (t < 2 ? 18 * SLOT + t * 4096 : (t - 2) * 4096);
#pragma unroll
    for (int i = 0; i < 7; ++i) k.ck[i] = i + r7 >= 7 ? i + r7 - 7 : i + r7;
    { const int row = 16 * t + (lane >> 3); const unsigned piece = ((lane & 7) ^ (((lane >> 4) & 1) << 2)) << 4;
      k.w0rs = j < 14 ? 2688u : 3584u; k.w0src = j < 14 ? wqkv + 896 + j * 128 : w1 + (j - 14) * 128;
      k.fasrc = w1 + (2 + j) * 128; k.dst = lds0 + t * 2048;
      k.w0vo = row * k.w0rs + piece; k.wfvo = row * 3584 + piece;
      k.wqsrc = wqkv + j * 56; k.wqvo = (16 * t + (lane >> 2)) * 2688 + (lane & 3) * 16; k.qdst = lds0 + L_WQ + t * 1024; k.ev = ev; }
    {
      COMMON_SETUP();
      STAMP(14);
      p1_stream<0>(k, xa, xrd, xwrap STA);
      STAMP(15);
      __builtin_amdgcn_s_setprio(GPRIO);
#pragma unroll
      for (int s = 0; s < 4; ++s) { wr0[s] += L_FA; wr1[s] += L_FA; PIN(wr0[s]); PIN(wr1[s]); }
      WF f[3]; f32x16 a0 = {}, a1 = {}; f32x4 c0 = {}, c1 = {}; float cr[16]; u32x4 o[2];
      pwait<L_FAL>(ev);
      wf_read(f[0], wr0[0], wr1[0]); wf_read(f[1], wr0[1], wr1[1]);
      f_steps<0, 0>(ev, xa, f, a0, a1, c0, c1, sel, wr0, wr1);
      cs_put(lds, L_CSA, 0, t, lane, c0); cs_put(lds, L_CSA, 1, t, lane, c1);
      pwait<A_ST>(ev);
      rs = *(LAS float*)(lds + L_STAT + t * 256 + r32 * 4); nmb = *(LAS float*)(lds + L_STAT + t * 256 + 128 + r32 * 4);
      post<L_CSAP>(ev); pwait<L_CSAP>(ev);
      STAMP(2);
      __builtin_amdgcn_s_setprio(FBPRIO);
      { const unsigned ad = WS_H + rt * 114688 + (2 + j) * 4096 + lane * 16;
        lane_to_regs(priv, r32, hh, cs_get(lds, L_CSA, 0, r32), cr); epi_T<true>(a0, cr, rs, nmb, o[0], o[1]); ST16(o[0], ad); ST16(o[1], ad + 1024);
        lane_to_regs(priv + 128, r32, hh, cs_get(lds, L_CSA, 1, r32), cr); epi_T<true>(a1, cr, rs, nmb, o[0], o[1]); ST16(o[0], ad + 2048); ST16(o[1], ad + 3072); }
      STAMP(3);
      if (j >= 6) {
#pragma unroll
        for (int s = 0; s < 4; ++s) { wr0[s] += L_FB - L_FA; wr1[s] += L_FB - L_FA; PIN(wr0[s]); PIN(wr1[s]); }
        a0 = (f32x16){}; a1 = (f32x16){}; c0 = (f32x4){}; c1 = (f32x4){};
        pwait<A_FB>(ev);
        wf_read(f[0], wr0[0], wr1[0]); wf_read(f[1], wr0[1], wr1[1]);
        f_steps<1, 0>(ev, xa, f, a0, a1, c0, c1, sel, wr0, wr1);
        cs_put(lds, L_CSB, 0, t, lane, c0); cs_put(lds, L_CSB, 1, t, lane, c1);
        post<L_CSBP>(ev); pwait<L_CSBP>(ev);
        const unsigned ad = WS_H + rt * 114688 + (12 + j) * 4096 + lane * 16;
        lane_to_regs(priv, r32, hh, cs_get(lds, L_CSB, 0, r32), cr); epi_T<true>(a0, cr, rs, nmb, o[0], o[1]); ST16(o[0], ad); ST16(o[1], ad + 1024);
        lane_to_regs(priv + 128, r32, hh, cs_get(lds, L_CSB, 1, r32), cr); epi_T<true>(a1, cr, rs, nmb, o[0], o[1]); ST16(o[0], ad + 2048); ST16(o[1], ad + 3072);
      } else post<L_CSBP>(ev);
    }
    __builtin_amdgcn_s_setprio(0);
    u64 tagraw = *(LAS u64*)(lds + L_TAG);
    GET_TAG();
    WAIT_VM(0);
    if (cnt_add(lds, C_H) == 7u && lane == 0) __hip_atomic_store((GAS u64*)(ws + WS_FH + fme), tag, RLX_AGENT);
    STAMP(4);
    const int cg = bi >> 2;
    if (cg < 7) {
      const int rt0 = 8 * xg + 2 * (bi & 3), rb3 = rt0 >> 2;
      const int ring = ring_of(t);
      const int ct = t >> 1, rtl = t & 1;
      G2 g; g.ev = ev; g.t = t; g.ringdst = lds0 + ring; g.lv = lane * 16;
      g.w2vo = (lane >> 3) * 896 + (((lane & 7) ^ (((lane >> 4) & 1) << 2)) << 4);
      g.w2s = w2 + t * 448 * 896 + cg * 128; g.wos = wo + t * 112 * 896 + cg * 128;
      g.hb0 = ws + WS_H + (rt0 + rtl) * 114688 + t * 28672; g.hb1 = ws + WS_H + (rt0 + (rtl ^ 1)) * 114688 + t * 28672;
      g.br0 = wrd0 + ring + ((ct ^ (q4 >> 1)) << 6); g.br1 = wrd0 + ring + ((ct ^ 1 ^ (q4 >> 1)) << 6);
      g2_ws<0, 12>(g);
      const int fb3 = rb3 * 16 + (lane & 15);
      __builtin_amdgcn_s_setprio(0);
      poll_flags<8>(ws + WS_FH + fb3 * 128, tag);
      __builtin_amdgcn_s_setprio(G2PRIO);
      WAIT_VM(0);
      STAMP(5);
      u32x4 hf[14][2];
#pragma unroll
      for (int s = 0; s < 14; ++s) { ldg_sc1(hf[s][0], g.hb0 + s * 1024, g.lv); ldg_sc1(hf[s][1], g.hb1 + s * 1024, g.lv); }
      f32x16 a00 = {}, a01 = {}, a10 = {}, a11 = {};
      PIN(g.br0); PIN(g.br1);
      { WF f[3]; wf_read(f[0], g.br0, g.br1); wf_read(f[1], g.br0 + 2048, g.br1 + 2048);
        g2_steps<0>(g, hf, f, a00, a01, a10, a11); }
      STAMP(6);
      WAIT_VM(0); post<L_WO>(ev);
      const size_t oo = (size_t)((rt0 + rtl) * 32 + r32) * 896 + cg * 128 + ct * 64 + hh * 16;
      const u32x4 xr0 = *(const GAS u32x4*)(x + oo), xr1 = *(const GAS u32x4*)(x + oo + 32);
      pwait<L_WO>(ev);
      bf16x8 wf[16][2];
#pragma unroll
      for (int g2 = 0; g2 < 2; ++g2) { const lptr o0 = lds + ring_of(2 * ct + g2) + SLOT + (4 * hh + q4) * 128 + pi_std * 8;
#pragma unroll
        for (int e = 0; e < 8; ++e)
#pragma unroll
          for (int cc = 0; cc < 2; ++cc) wf[8 * g2 + e][cc] = trfrag(o0 + ((cc ^ ct ^ (q4 >> 1)) << 6) + ((e >> 1) * 28 + (e & 1) * 16) * 128, 1024); }
      post<L_WOR>(ev); pwait<L_WOR>(ev);
      auto put = [&](const f32x16& a, int e) { const lptr m = lds + ring + e * 4096 + lane * 16;
#pragma unroll
        for (int i = 0; i < 4; ++i) *(LAS f32x4*)(m + i * 1024) = (f32x4){a[4 * i], a[4 * i + 1], a[4 * i + 2], a[4 * i + 3]}; };
      auto add = [&](f32x16& a, int src, int e) { const lptr m = lds + ring_of(src) + e * 4096 + lane * 16;
#pragma unroll
        for (int i = 0; i < 4; ++i) { const f32x4 v = ldsf4(m + i * 1024);
#pragma unroll
          for (int n = 0; n < 4; ++n) a[4 * i + n] = sc1(a[4 * i + n] + v[n]); } };
      put(a11, 3); put(a10, 2); put(a01, 1);
      post<L_RED>(ev); pwait<L_RED>(ev);
      add(a00, t ^ 1, 1); add(a00, t ^ 2, 2); add(a00, t ^ 3, 3);
#pragma unroll
      for (int p = 0; p < 4; ++p) { a00[2 * p] = sc1(a00[2 * p] + bflo(xr0[p])); a00[2 * p + 1] = sc1(a00[2 * p + 1] + bfhi(xr0[p]));
        a00[8 + 2 * p] = sc1(a00[8 + 2 * p] + bflo(xr1[p])); a00[9 + 2 * p] = sc1(a00[9 + 2 * p] + bfhi(xr1[p])); }
      a10 = (f32x16){}; PIN(a00); PIN(a10);
      STAMP(11);
      { const char* fl = ws + WS_FO + (rb3 * 16 + ct * 8 + (lane & 7)) * 128; const unsigned ob = WS_O + (rt0 + rtl) * 32768 + ct * 16384 + lane * 16;
        unsigned ready = 0; u32x4 of[8][2];
#pragma unroll
        for (int h = 0; h < 8 + OLAG; ++h) {
          if (h < 8) {
            if (__builtin_expect(!((ready >> h) & 1u), 0)) ready = poll_bit(fl, tag, h);
            if (h == 7) STAMP(7);
            of[h][0] = LD16(ob + h * 2048); of[h][1] = LD16(ob + h * 2048 + 1024); }
          if (h >= OLAG) {
#pragma unroll
            for (int s = 0; s < 2; ++s) { const bf16x8 o_ = asfrag(of[h - OLAG][s]); a10 = MFMA32(wf[2 * (h - OLAG) + s][1], o_, a10); a00 = MFMA32(wf[2 * (h - OLAG) + s][0], o_, a00); }
            PIN(a10); PIN(a00); }
          SBAR(); } }
      STAMP(9);
      put(a10, 0);
      post<L_FIN>(ev);
      STAMP(10); pwait<L_FIN>(ev);
      add(a00, t ^ 2, 0);
      u32x4 r0, r1;
#pragma unroll
      for (int p = 0; p < 4; ++p) { r0[p] = cvtpk(a00[2 * p], a00[2 * p + 1]); r1[p] = cvtpk(a00[8 + 2 * p], a00[9 + 2 * p]); }
      *(GAS u32x4*)(out + oo) = r0; *(GAS u32x4*)(out + oo + 32) = r1;
      if (b == 0 && tid == 256) __hip_atomic_store((GAS u64*)(ws + WS_EPOCH), tag, RLX_AGENT);
      STAMP(8);
    }
    STAMP_OUT();
  }
}

extern "C" void kernel_launch(void* const* d_in, const int* in_sizes, int n_in, void* d_out, int out_size, void* d_ws, size_t ws_size, hipStream_t stream) {
  fused_block<<<256, 512, 0, stream>>>((const char*)d_in[0], (const char*)d_in[1], (const char*)d_in[2], (const char*)d_in[4], (const char*)d_in[5], (char*)d_out, (char*)d_ws + WS_BASE);
}
```

```cpp
#include <hip/hip_runtime.h>
#include <stdint.h>

typedef short bf16x8 __attribute__((ext_vector_type(8)));
typedef short s16x4 __attribute__((ext_vector_type(4)));
typedef float f32x16 __attribute__((ext_vector_type(16)));
typedef float f32x4 __attribute__((ext_vector_type(4)));
typedef float f32x2 __attribute__((ext_vector_type(2)));
typedef __bf16 bf16x2v __attribute__((ext_vector_type(2)));
typedef unsigned u32x4 __attribute__((ext_vector_type(4)));
typedef unsigned long long u64;
#define LAS __attribute__((address_space(3)))
#define GAS __attribute__((address_space(1)))
#define DI __device__ __forceinline__
#define MFMA32(a, b, c) __builtin_amdgcn_mfma_f32_32x32x16_bf16(a, b, c, 0, 0, 0)
#define MFMA16(a, b, c) __builtin_amdgcn_mfma_f32_16x16x32_bf16(a, b, c, 0, 0, 0)
#define SBAR() __builtin_amdgcn_sched_barrier(0)
#define PIN(x) asm volatile("" : "+v"(x))
#define RLX_AGENT __ATOMIC_RELAXED, __HIP_MEMORY_SCOPE_AGENT
#define WAIT_BAR(N) asm volatile("s_waitcnt vmcnt(" #N ") lgkmcnt(0)\n\ts_barrier" ::: "memory")
#define WAIT_VM(N) asm volatile("s_waitcnt vmcnt(" #N ")" ::: "memory")
#define WAIT_LGKM0() asm volatile("s_waitcnt lgkmcnt(0)" ::: "memory")

constexpr unsigned WS_EPOCH = 0;
constexpr unsigned WS_FKV = 4096, WS_FH = WS_FKV + 32768, WS_FO = WS_FH + 32768;
constexpr unsigned WS_KV = 1u << 20;
constexpr unsigned WS_H = 5u << 20;
constexpr unsigned WS_O = 13u << 20;
constexpr unsigned WS_BYTES = 15u << 20;
constexpr int SLOT = 8192, NSLOT = 19;
constexpr int L_MISC = NSLOT * SLOT;
constexpr int L_STAT = L_MISC;
constexpr int L_CS0 = L_MISC + 2048, L_CSA = L_CS0 + 1024, L_CSB = L_CSA + 1024;
constexpr int L_PRIV = L_MISC + 5120;
constexpr int L_CNT = L_MISC + 7168;
constexpr int L_TAG = L_MISC + 7424;
constexpr int LDS_BYTES = 163840;
constexpr int L_FA = 2 * SLOT, L_WQ = 9 * SLOT, L_FB = 13 * SLOT, L_QX = SLOT;
constexpr int ring_of(int t) { return (t < 2 ? 2 + 3 * t : 7 + 3 * t) * SLOT; }
constexpr int p1_slot(int item) { return (item % NSLOT) * SLOT; }
enum { C_KV, C_H, C_O, C_O2, C_BAD,
       A_RD0, A_CS0 = A_RD0 + 6, A_CSX, A_ST, A_FB, A_QW, A_DONE, A_CMB, A_CMB2,
       L_LD0, L_X6 = L_LD0 + 7, L_FAL, L_WQL, L_CSAP, L_CSBP, L_WO, L_RED, L_FIN, N_EV };

#define WS_BASE 0
#define TOUCH 0
#define ROT7 0
#define MAPA 8
#define G2_SPLIT 10
#define CODE_BYTES 55296
constexpr float QSCALE = 0.18898223650461362f * 1.4426950408889634f;
constexpr float REF_MARGIN = 80.f;
constexpr float REF_LIMIT = 4096.f;
constexpr unsigned SPIN_CAP = 1u << 21;

DI unsigned cvtpk(float lo, float hi) { f32x2 v = {lo, hi}; return __builtin_bit_cast(unsigned, __builtin_convertvector(v, bf16x2v)); }
DI float sc1(float x) { asm("" : "+v"(x)); return x; }
DI float bf16r(float x) { return __uint_as_float(cvtpk(x, 0.f) << 16); }
DI float bflo(unsigned v) { return __uint_as_float(v << 16); }
DI float bfhi(unsigned v) { return __uint_as_float(v & 0xffff0000u); }
DI float swap_add(float v) { auto r = __builtin_amdgcn_permlane32_swap(__float_as_uint(v), __float_as_uint(v), false, false); return __uint_as_float(r[0]) + __uint_as_float(r[1]); }
DI float swap_max(float v) { auto r = __builtin_amdgcn_permlane32_swap(__float_as_uint(v), __float_as_uint(v), false, false); return __builtin_fmaxf(__uint_as_float(r[0]), __uint_as_float(r[1])); }
DI float upper_bcast(float v) { auto r = __builtin_amdgcn_permlane32_swap(__float_as_uint(v), __float_as_uint(v), false, false); return __uint_as_float(r[1]); }
DI void glds16(const void* sbase, unsigned voff, unsigned ldsdst) {
  unsigned keep;
  asm volatile("s_mov_b32 %0, m0\n\ts_mov_b32 m0, %3\n\ts_nop 0\n\tglobal_load_lds_dwordx4 %1, %2\n\ts_mov_b32 m0, %0"
               : "=&s"(keep) : "v"(voff), "s"(sbase), "s"(ldsdst) : "memory");
}
typedef LAS char* lptr;
typedef short v4i16_t __attribute__((ext_vector_type(4)));
DI s16x4 trd(lptr p) { return __builtin_bit_cast(s16x4, __builtin_amdgcn_ds_read_tr16_b64_v4i16((LAS v4i16_t*)p)); }
DI bf16x8 trfrag(lptr p, int second) { s16x4 lo = trd(p), hi = trd(p + second); return __builtin_shufflevector(lo, hi, 0, 1, 2, 3, 4, 5, 6, 7); }
DI bf16x8 ldsb128(lptr p) { return *(LAS bf16x8*)p; }
DI f32x4 ldsf4(lptr p) { return *(LAS f32x4*)p; }
DI bf16x8 asfrag(u32x4 v) { return __builtin_bit_cast(bf16x8, v); }
struct Ev { unsigned base, one; };
template <int E> DI void post(const Ev& e) {
  asm volatile("s_mov_b64 exec, 1\n\tds_add_u32 %0, %1 offset:%2\n\ts_mov_b64 exec, -1" :: "v"(e.base), "v"(e.one), "i"(4 * E) : "memory"); }
template <int E> DI void pwait(const Ev& e) {
  unsigned a;
  asm volatile("1:\n\tds_read_b32 %0, %1 offset:%2\n\ts_waitcnt lgkmcnt(0)\n\tv_cmp_gt_u32 vcc, 4, %0\n\ts_cbranch_vccnz 1b"
               : "=&v"(a) : "v"(e.base), "i"(4 * E) : "vcc", "memory"); }
template <int E> DI void pwait_long(const Ev& e) {
  unsigned a;
  asm volatile("1:\n\ts_sleep 8\n\tds_read_b32 %0, %1 offset:%2\n\ts_waitcnt lgkmcnt(0)\n\tv_cmp_gt_u32 vcc, 4, %0\n\ts_cbranch_vccnz 1b"
               : "=&v"(a) : "v"(e.base), "i"(4 * E) : "vcc", "memory"); }
DI unsigned cnt_add(lptr lds, int c) { unsigned r = 0; if ((threadIdx.x & 63) == 0) r = __hip_atomic_fetch_add((LAS unsigned*)(lds + L_CNT + 4 * c), 1u, __ATOMIC_RELAXED, __HIP_MEMORY_SCOPE_WORKGROUP);
  return __builtin_amdgcn_readfirstlane(r); }
template <int SLEEP> DI void poll_flags(const char* flag, u64 tag) {
  u64 v; unsigned n = SPIN_CAP;
  asm volatile("1:\n\tglobal_load_dwordx2 %0, %2, off sc1\n\ts_sub_u32 %1, %1, 1\n\ts_cbranch_scc1 2f\n\ts_sleep %4\n\ts_waitcnt vmcnt(0)\n\t"
               "v_cmp_ne_u64 vcc, %3, %0\n\ts_cbranch_vccnz 1b\n2:\n\ts_waitcnt vmcnt(0)"
               : "=&v"(v), "+s"(n) : "v"(flag), "s"(tag), "i"(SLEEP) : "vcc", "scc", "memory");
}
DI unsigned poll_bit(const char* flag, u64 tag, int h) {
  u64 v, m; unsigned n = SPIN_CAP;
  asm volatile("1:\n\tglobal_load_dwordx2 %0, %3, off sc1\n\ts_sub_u32 %2, %2, 1\n\ts_cbranch_scc1 2f\n\ts_waitcnt vmcnt(0)\n\t"
               "v_cmp_eq_u64 %1, %4, %0\n\ts_nop 3\n\ts_bitcmp0_b64 %1, %5\n\ts_cbranch_scc1 1b\n2:\n\ts_waitcnt vmcnt(0)"
               : "=&v"(v), "=&s"(m), "+s"(n) : "v"(flag), "s"(tag), "i"(h) : "scc", "memory");
  return (unsigned)m;
}
#define STAMP(i) do {} while (0)
#define CSTAMP(i) do {} while (0)
#define VSTAMP(i, v) do {} while (0)
#define STAMP_OUT() do {} while (0)
#define NRESC
#define STV
#define STA
#define FSTAMP(i) do {} while (0)
#define LSTAMP(i) do {} while (0)
#define BAR() asm volatile("s_waitcnt lgkmcnt(0)\n\ts_barrier" ::: "memory")

#define QPRIO 3
#define LPRIO 2
#define GPRIO 1
#define G2PRIO 0
#define NOCHECK 0
#define QSPLIT 8
#define OLAG 4
#define FBPRIO 3
#define NOFB 0x003fu
static_assert(__builtin_popcount(NOFB) == 6, "w1 has 28 lines");
#define HSLEEP 6
#define P1PRIO 2
#define FDPRIO 0
#define LOOK 4
DI float pick(u64 m, float hi, float lo) { float r; asm("v_cndmask_b32 %0, %1, %2, %3" : "=v"(r) : "v"(lo), "v"(hi), "s"(m)); return r; }
DI float gram_diag(const f32x16& g) {
  const u64 m1 = 0xaaaaaaaaaaaaaaaaull, m2 = 0xccccccccccccccccull, m4 = 0xf0f0f0f0f0f0f0f0ull, m8 = 0xff00ff00ff00ff00ull, m16 = 0xffff0000ffff0000ull;
  const float b0 = pick(m2, pick(m1, g[3], g[2]), pick(m1, g[1], g[0])), b1 = pick(m2, pick(m1, g[7], g[6]), pick(m1, g[5], g[4]));
  const float b2 = pick(m2, pick(m1, g[11], g[10]), pick(m1, g[9], g[8])), b3 = pick(m2, pick(m1, g[15], g[14]), pick(m1, g[13], g[12]));
  float d = pick(m16, pick(m8, b3, b2), pick(m8, b1, b0));
  asm volatile("s_nop 1" : "+v"(d));
  auto r = __builtin_amdgcn_permlane32_swap(__float_as_uint(d), __float_as_uint(d), false, false);
  return pick(m4, __uint_as_float(r[1]), __uint_as_float(r[0]));
}
template <int C> DI void x_frags(bf16x8 (&xa)[28], const lptr (&xrd)[4], int xwrap) {
#pragma unroll
  for (int s = 0; s < 4; ++s) xa[4 * C + s] = ldsb128(xrd[s] + p1_slot(3 * C) + (C == 6 ? xwrap : 0));
}
DI void row_norm(float s1, float s2, float& rs, float& nmb) {
  const float mean = sc1(s1 * (1.f / 448.f)), var = __builtin_fmaxf(sc1(s2 * (1.f / 448.f)) - mean * mean, 0.f);
  const float sd = bf16r(__builtin_amdgcn_sqrtf(bf16r(bf16r(var) + 1e-5f))); rs = __builtin_amdgcn_rcpf(sd); nmb = -bf16r(mean);
}
struct P1A { lptr x[4], w0[4], w1[4]; int xwrap; Ev ev; };
struct WF { s16x4 l0, h0, l1, h1; };
DI bf16x8 cat(const s16x4& lo, const s16x4& hi) { return __builtin_shufflevector(lo, hi, 0, 1, 2, 3, 4, 5, 6, 7); }
struct P1R { f32x16 a0, a1; f32x4 c0, c1, s1; f32x16 s2; };
#define MM(VT, acc, xf, wf) do { if constexpr (VT) acc = MFMA32(xf, wf, acc); else acc = MFMA32(wf, xf, acc); PIN(acc); SBAR(); } while (0)
template <bool VT, int ST, int C, int S> DI void p1_group(bf16x8 (&xa)[28], WF (&f)[4], P1R& r, const bf16x8& sel, const P1A& p) {
  constexpr int xo = p1_slot(3 * C) - (C >> 1) * 6 * SLOT, wo = p1_slot(3 * C + 2) - (C >> 1) * 6 * SLOT;
  if constexpr (ST >= 0) MM(VT, r.a0, xa[ST], cat(f[ST & 3].l0, f[ST & 3].h0));
  xa[4 * C + S] = ldsb128(p.x[S] + xo); f[S].l0 = trd(p.w0[S] + wo); SBAR();
  if constexpr (ST >= 0) MM(VT, r.a1, xa[ST], cat(f[ST & 3].l1, f[ST & 3].h1));
  f[S].h0 = trd(p.w0[S] + wo + 512); f[S].l1 = trd(p.w1[S] + wo); SBAR();
  if constexpr (ST >= 0) { r.s2 = MFMA32(xa[ST], xa[ST], r.s2); PIN(r.s2); SBAR(); }
  f[S].h1 = trd(p.w1[S] + wo + 512); SBAR();
  if constexpr (ST >= 0) { r.s1 = MFMA16(sel, xa[ST], r.s1); PIN(r.s1);
    if constexpr ((ST & 3) == 0) { r.c0 = MFMA16(sel, cat(f[0].l0, f[0].h0), r.c0); r.c1 = MFMA16(sel, cat(f[0].l1, f[0].h1), r.c1); PIN(r.c0); PIN(r.c1); }
    SBAR(); }
}
template <bool VT, int C> DI void p1_compute(bf16x8 (&xa)[28], WF (&f)[4], P1R& r, const bf16x8& sel, P1A& p STV) {
  if constexpr (C == 0) pwait<L_LD0>(p.ev);
  if (C == 0) STAMP(12);
  FSTAMP(15 + C);
  if constexpr (C < 6) {
    p1_group<VT, 4 * C - 2, C, 0>(xa, f, r, sel, p); p1_group<VT, 4 * C - 1, C, 1>(xa, f, r, sel, p); p1_group<VT, 4 * C, C, 2>(xa, f, r, sel, p);
    const unsigned nxt = *(volatile LAS unsigned*)(uintptr_t)(p.ev.base + 4 * (L_LD0 + C + 1));
    p1_group<VT, 4 * C + 1, C, 3>(xa, f, r, sel, p);
    post<A_RD0 + C>(p.ev);
    if constexpr (C == 1 || C == 3) {
#pragma unroll
      for (int s = 0; s < 4; ++s) { p.x[s] += 6 * SLOT; p.w0[s] += 6 * SLOT; p.w1[s] += 6 * SLOT; PIN(p.x[s]); PIN(p.w0[s]); PIN(p.w1[s]); }
    }
    if constexpr (C == 5) {
#pragma unroll
      for (int s = 0; s < 4; ++s) { p.x[s] += p.xwrap; p.w0[s] -= 12 * SLOT; p.w1[s] -= 12 * SLOT; PIN(p.x[s]); PIN(p.w0[s]); PIN(p.w1[s]); }
    }
    SBAR();
    if (__builtin_expect(__builtin_amdgcn_readfirstlane(nxt) < 4u, 0)) pwait<L_LD0 + C + 1>(p.ev);
    p1_compute<VT, C + 1>(xa, f, r, sel, p STA);
  }
}
struct Ld { const char *xsrc, *w0src, *wqsrc, *fasrc; unsigned xvo0, xvo1, w0vo, wfvo, wqvo, w0rs, dst, xdst, xdst6, qdst; Ev ev; int ck[7]; };
template <int N, int E> DI void ld_piece(const Ld& k) {
  const unsigned dst = k.dst + p1_slot(N) + E * 1024;
  if constexpr (N < 21) {
    constexpr int c = N / 3, q = N % 3;
    if constexpr (q < 2) glds16(k.xsrc + (q * 16 + E * 8) * 896 + k.ck[c] * 128, E ? k.xvo1 : k.xvo0, (c < 6 ? k.xdst + p1_slot(3 * c) : k.xdst6) + (2 * q + E) * 1024);
    else glds16(k.w0src + (k.ck[c] * 64 + E * 8) * k.w0rs, k.w0vo, dst);
  } else if constexpr (N < 28) glds16(k.fasrc + (k.ck[N - 21] * 64 + E * 8) * 3584, k.wfvo, dst);
  else { constexpr int c = 2 * (N - 28) + E;
    glds16(k.wqsrc + k.ck[c < 7 ? c : 0] * (64 * 2688), k.wqvo, k.qdst + c * 4096); }
}
template <int N> DI void p1_stream(const Ld& k, bf16x8 (&xa)[28], const lptr (&xrd)[4], int xwrap STV) {
  if constexpr (N < 32) {
    if constexpr (N == 18 || (N > 19 && (N - 19) % 3 == 0)) { pwait<A_RD0 + (N - 18) / 3>(k.ev); pwait<L_LD0 + (N - 18) / 3 + 1>(k.ev); }
    ld_piece<N, 0>(k); ld_piece<N, 1>(k);
    if constexpr (N == 2) asm volatile("s_barrier" ::: "memory");
    constexpr int M = N - LOOK;
    if constexpr (M >= 2 && M <= 20 && M % 3 == 2) {
      constexpr int C = M / 3;
      asm volatile("s_waitcnt vmcnt(%0)" :: "i"(2 * LOOK) : "memory");
      post<L_LD0 + C>(k.ev);
      if (C == 0) STAMP(12);
      x_frags<C>(xa, xrd, xwrap); SBAR();
      if (C == 6) STAMP(1);
    } else if constexpr (M == 21) post<L_X6>(k.ev);
    else if constexpr (M == 27) { asm volatile("s_waitcnt vmcnt(%0)" :: "i"(2 * LOOK) : "memory"); post<L_FAL>(k.ev); }
    p1_stream<N + 1>(k, xa, xrd, xwrap STA);
  }
}
DI void cs_put(lptr lds, int area, int c, int t, int lane, const f32x4& cs) {
  if (lane < 16) { *(LAS float*)(lds + area + c * 512 + lane * 16 + t * 4) = cs[0]; *(LAS float*)(lds + area + c * 512 + (lane + 16) * 16 + t * 4) = cs[1]; }
}
DI float cs_get(lptr lds, int area, int c, int r32) { const f32x4 p = ldsf4(lds + area + c * 512 + r32 * 16); return sc1(sc1(p[0] + p[1]) + sc1(p[2] + p[3])); }
DI bf16x8 cs_frag(float c) {
  const unsigned hi = __float_as_uint(c) & 0xffff0000u; const float r = c - __uint_as_float(hi);
  const unsigned mid = __float_as_uint(r) & 0xffff0000u; const float lo = r - __uint_as_float(mid);
  return asfrag((u32x4){(hi >> 16) | mid, __float_as_uint(lo) >> 16, 0u, 0u});
}
DI bf16x8 mean_frag(float nmb, int hh) { const unsigned m = hh ? 0u : __float_as_uint(nmb) >> 16; return asfrag((u32x4){m | (m << 16), m, 0u, 0u}); }
typedef short s16x2 __attribute__((ext_vector_type(2)));
template <bool RELU> DI void epi_T(const f32x16& acc, float sc, u32x4& o0, u32x4& o1) {
#pragma unroll
  for (int p = 0; p < 8; ++p) {
    unsigned w = cvtpk(sc1(acc[2 * p] * sc), sc1(acc[2 * p + 1] * sc));
    if (RELU) w = __builtin_bit_cast(unsigned, __builtin_elementwise_max(__builtin_bit_cast(s16x2, w), (s16x2){0, 0}));
    if (p < 4) o0[p] = w; else o1[p - 4] = w;
  }
}
#define ST16(v, off) __builtin_amdgcn_raw_buffer_store_b128(v, rsw, off, 0, 16)
#define ABL_NOKV 0
#define LDAUX 16
#define LD16(off) __builtin_amdgcn_raw_buffer_load_b128(rsw, off, 0, LDAUX)

DI float set_ref(bf16x8& q1, float m, int hh) {
  const unsigned w = cvtpk(-__builtin_rintf(m), 0.f);
  u32x4 v = __builtin_bit_cast(u32x4, q1); v[2] = hh ? w : v[2]; q1 = __builtin_bit_cast(bf16x8, v);
  return -__uint_as_float(w << 16);
}
DI float tile_max(const f32x16& s) {
  float a = __builtin_fmaxf(__builtin_fmaxf(s[0], s[1]), s[2]), b = __builtin_fmaxf(__builtin_fmaxf(s[3], s[4]), s[5]);
#pragma unroll
  for (int r = 6; r < 14; r += 4) { a = __builtin_fmaxf(__builtin_fmaxf(a, s[r]), s[r + 1]); b = __builtin_fmaxf(__builtin_fmaxf(b, s[r + 2]), s[r + 3]); }
  a = __builtin_fmaxf(__builtin_fmaxf(a, s[14]), s[15]);
  return swap_max(__builtin_fmaxf(a, b));
}

template <int P, int ST> DI void f_steps(const Ev& ev, const bf16x8 (&xa)[28], WF (&f)[3], f32x16& a0, f32x16& a1, f32x4& c0, f32x4& c1, const bf16x8& sel, const lptr (&w0)[4], const lptr (&w1)[4]) {
  if constexpr (ST < 28) {
    constexpr int N = ST + 2, off = p1_slot((P ? 32 : 21) + ((N < 28 ? N : 0) >> 2)) - (P ? L_FB : L_FA);
    SBAR(); MM(false, a0, xa[ST], cat(f[ST % 3].l0, f[ST % 3].h0));
    if constexpr (N < 28) { f[N % 3].l0 = trd(w0[N & 3] + off); f[N % 3].h0 = trd(w0[N & 3] + off + 512); SBAR(); }
    MM(false, a1, xa[ST], cat(f[ST % 3].l1, f[ST % 3].h1));
    if constexpr (N < 28) { f[N % 3].l1 = trd(w1[N & 3] + off); f[N % 3].h1 = trd(w1[N & 3] + off + 512); SBAR(); }
    if constexpr ((ST & 3) == 0) { c0 = MFMA16(sel, cat(f[ST % 3].l0, f[ST % 3].h0), c0); c1 = MFMA16(sel, cat(f[ST % 3].l1, f[ST % 3].h1), c1); PIN(c0); PIN(c1); }
    if constexpr (P == 0 && ST == 6) { WAIT_VM(0); post<L_WQL>(ev); }
    f_steps<P, ST + 1>(ev, xa, f, a0, a1, c0, c1, sel, w0, w1);
  }
}
DI void wf_read(WF& f, lptr p0, lptr p1) { f.l0 = trd(p0); f.h0 = trd(p0 + 512); f.l1 = trd(p1); f.h1 = trd(p1 + 512); }

constexpr int g2_issued(int st) { int n = 28; for (int s = 0; s < st; ++s) { if (s < 14) n += 2; if (s < 24) n += 2; } return n; }
constexpr int g2_pos_h(int s) { return s < 14 ? 2 * s + 1 : g2_issued(s - 14) + 1; }
constexpr int g2_pos_w(int s) { return s < 12 ? -1 : g2_issued(s - 12) + (s - 12 < 14 ? 2 : 0) + 1; }
constexpr int g2_wait(int st) { const int a = g2_pos_h(st), b = st + 2 < 28 ? g2_pos_w(st + 2) : -1; const int n = g2_issued(st) - 1 - (a > b ? a : b); return n > 63 ? 63 : n; }
DI void ldg_sc1(u32x4& d, const char* sbase, unsigned voff) { asm volatile("global_load_dwordx4 %0, %1, %2 sc1" : "=v"(d) : "v"(voff), "s"(sbase) : "memory"); }
struct G2 { const char *hb0, *hb1, *w2s, *wos; unsigned lv, w2vo, ringdst; Ev ev; int t; lptr br0, br1; };
template <int ST, int E> DI void g2_w(const G2& g) {
  const unsigned dst = g.ringdst + (ST % 12) * 2048 + E * 1024;
  if constexpr (ST < 28) glds16(g.w2s + (ST * 16 + E * 8) * 896, g.w2vo, dst);
  else if constexpr (ST < 35) glds16(g.wos + ((ST - 28) * 16 + E * 8) * 896, g.w2vo, dst);
  else glds16(g.wos + (g.t == 3 ? 104 : 112 + E * 8) * 896, g.w2vo, dst);
}
template <int A, int B> DI void g2_ws(const G2& g) { if constexpr (A < B) { g2_w<A, 0>(g); g2_w<A, 1>(g); g2_ws<A + 1, B>(g); } }
template <int ST> DI void g2_steps(const G2& g, u32x4 (&hf)[14][2], WF (&f)[3], f32x16& a00, f32x16& a01, f32x16& a10, f32x16& a11) {
  if constexpr (ST < 28) {
    if constexpr (ST == G2_SPLIT) { __builtin_amdgcn_s_setprio(0); pwait_long<A_DONE>(g.ev); __builtin_amdgcn_s_setprio(GPRIO); }
    asm volatile("s_waitcnt vmcnt(%2)" : "+v"(hf[ST % 14][0]), "+v"(hf[ST % 14][1]) : "i"(g2_wait(ST)) : "memory");
    constexpr int N = ST + 2, off = (N % 12) * 2048;
    const bf16x8 h0 = asfrag(hf[ST % 14][0]), h1 = asfrag(hf[ST % 14][1]), w0 = cat(f[ST % 3].l0, f[ST % 3].h0), w1 = cat(f[ST % 3].l1, f[ST % 3].h1);
    SBAR(); MM(false, a00, h0, w0);
    if constexpr (N < 28) { f[N % 3].l0 = trd(g.br0 + off); f[N % 3].h0 = trd(g.br0 + off + 512); SBAR(); }
    MM(false, a01, h1, w0);
    if constexpr (N < 28) { f[N % 3].l1 = trd(g.br1 + off); f[N % 3].h1 = trd(g.br1 + off + 512); SBAR(); }
    MM(false, a10, h0, w1);
    MM(false, a11, h1, w1);
    if constexpr (ST < 14) { ldg_sc1(hf[ST][0], g.hb0 + (ST + 14) * 1024, g.lv); ldg_sc1(hf[ST][1], g.hb1 + (ST + 14) * 1024, g.lv); }
    if constexpr (ST < 24) { g2_w<ST + 12, 0>(g); g2_w<ST + 12, 1>(g); }
    if constexpr (ST == 26) { WAIT_VM(0); post<L_WO>(g.ev); }
    g2_steps<ST + 1>(g, hf, f, a00, a01, a10, a11);
  }
}

__global__ void __launch_bounds__(512, 2) fused_block(const char* x, const char* wqkv, const char* wo, const char* w1, const char* w2, char* out, char* ws) {
  asm volatile("" : "+s"(x), "+s"(wqkv), "+s"(wo), "+s"(w1), "+s"(w2), "+s"(out), "+s"(ws));
  __shared__ __attribute__((aligned(16))) char smem[LDS_BYTES];
  u64 pc; asm volatile("s_getpc_b64 %0" : "=s"(pc));
  const lptr lds = (lptr)smem; const unsigned lds0 = (unsigned)(uintptr_t)lds;
  const int tid = threadIdx.x, lane = tid & 63, r32 = lane & 31, hh = lane >> 5;
  const int w = __builtin_amdgcn_readfirstlane(tid >> 6), t = w & 3, c = w >> 2;
  const int b = blockIdx.x, xg = b & 7, bi = b >> 3;
  const int j = (xg & 3) * 4 + (bi >> 3), rb = (xg >> 2) * 8 + (bi & 7);
  const int fme = (rb * 16 + j) * 128, rt = rb * 4 + t;
  const bool hasFb = !((NOFB >> j) & 1);
  const int fbl = 18 + __builtin_popcount(~NOFB & ((1u << j) - 1u));
  STAMP(0); CSTAMP(30); VSTAMP(29, (unsigned)__builtin_amdgcn_s_getreg(20 | (3 << 11)) & 0xfu);
  const int rot = t, r7 = ROT7 ? (rb + 2 * j) % 7 : 0;
  const int q4 = (lane & 15) >> 2, p4 = lane & 3, g16 = (lane >> 4) & 1;
  const int pi_std = 4 * g16 + 2 * (p4 & 1) + (p4 >> 1);
  const lptr wrd0 = lds + (8 * hh + q4) * 128 + pi_std * 8;
  const lptr priv = lds + L_PRIV + w * 256;
  Ev ev = {lds0 + L_CNT, 1u}; PIN(ev.base); PIN(ev.one);
  const __amdgpu_buffer_rsrc_t rsw = __builtin_amdgcn_make_buffer_rsrc(ws, 0, WS_BYTES, 0x00020000);
#define COMMON_SETUP() \
  lptr xrd[4], wr0[4], wr1[4]; \
  _Pragma("unroll") for (int s = 0; s < 4; ++s) { xrd[s] = lds + (t * 32 + r32) * 128 + (((2 * (s ^ rot) + hh) ^ ((r32 >> 1) & 7)) << 4); \
    wr0[s] = wrd0 + ((q4 >> 1) << 6) + (s ^ rot) * 2048; wr1[s] = wrd0 + ((1 ^ (q4 >> 1)) << 6) + (s ^ rot) * 2048; } \
  const int xwrap = (t >> 1) ? -NSLOT * SLOT : 0; \
  bf16x8 sel; { const short one = ((lane & 15) == ((lane >> 4) & 1)) ? (short)0x3f80 : (short)0; sel = (bf16x8){one, one, one, one, one, one, one, one}; } \
  bf16x8 xa[28]; float rs, nmb
#define GET_TAG() PIN(tagraw); const u64 tag = (((u64)(unsigned)__builtin_amdgcn_readfirstlane((unsigned)(tagraw >> 32)) << 32) | (unsigned)__builtin_amdgcn_readfirstlane((unsigned)tagraw)) + 1

  if (c == 0) {
    unsigned cpf0;
    asm volatile("global_load_dword %0, %1, %2" : "=v"(cpf0) : "v"(min((unsigned)(bi * 16 + (tid & 15)) * 128u, (unsigned)CODE_BYTES - 128u)), "s"(pc) : "memory");
    unsigned tch = 0;
    u64 tagraw = __hip_atomic_load((GAS u64*)(ws + WS_EPOCH), RLX_AGENT);
    if (tid < N_EV) *(LAS unsigned*)(lds + L_CNT + 4 * tid) = 0u;
    BAR();
    __builtin_amdgcn_s_setprio(P1PRIO);
    const bool isK = j < 7, isV = j >= 7 && j < 14;
    bf16x8 qf[4][2]; u64 tag;
    {
      COMMON_SETUP();
      unsigned ad[4]; bool strad[4] = {false, false, false, false};
      if (isV) { const int rho = r32 >> 2, col0 = (j - 7) * 64 + 4 * ((rho & 4) + 2 * (rho & 1) + ((rho >> 1) & 1)) + (r32 & 3);
#pragma unroll
        for (int cc = 0; cc < 2; ++cc) { const int col = col0 + cc * 32, hd = col / 28, d = col - hd * 28;
          ad[cc] = WS_KV + hd * 262144 + rt * 4096 + 2048 + (hh * 32 + d) * 16; ad[2 + cc] = d >= 24 ? ad[cc] + 64 : 0x80000000u; strad[cc] = d == 24; }
      } else if (isK) {
#pragma unroll
        for (int s = 0; s < 4; ++s) { const int P = j * 8 + 2 * s + hh, hp = P / 7, rr = P - hp * 7, hd = 2 * hp + (rr > 3), sg = rr > 3 ? rr - 4 : rr;
          ad[s] = WS_KV + hd * 262144 + rt * 4096 + sg * 512 + r32 * 16; strad[s] = rr == 3; }
      } else {
#pragma unroll
        for (int s = 0; s < 4; ++s) ad[s] = WS_H + rt * 114688 + (j - 14) * 4096 + s * 1024 + lane * 16;
      }
      PIN(ad[0]); PIN(ad[1]); PIN(ad[2]); PIN(ad[3]);
      asm volatile("s_waitcnt vmcnt(0)" : "+v"(cpf0), "+v"(tch), "+v"(tagraw) :: "memory");
      *(LAS u64*)(lds + L_TAG) = tagraw;
      tag = (((u64)(unsigned)__builtin_amdgcn_readfirstlane((unsigned)(tagraw >> 32)) << 32) | (unsigned)__builtin_amdgcn_readfirstlane((unsigned)tagraw)) + 1;
      P1R r = {}; bf16x8 mf;
      { P1A p; p.xwrap = xwrap; p.ev = ev; WF f[4];
#pragma unroll
        for (int s = 0; s < 4; ++s) { p.x[s] = xrd[s]; p.w0[s] = wr0[s]; p.w1[s] = wr1[s]; }
        const lptr csw = lds + L_CS0 + lane * 16 + t * 4;
#define GS(st) do { r.s2 = MFMA32(xa[st], xa[st], r.s2); PIN(r.s2); r.s1 = MFMA16(sel, xa[st], r.s1); PIN(r.s1); SBAR(); } while (0)
#define MA(VT, st) MM(VT, r.a0, xa[st], cat(f[st & 3].l0, f[st & 3].h0))
#define MB(VT, st) MM(VT, r.a1, xa[st], cat(f[st & 3].l1, f[st & 3].h1))
#define WRD(s) do { wf_read(f[s], p.w0[s] + SLOT, p.w1[s] + SLOT); SBAR(); } while (0)
#define P1_TAIL(VT) do { \
        p1_compute<VT, 0>(xa, f, r, sel, p STA); \
        __builtin_amdgcn_s_setprio(3); \
        _Pragma("unroll") for (int s = 0; s < 4; ++s) xa[24 + s] = ldsb128(p.x[s] + 6 * SLOT); \
        SBAR(); GS(22); WRD(0); GS(23); WRD(1); GS(24); \
        r.c0 = MFMA16(sel, cat(f[0].l0, f[0].h0), r.c0); r.c1 = MFMA16(sel, cat(f[0].l1, f[0].h1), r.c1); PIN(r.c0); PIN(r.c1); SBAR(); \
        GS(25); GS(26); \
        if (lane < 16) { *(LAS float*)csw = r.c0[0]; *(LAS float*)(csw + 256) = r.c0[1]; *(LAS float*)(csw + 512) = r.c1[0]; *(LAS float*)(csw + 768) = r.c1[1]; } \
        post<A_CSX>(ev); SBAR(); \
        GS(27); MA(VT, 22); MB(VT, 22); WRD(2); MA(VT, 23); \
        if (lane < 16) { *(LAS float*)(priv + lane * 4) = r.s1[0]; *(LAS float*)(priv + 64 + lane * 4) = r.s1[1]; } \
        const float sx_ = *(LAS float*)(priv + r32 * 4); SBAR(); \
        MB(VT, 23); WRD(3); post<A_CS0>(ev); MA(VT, 24); \
        const float sq_ = gram_diag(r.s2); SBAR(); \
        MB(VT, 24); \
        pwait<A_CSX>(ev); \
        const f32x4 p0_ = ldsf4(lds + L_CS0 + r32 * 16), p1_ = ldsf4(lds + L_CS0 + 512 + r32 * 16); SBAR(); \
        MA(VT, 25); \
        row_norm(sx_, sq_, rs, nmb); PIN(rs); PIN(nmb); SBAR(); \
        MB(VT, 25); \
        bf16x8 k0_ = cs_frag(sc1(sc1(p0_[0] + p0_[1]) + sc1(p0_[2] + p0_[3]))); PIN(k0_); \
        *(LAS float*)(lds + L_STAT + t * 256 + lane * 4) = hh ? nmb : rs; post<A_ST>(ev); SBAR();     \
        MA(VT, 26); \
        mf = mean_frag(nmb, hh); PIN(mf); SBAR(); \
        MA(VT, 27); \
        bf16x8 k1_ = cs_frag(sc1(sc1(p1_[0] + p1_[1]) + sc1(p1_[2] + p1_[3]))); PIN(k1_); SBAR(); \
        MM(VT, r.a0, mf, k0_); MB(VT, 26); MB(VT, 27); MM(VT, r.a1, mf, k1_); } while (0)
        if (isV) P1_TAIL(true); else P1_TAIL(false); }
      STAMP(1);
      u32x4 o[4];
      if (isV) {
#pragma unroll
        for (int i = 0; i < 4; ++i) { const f32x4 rr = ldsf4(lds + L_STAT + t * 256 + (8 * i + 4 * hh) * 4);
#pragma unroll
          for (int e = 0; e < 2; ++e) { const int n = 4 * i + 2 * e;
            o[i >> 1][2 * (i & 1) + e] = cvtpk(sc1(r.a0[n] * rr[2 * e]), sc1(r.a0[n + 1] * rr[2 * e + 1]));
            o[2 + (i >> 1)][2 * (i & 1) + e] = cvtpk(sc1(r.a1[n] * rr[2 * e]), sc1(r.a1[n + 1] * rr[2 * e + 1])); } }
        ST16(o[0], ad[0]); ST16(o[1], ad[0] + 1024); ST16(o[2], ad[1]); ST16(o[3], ad[1] + 1024);
#pragma unroll
        for (int cc = 0; cc < 2; ++cc) { const unsigned e = strad[cc] ? 0x3f803f80u : 0u; const u32x4 pad = {e, e, e, e}; ST16(pad, ad[2 + cc]); ST16(pad, ad[2 + cc] + 1024); }
      } else {
        if (isK) {
          epi_T<false>(r.a0, rs, o[0], o[1]);
#pragma unroll
          for (int s = 0; s < 2; ++s) { u32x4 v = o[s]; if (strad[s]) { v[2] = 0x3f803f80u; v[3] = 0x00003f80u; } ST16(v, ad[s]); }
          epi_T<false>(r.a1, rs, o[2], o[3]);
#pragma unroll
          for (int s = 2; s < 4; ++s) { u32x4 v = o[s]; if (strad[s]) { v[2] = 0x3f803f80u; v[3] = 0x00003f80u; } ST16(v, ad[s]); }
#pragma unroll
          for (int s = 0; s < 4; ++s) if (strad[s]) { const u32x4 e = {o[s][2], o[s][3], 0x3f803f80u, 0x00003f80u}; ST16(e, ad[s] + 262144); }
        } else {
          epi_T<true>(r.a0, rs, o[0], o[1]); epi_T<true>(r.a1, rs, o[2], o[3]);
#pragma unroll
          for (int s = 0; s < 4; ++s) ST16(o[s], ad[s]);
        }
      }
      STAMP(13);
      if (hasFb) {
        pwait<A_CS0>(ev); pwait<L_X6>(ev);
        __builtin_amdgcn_s_setprio(FDPRIO);
        const unsigned vo = (16 * t + (lane >> 3)) * 3584 + (((lane & 7) ^ (((lane >> 4) & 1) << 2)) << 4);
#pragma unroll
        for (int i = 0; i < 14; ++i) { const int c7 = (i >> 1) + r7 >= 7 ? (i >> 1) + r7 - 7 : (i >> 1) + r7;
          glds16(w1 + fbl * 128 + (c7 * 64 + (i & 1) * 8) * 3584, vo, lds0 + t * 2048 + p1_slot(32 + (i >> 1)) + (i & 1) * 1024); }
        asm volatile("s_waitcnt vmcnt(14)" ::: "memory");
        __builtin_amdgcn_s_setprio(3);
      } else WAIT_VM(0);
      if (cnt_add(lds, C_KV) == 3u && lane == 0) __hip_atomic_store((GAS u64*)(ws + WS_FKV + fme), tag, RLX_AGENT);
      if (cnt_add(lds, C_H) == 7u && lane == 0) __hip_atomic_store((GAS u64*)(ws + WS_FH + fme), tag, RLX_AGENT);
      STAMP(2);
      __builtin_amdgcn_s_setprio(QPRIO);
      f32x16 a0, a1; f32x4 c0;
      const int rho = 4 * g16 + p4, sg = 2 * (rho >> 2) + (rho & 1), half = (rho >> 1) & 1;
      const int g = (j & 1) ? (sg < 3 ? 2 * sg + 1 + half : (half ? 7 : 0)) : 2 * sg + half;
      lptr qb[4];
#pragma unroll
      for (int s = 0; s < 4; ++s) { qb[s] = lds + L_WQ + (8 * hh + q4) * 64 + g * 8 + (s ^ rot) * 1024; PIN(qb[s]); }
      a0 = (f32x16){}; a1 = (f32x16){}; c0 = (f32x4){};
      pwait<L_WQL>(ev);
      s16x4 ql[6], qh[6];
#pragma unroll
      for (int st = 0; st < 4; ++st) { ql[st] = trd(qb[st]); qh[st] = trd(qb[st] + 256); }
#pragma unroll
      for (int st = 0; st < 28; ++st) {
        if (st == QSPLIT) {
          WAIT_VM(0); post<A_FB>(ev); }
        SBAR();
        if (st & 1) { a1 = MFMA32(cat(ql[st % 6], qh[st % 6]), xa[st], a1); PIN(a1); } else { a0 = MFMA32(cat(ql[st % 6], qh[st % 6]), xa[st], a0); PIN(a0); }
        SBAR();
        if (st + 4 < 28) { const int o_ = ((st + 4) >> 2) * 4096; ql[(st + 4) % 6] = trd(qb[st & 3] + o_); qh[(st + 4) % 6] = trd(qb[st & 3] + o_ + 256); }
        SBAR();
        c0 = MFMA16(sel, cat(ql[st % 6], qh[st % 6]), c0); PIN(c0);
      }
      if (lane < 16) { *(LAS float*)(priv + lane * 4) = c0[0]; *(LAS float*)(priv + 64 + lane * 4) = c0[1]; }
      a1 = MFMA32(cs_frag(*(LAS float*)(priv + r32 * 4)), mf, a1);
#pragma unroll
      for (int i = 0; i < 16; ++i) a0[i] = sc1(a0[i] + a1[i]);
      epi_T<false>(a0, rs * QSCALE, o[0], o[1]);
      if (hh) { o[1][2] = 0u; o[1][3] = 0u; }
      *(LAS u32x4*)(lds + L_QX + t * 2048 + lane * 16) = o[0]; *(LAS u32x4*)(lds + L_QX + t * 2048 + 1024 + lane * 16) = o[1];
    }
    post<A_QW>(ev);
    const f32x16 zero = {};
    f32x16 o[4]; u32x4 kv[4][4], pA0 = {}, pA1 = {}, pB0 = {}, pB1 = {};
#pragma unroll
    for (int q = 0; q < 4; ++q) { o[q] = zero; PIN(o[q]); }
    kv[3][2] = (u32x4){}; kv[3][3] = (u32x4){}; PIN(kv[3][2]); PIN(kv[3][3]); PIN(pB0); PIN(pB1);
    STAMP(4);
    { const int c0 = (28 * j) >> 6, c1 = (28 * j + 27) >> 6, sel4 = lane >> 4;
      int jj = sel4 == 0 ? c0 : sel4 == 1 ? c1 : sel4 == 2 ? 7 + c0 : 7 + c1;
      if (j == 0 && (sel4 & 1)) jj = 14 + (sel4 >> 1);
      poll_flags<0>(ws + WS_FKV + ((lane & 15) * 16 + jj) * 128, tag); }
    STAMP(5);
    const unsigned kvb = WS_KV + j * 262144 + t * 65536 + lane * 16;
#pragma unroll
    for (int u = 0; u < 3; ++u)
#pragma unroll
      for (int e = 0; e < 4; ++e) kv[u][e] = LD16(kvb + u * 4096 + e * 1024);
    pwait<A_QW>(ev);
#pragma unroll
    for (int q = 0; q < 4; ++q) { const lptr a = lds + L_QX + ((q + t + 1) & 3) * 2048 + lane * 16; qf[q][0] = ldsb128(a); qf[q][1] = ldsb128(a + 1024); }
    float mref[4]; bool redo;
#pragma unroll
    for (int q = 0; q < 4; ++q) {
      f32x16 s = MFMA32(asfrag(kv[0][0]), qf[q][0], zero); s = MFMA32(asfrag(kv[0][1]), qf[q][1], s);
      mref[q] = set_ref(qf[q][1], tile_max(s) + REF_MARGIN, hh);
    }
    redo = !(__builtin_fmaxf(__builtin_fmaxf(__builtin_fabsf(mref[0]), __builtin_fabsf(mref[1])), __builtin_fmaxf(__builtin_fabsf(mref[2]), __builtin_fabsf(mref[3]))) < REF_LIMIT);
    __builtin_amdgcn_s_setprio(LPRIO);
    f32x16 sA, sB;
    sA = MFMA32(asfrag(kv[0][0]), qf[0][0], zero); sA = MFMA32(asfrag(kv[0][1]), qf[0][1], sA);
    PIN(sA); STAMP(6);
    auto area = [&](int dst, int e) -> lptr { const int n = dst * 3 + e; return lds + (n < 10 ? 8 * SLOT + n * 4096 : (n - 10) * 4096) + lane * 16; };
    auto put_o = [&](const f32x16& v, float m, int q) { const lptr a = area((q + t + 1) & 3, q);
      *(LAS f32x4*)a = (f32x4){v[0], v[1], v[2], v[3]}; *(LAS f32x4*)(a + 1024) = (f32x4){v[4], v[5], v[6], v[7]};
      *(LAS f32x4*)(a + 2048) = (f32x4){v[8], v[9], v[10], v[11]}; *(LAS f32x4*)(a + 3072) = (f32x4){v[12], hh ? m : v[13], v[14], v[15]}; };
#define PUT_O(q) put_o(o[q], mref[q], q)
#define EX4(S, i) do { S[i] = __builtin_amdgcn_exp2f(S[i]); S[i + 1] = __builtin_amdgcn_exp2f(S[i + 1]); S[i + 2] = __builtin_amdgcn_exp2f(S[i + 2]); S[i + 3] = __builtin_amdgcn_exp2f(S[i + 3]); } while (0)
#define UNIT(SC, SN, PC0, PC1, PP0, PP1, KN0, KN1, QN, QP, VV0, VV1, EXTRA) do { \
      SBAR(); if (QN >= 0) { SN = MFMA32(asfrag(KN0), qf[QN & 3][0], zero); PIN(SN); } SBAR(); EX4(SC, 0); PIN(SC); SBAR(); \
      if (QN >= 0) { SN = MFMA32(asfrag(KN1), qf[QN & 3][1], SN); PIN(SN); } SBAR(); EX4(SC, 4); PIN(SC); SBAR(); \
      o[QP] = MFMA32(asfrag(VV0), asfrag(PP0), o[QP]); PIN(o[QP]); SBAR(); EXTRA; EX4(SC, 8); PIN(SC); SBAR(); \
      o[QP] = MFMA32(asfrag(VV1), asfrag(PP1), o[QP]); PIN(o[QP]); SBAR(); EX4(SC, 12); SBAR(); \
      PC0 = (u32x4){cvtpk(SC[0], SC[1]), cvtpk(SC[2], SC[3]), cvtpk(SC[4], SC[5]), cvtpk(SC[6], SC[7])}; \
      PC1 = (u32x4){cvtpk(SC[8], SC[9]), cvtpk(SC[10], SC[11]), cvtpk(SC[12], SC[13]), cvtpk(SC[14], SC[15])}; PIN(PC0); PIN(PC1); SBAR(); } while (0)
    const float lthr = hh ? 16777216.f : __builtin_inff();
#define CHECK3 do { float m_; asm volatile("v_max3_f32 %0, %2, %3, %4\n\tv_cmp_lt_f32 %1, %5, %0" : "=&v"(m_), "=s"(g0) : "v"(o[1][12]), "v"(o[2][12]), "v"(o[3][12]), "v"(lthr)); } while (0)
#define CHECK1 asm volatile("v_cmp_lt_f32 %0, %2, %1" : "=s"(g1) : "v"(o[0][12]), "v"(lthr))
#define LDKV(UL, e) kv[UL][e] = __builtin_amdgcn_raw_buffer_load_b128(rsw, kvb + (e) * 1024, nt, LDAUX)
#define KEYTILE(U, UN, UL, LOAD, QLAST, X1, X2, X3) do { const int nt = (kt + (U) + 3) * 4096; \
      UNIT(sA, sB, pA0, pA1, pB0, pB1, kv[U][0], kv[U][1], 1, 3, kv[UL][2], kv[UL][3], if (LOAD) { LDKV(UL, 0); LDKV(UL, 1); }); \
      UNIT(sB, sA, pB0, pB1, pA0, pA1, kv[U][0], kv[U][1], 2, 0, kv[U][2], kv[U][3], if (LOAD) { LDKV(UL, 2); LDKV(UL, 3); } X1); \
      UNIT(sA, sB, pA0, pA1, pB0, pB1, kv[U][0], kv[U][1], 3, 1, kv[U][2], kv[U][3], X2); \
      UNIT(sB, sA, pB0, pB1, pA0, pA1, kv[UN][0], kv[UN][1], QLAST, 2, kv[U][2], kv[U][3], X3); \
    } while (0)
    for (int kt = 0; kt < 12; kt += 4) {
      LSTAMP(16 + (kt >> 1)); KEYTILE(0, 1, 3, true, 0, , , ); KEYTILE(1, 2, 0, true, 0, , , ); LSTAMP(17 + (kt >> 1)); KEYTILE(2, 3, 1, true, 0, , , ); u64 g0, g1;
      KEYTILE(3, 0, 2, true, 0, CHECK3, CHECK1, );
      if (!NOCHECK && __builtin_expect((g0 | g1) != 0ull, 0)) {
        NRESC;
        o[3] = MFMA32(asfrag(kv[3][2]), asfrag(pB0), o[3]); o[3] = MFMA32(asfrag(kv[3][3]), asfrag(pB1), o[3]); pB0 = (u32x4){}; pB1 = (u32x4){};
#pragma unroll
        for (int q = 0; q < 4; ++q) { const float l = upper_bcast(o[q][12]); redo |= !(l < 1.2676506002282294e30f);
          const float mn = set_ref(qf[q][1], mref[q] + (float)max(__builtin_amdgcn_frexp_expf(l) + (int)REF_MARGIN, 0), hh);
          const int e = (int)(mn - mref[q]); mref[q] = mn; redo |= !(__builtin_fabsf(mn) < REF_LIMIT);
#pragma unroll
          for (int r = 0; r < 16; ++r) o[q][r] = __builtin_ldexpf(o[q][r], -e);
          if (q == 0) {
#pragma unroll
            for (int r = 0; r < 16; ++r) sA[r] -= (float)e; } } }
    }
    { const int kt = 12;
      if (__builtin_expect(__any(redo), 0)) { mref[0] = mref[1] = mref[2] = mref[3] = __builtin_nanf(""); }
      pwait<L_CSBP>(ev);
      LSTAMP(22); KEYTILE(0, 1, 3, true, 0, , , ); KEYTILE(1, 2, 0, false, 0, , , ); LSTAMP(23); KEYTILE(2, 3, 1, false, 0, , , );
      post<A_DONE>(ev); __builtin_amdgcn_s_setprio(3);
      KEYTILE(3, 0, 2, false, -1, , PUT_O(0), PUT_O(1)); }
    o[3] = MFMA32(asfrag(kv[3][2]), asfrag(pB0), o[3]); o[3] = MFMA32(asfrag(kv[3][3]), asfrag(pB1), o[3]); PIN(o[3]); SBAR();
    PUT_O(2);
    STAMP(7);
    VSTAMP(28, nresc + (__any(redo) ? 1000 : 0));
    auto combine = [&]() -> bool {
      f32x4 pp[3][4]; f32x16 of;
#pragma unroll
      for (int e = 0; e < 3; ++e) { const lptr a = area(t, e); pp[e][3] = ldsf4(a + 3072); }
#pragma unroll
      for (int e = 0; e < 3; ++e) { const lptr a = area(t, e);
#pragma unroll
        for (int i = 0; i < 3; ++i) pp[e][i] = ldsf4(a + i * 1024); }
      const float l0 = o[3][12], l1 = pp[0][3][0], l2 = pp[1][3][0], l3 = pp[2][3][0], m1 = pp[0][3][1], m2 = pp[1][3][1], m3 = pp[2][3][1];
      const float mx = __builtin_fmaxf(__builtin_fmaxf(mref[3] + (float)__builtin_amdgcn_frexp_expf(l0), m1 + (float)__builtin_amdgcn_frexp_expf(l1)),
                                       __builtin_fmaxf(m2 + (float)__builtin_amdgcn_frexp_expf(l2), m3 + (float)__builtin_amdgcn_frexp_expf(l3)));
      float f0 = __builtin_amdgcn_exp2f(mref[3] - mx), f1 = __builtin_amdgcn_exp2f(m1 - mx), f2 = __builtin_amdgcn_exp2f(m2 - mx), f3 = __builtin_amdgcn_exp2f(m3 - mx);
      const float inv = __builtin_amdgcn_rcpf(__builtin_fmaf(l3, f3, __builtin_fmaf(l2, f2, __builtin_fmaf(l1, f1, l0 * f0))));
      f0 = upper_bcast(f0 * inv); f1 = upper_bcast(f1 * inv); f2 = upper_bcast(f2 * inv); f3 = upper_bcast(f3 * inv);
      const bool big = hh && !(__builtin_fmaxf(__builtin_fmaxf(l0, l1), __builtin_fmaxf(l2, l3)) < 1.2676506002282294e30f);
#pragma unroll
      for (int r = 0; r < 16; ++r) of[r] = sc1(__builtin_fmaf(pp[2][r >> 2][r & 3], f3, sc1(__builtin_fmaf(pp[1][r >> 2][r & 3], f2, sc1(__builtin_fmaf(pp[0][r >> 2][r & 3], f1, sc1(o[3][r] * f0)))))));
      u32x4 e0, e1;
#pragma unroll
      for (int p = 0; p < 4; ++p) { e0[p] = cvtpk(of[2 * p], of[2 * p + 1]); e1[p] = cvtpk(of[8 + 2 * p], of[9 + 2 * p]); }
      if (hh) { e1[2] = 0u; e1[3] = 0u; }
      const unsigned a = WS_O + rt * 32768 + j * 2048 + lane * 16;
      ST16(e0, a); ST16(e1, a + 1024);
      unsigned ex = 0;
#pragma unroll
      for (int p = 0; p < 4; ++p) ex |= ((e0[p] & 0x7f807f80u) + 0x00800080u) | ((e1[p] & 0x7f807f80u) + 0x00800080u);
      return __any((ex & 0x80008000u) != 0u || big);
    };
    post<A_CMB>(ev); STAMP(9); pwait<A_CMB>(ev);
    if (__builtin_expect(combine(), 0)) *(LAS unsigned*)(lds + L_CNT + 4 * C_BAD) = 1u;
    STAMP(10);
    WAIT_VM(0);
    const unsigned arr = cnt_add(lds, C_O);
    __builtin_amdgcn_s_setprio(0);
    if (arr != 3u) pwait_long<C_O>(ev);
    const bool bad = __builtin_amdgcn_readfirstlane(*(volatile LAS unsigned*)(lds + L_CNT + 4 * C_BAD)) != 0u;
    if (!bad && arr == 3u && lane == 0) __hip_atomic_store((GAS u64*)(ws + WS_FO + fme), tag, RLX_AGENT);
    if (__builtin_expect(bad, 0)) {
#pragma unroll
      for (int q = 0; q < 4; ++q) { set_ref(qf[q][1], 0.f, hh); o[q] = zero; mref[q] = -3.0e38f; }
#pragma nounroll
      for (int kt = 0; kt < 16; ++kt) {
        const u32x4 k0 = LD16(kvb + kt * 4096), k1 = LD16(kvb + kt * 4096 + 1024), v0 = LD16(kvb + kt * 4096 + 2048), v1 = LD16(kvb + kt * 4096 + 3072);
#pragma unroll
        for (int q = 0; q < 4; ++q) {
          f32x16 s = MFMA32(asfrag(k0), qf[q][0], zero); s = MFMA32(asfrag(k1), qf[q][1], s);
          const float mn = __builtin_fmaxf(mref[q], tile_max(s)), al = __builtin_amdgcn_exp2f(mref[q] - mn); mref[q] = mn;
#pragma unroll
          for (int r = 0; r < 16; ++r) { o[q][r] *= al; s[r] = __builtin_amdgcn_exp2f(s[r] - mn); }
          const u32x4 p0 = {cvtpk(s[0], s[1]), cvtpk(s[2], s[3]), cvtpk(s[4], s[5]), cvtpk(s[6], s[7])}, p1 = {cvtpk(s[8], s[9]), cvtpk(s[10], s[11]), cvtpk(s[12], s[13]), cvtpk(s[14], s[15])};
          o[q] = MFMA32(asfrag(v0), asfrag(p0), o[q]); o[q] = MFMA32(asfrag(v1), asfrag(p1), o[q]);
        }
      }
      PUT_O(0); PUT_O(1); PUT_O(2);
      post<A_CMB2>(ev); pwait<A_CMB2>(ev);
      combine();
      WAIT_VM(0);
      if (cnt_add(lds, C_O2) == 3u && lane == 0) __hip_atomic_store((GAS u64*)(ws + WS_FO + fme), tag, RLX_AGENT);
    }
    STAMP(8); CSTAMP(31); STAMP_OUT();
  } else {
    Ld k;
    k.xsrc = x + (rb * 128 + t * 32) * 896; k.xvo0 = (lane >> 3) * 896 + (((lane & 7) ^ (lane >> 4)) << 4); k.xvo1 = k.xvo0 ^ 64u;
    k.xdst = lds0 + t * 4096; k.xdst6 = lds0 + (t < 2 ? 18 * SLOT + t * 4096 : (t - 2) * 4096);
#pragma unroll
    for (int i = 0; i < 7; ++i) k.ck[i] = i + r7 >= 7 ? i + r7 - 7 : i + r7;
    { const int row = 16 * t + (lane >> 3); const unsigned piece = ((lane & 7) ^ (((lane >> 4) & 1) << 2)) << 4;
      k.w0rs = j < 14 ? 2688u : 3584u; k.w0src = j < 14 ? wqkv + 896 + j * 128 : w1 + (j - 14) * 128;
      k.fasrc = w1 + (2 + j) * 128; k.dst = lds0 + t * 2048;
      k.w0vo = row * k.w0rs + piece; k.wfvo = row * 3584 + piece;
      k.wqsrc = wqkv + j * 56; k.wqvo = (16 * t + (lane >> 2)) * 2688 + (lane & 3) * 16; k.qdst = lds0 + L_WQ + t * 1024; k.ev = ev; }
    {
      COMMON_SETUP();
      STAMP(14);
      p1_stream<0>(k, xa, xrd, xwrap STA);
      STAMP(15);
      __builtin_amdgcn_s_setprio(GPRIO);
#pragma unroll
      for (int s = 0; s < 4; ++s) { wr0[s] += L_FA; wr1[s] += L_FA; PIN(wr0[s]); PIN(wr1[s]); }
      WF f[3]; f32x16 a0 = {}, a1 = {}; f32x4 c0 = {}, c1 = {}; u32x4 o[2];
      pwait<L_FAL>(ev);
      wf_read(f[0], wr0[0], wr1[0]); wf_read(f[1], wr0[1], wr1[1]);
      f_steps<0, 0>(ev, xa, f, a0, a1, c0, c1, sel, wr0, wr1);
      cs_put(lds, L_CSA, 0, t, lane, c0); cs_put(lds, L_CSA, 1, t, lane, c1);
      pwait<A_ST>(ev);
      rs = *(LAS float*)(lds + L_STAT + t * 256 + r32 * 4); nmb = *(LAS float*)(lds + L_STAT + t * 256 + 128 + r32 * 4);
      const bf16x8 mf = mean_frag(nmb, hh);
      post<L_CSAP>(ev); pwait<L_CSAP>(ev);
      STAMP(2);
      __builtin_amdgcn_s_setprio(FBPRIO);
      { const unsigned ad = WS_H + rt * 114688 + (2 + j) * 4096 + lane * 16;
        a0 = MFMA32(cs_frag(cs_get(lds, L_CSA, 0, r32)), mf, a0); a1 = MFMA32(cs_frag(cs_get(lds, L_CSA, 1, r32)), mf, a1);
        epi_T<true>(a0, rs, o[0], o[1]); ST16(o[0], ad); ST16(o[1], ad + 1024);
        epi_T<true>(a1, rs, o[0], o[1]); ST16(o[0], ad + 2048); ST16(o[1], ad + 3072); }
      STAMP(3);
      if (hasFb) {
#pragma unroll
        for (int s = 0; s < 4; ++s) { wr0[s] += L_FB - L_FA; wr1[s] += L_FB - L_FA; PIN(wr0[s]); PIN(wr1[s]); }
        a0 = (f32x16){}; a1 = (f32x16){}; c0 = (f32x4){}; c1 = (f32x4){};
        pwait<A_FB>(ev);
        wf_read(f[0], wr0[0], wr1[0]); wf_read(f[1], wr0[1], wr1[1]);
        f_steps<1, 0>(ev, xa, f, a0, a1, c0, c1, sel, wr0, wr1);
        cs_put(lds, L_CSB, 0, t, lane, c0); cs_put(lds, L_CSB, 1, t, lane, c1);
        post<L_CSBP>(ev); pwait<L_CSBP>(ev);
        const unsigned ad = WS_H + rt * 114688 + fbl * 4096 + lane * 16;
        a0 = MFMA32(cs_frag(cs_get(lds, L_CSB, 0, r32)), mf, a0); a1 = MFMA32(cs_frag(cs_get(lds, L_CSB, 1, r32)), mf, a1);
        epi_T<true>(a0, rs, o[0], o[1]); ST16(o[0], ad); ST16(o[1], ad + 1024);
        epi_T<true>(a1, rs, o[0], o[1]); ST16(o[0], ad + 2048); ST16(o[1], ad + 3072);
      } else post<L_CSBP>(ev);
    }
    u64 tagraw = *(LAS u64*)(lds + L_TAG);
    GET_TAG();
    WAIT_VM(0);
    if (cnt_add(lds, C_H) == 7u && lane == 0) __hip_atomic_store((GAS u64*)(ws + WS_FH + fme), tag, RLX_AGENT);
    STAMP(4);
    const int cg = bi >> 2;
    if (cg < 7) {
      const int rt0 = 8 * xg + 2 * (bi & 3), rb3 = rt0 >> 2;
      const int ring = ring_of(t);
      const int ct = t >> 1, rtl = t & 1;
      G2 g; g.ev = ev; g.t = t; g.ringdst = lds0 + ring; g.lv = lane * 16;
      g.w2vo = (lane >> 3) * 896 + (((lane & 7) ^ (((lane >> 4) & 1) << 2)) << 4);
      g.w2s = w2 + t * 448 * 896 + cg * 128; g.wos = wo + t * 112 * 896 + cg * 128;
      g.hb0 = ws + WS_H + (rt0 + rtl) * 114688 + t * 28672; g.hb1 = ws + WS_H + (rt0 + (rtl ^ 1)) * 114688 + t * 28672;
      g.br0 = wrd0 + ring + ((ct ^ (q4 >> 1)) << 6); g.br1 = wrd0 + ring + ((ct ^ 1 ^ (q4 >> 1)) << 6);
      unsigned oo = ((rt0 + rtl) * 32 + r32) * 896 + cg * 128 + ct * 64 + hh * 16;
      const char* hfl = ws + WS_FH + (rb3 * 16 + (lane & 15)) * 128;
      f32x16 a00, a01 = {}, a10 = {}, a11 = {};
      PIN(g.br0); PIN(g.br1); PIN(g.w2vo); PIN(g.lv); PIN(oo); PIN(hfl); PIN(a01); PIN(a10); PIN(a11);
      __builtin_amdgcn_s_setprio(0);
      g2_ws<0, 12>(g);
      u32x4 xr0 = *(const GAS u32x4*)(x + oo), xr1 = *(const GAS u32x4*)(x + oo + 32);
      poll_flags<HSLEEP>(hfl, tag);
      PIN(xr0); PIN(xr1);
      STAMP(5);
      u32x4 hf[14][2];
#pragma unroll
      for (int s = 0; s < 14; ++s) { ldg_sc1(hf[s][0], g.hb0 + s * 1024, g.lv); ldg_sc1(hf[s][1], g.hb1 + s * 1024, g.lv); }
      __builtin_amdgcn_s_setprio(3);
#pragma unroll
      for (int p = 0; p < 4; ++p) { a00[2 * p] = bflo(xr0[p]); a00[2 * p + 1] = bfhi(xr0[p]); a00[8 + 2 * p] = bflo(xr1[p]); a00[9 + 2 * p] = bfhi(xr1[p]); }
      PIN(a00);
      __builtin_amdgcn_s_setprio(G2PRIO);
      { WF f[3]; wf_read(f[0], g.br0, g.br1); wf_read(f[1], g.br0 + 2048, g.br1 + 2048);
        g2_steps<0>(g, hf, f, a00, a01, a10, a11); }
      STAMP(6);
      pwait<L_WO>(ev);
      bf16x8 wf[16][2];
#pragma unroll
      for (int g2 = 0; g2 < 2; ++g2) { const lptr o0 = lds + ring_of(2 * ct + g2) + SLOT + (4 * hh + q4) * 128 + pi_std * 8;
#pragma unroll
        for (int e = 0; e < 8; ++e)
#pragma unroll
          for (int cc = 0; cc < 2; ++cc) wf[8 * g2 + e][cc] = trfrag(o0 + ((cc ^ ct ^ (q4 >> 1)) << 6) + ((e >> 1) * 28 + (e & 1) * 16) * 128, 1024); }
      auto put = [&](const f32x16& a, int e) { const lptr m = lds + ring + e * 4096 + lane * 16;
#pragma unroll
        for (int i = 0; i < 4; ++i) *(LAS f32x4*)(m + i * 1024) = (f32x4){a[4 * i], a[4 * i + 1], a[4 * i + 2], a[4 * i + 3]}; };
      auto add = [&](f32x16& a, int src, int e) { const lptr m = lds + ring_of(src) + e * 4096 + lane * 16;
#pragma unroll
        for (int i = 0; i < 4; ++i) { const f32x4 v = ldsf4(m + i * 1024);
#pragma unroll
          for (int n = 0; n < 4; ++n) a[4 * i + n] = sc1(a[4 * i + n] + v[n]); } };
      put(a01, 0); put(a11, 1);
      post<L_RED>(ev); pwait<L_RED>(ev);
      add(a00, t ^ 1, 0); add(a10, t ^ 1, 1);
      PIN(a00); PIN(a10);
      STAMP(11);
      { const char* fl = ws + WS_FO + (rb3 * 16 + ct * 8 + (lane & 7)) * 128; const unsigned ob = WS_O + (rt0 + rtl) * 32768 + ct * 16384 + lane * 16;
        unsigned ready = 0; u32x4 of[8][2];
#pragma unroll
        for (int h = 0; h < 8 + OLAG; ++h) {
          if (h < 8) {
            if (__builtin_expect(!((ready >> h) & 1u), 0)) ready = poll_bit(fl, tag, h);
            if (h == 7) STAMP(7);
            of[h][0] = LD16(ob + h * 2048); of[h][1] = LD16(ob + h * 2048 + 1024); }
          if (h >= OLAG) {
#pragma unroll
            for (int s = 0; s < 2; ++s) { const bf16x8 o_ = asfrag(of[h - OLAG][s]); a10 = MFMA32(wf[2 * (h - OLAG) + s][1], o_, a10); a00 = MFMA32(wf[2 * (h - OLAG) + s][0], o_, a00); }
            PIN(a10); PIN(a00); }
          SBAR(); } }
      STAMP(9);
      put(a10, 2);
      post<L_FIN>(ev);
      STAMP(10); pwait<L_FIN>(ev);
      add(a00, t ^ 2, 2);
      u32x4 r0, r1;
#pragma unroll
      for (int p = 0; p < 4; ++p) { r0[p] = cvtpk(a00[2 * p], a00[2 * p + 1]); r1[p] = cvtpk(a00[8 + 2 * p], a00[9 + 2 * p]); }
      *(GAS u32x4*)(out + oo) = r0; *(GAS u32x4*)(out + oo + 32) = r1;
      if (b == 0 && tid == 256) __hip_atomic_store((GAS u64*)(ws + WS_EPOCH), tag, RLX_AGENT);
      STAMP(8);
    }
    STAMP_OUT();
  }
}

extern "C" void kernel_launch(void* const* d_in, const int* in_sizes, int n_in, void* d_out, int out_size, void* d_ws, size_t ws_size, hipStream_t stream) {
  fused_block<<<256, 512, 0, stream>>>((const char*)d_in[0], (const char*)d_in[1], (const char*)d_in[2], (const char*)d_in[4], (const char*)d_in[5], (char*)d_out, (char*)d_ws + WS_BASE);
}
```

```cpp
#include <hip/hip_runtime.h>
#include <stdint.h>

typedef short bf16x8 __attribute__((ext_vector_type(8)));
typedef short s16x4 __attribute__((ext_vector_type(4)));
typedef float f32x16 __attribute__((ext_vector_type(16)));
typedef float f32x4 __attribute__((ext_vector_type(4)));
typedef float f32x2 __attribute__((ext_vector_type(2)));
typedef __bf16 bf16x2v __attribute__((ext_vector_type(2)));
typedef unsigned u32x4 __attribute__((ext_vector_type(4)));
typedef unsigned long long u64;
#define LAS __attribute__((address_space(3)))
#define GAS __attribute__((address_space(1)))
#define DI __device__ __forceinline__
#define MFMA32(a, b, c) __builtin_amdgcn_mfma_f32_32x32x16_bf16(a, b, c, 0, 0, 0)
#define MFMA16(a, b, c) __builtin_amdgcn_mfma_f32_16x16x32_bf16(a, b, c, 0, 0, 0)
#define SBAR() __builtin_amdgcn_sched_barrier(0)
#define PIN(x) asm volatile("" : "+v"(x))
#define RLX_AGENT __ATOMIC_RELAXED, __HIP_MEMORY_SCOPE_AGENT
#define WAIT_BAR(N) asm volatile("s_waitcnt vmcnt(" #N ") lgkmcnt(0)\n\ts_barrier" ::: "memory")
#define WAIT_VM(N) asm volatile("s_waitcnt vmcnt(" #N ")" ::: "memory")
#define WAIT_LGKM0() asm volatile("s_waitcnt lgkmcnt(0)" ::: "memory")

constexpr unsigned WS_EPOCH = 0;
constexpr unsigned WS_FKV = 4096, WS_FH = WS_FKV + 32768, WS_FO = WS_FH + 32768;
constexpr unsigned WS_KV = 1u << 20;
constexpr unsigned WS_H = 5u << 20;
constexpr unsigned WS_O = 13u << 20;
constexpr unsigned WS_BYTES = 15u << 20;
constexpr int SLOT = 8192, NSLOT = 19;
constexpr int L_MISC = NSLOT * SLOT;
constexpr int L_STAT = L_MISC;
constexpr int L_CS0 = L_MISC + 2048, L_CSA = L_CS0 + 1024, L_CSB = L_CSA + 1024;
constexpr int L_PRIV = L_MISC + 5120;
constexpr int L_CNT = L_MISC + 7168;
constexpr int L_TAG = L_MISC + 7424;
constexpr int LDS_BYTES = 163840;
constexpr int L_FA = 2 * SLOT, L_WQ = 9 * SLOT, L_FB = 13 * SLOT, L_QX = SLOT;
constexpr int ring_of(int t) { return (t < 2 ? 2 + 3 * t : 7 + 3 * t) * SLOT; }
constexpr int p1_slot(int item) { return (item % NSLOT) * SLOT; }
enum { C_KV, C_H, C_O, C_O2, C_BAD,
       A_RD0, A_CS0 = A_RD0 + 6, A_CSX, A_ST, A_FB, A_QW, A_DONE, A_CMB, A_CMB2,
       L_LD0, L_X6 = L_LD0 + 7, L_FAL, L_WQL, L_CSAP, L_CSBP, L_WO, L_RED, L_FIN, N_EV };

#define WS_BASE 0
#define TOUCH 0
#define ROT7 0
#define MAPA 8
#define G2_SPLIT 12
#define CODE_BYTES 55296
constexpr float QSCALE = 0.18898223650461362f * 1.4426950408889634f;
constexpr float REF_MARGIN = 80.f;
constexpr float REF_LIMIT = 4096.f;
constexpr unsigned SPIN_CAP = 1u << 21;

DI unsigned cvtpk(float lo, float hi) { f32x2 v = {lo, hi}; return __builtin_bit_cast(unsigned, __builtin_convertvector(v, bf16x2v)); }
DI float sc1(float x) { asm("" : "+v"(x)); return x; }
DI float bf16r(float x) { return __uint_as_float(cvtpk(x, 0.f) << 16); }
DI float bflo(unsigned v) { return __uint_as_float(v << 16); }
DI float bfhi(unsigned v) { return __uint_as_float(v & 0xffff0000u); }
DI float swap_add(float v) { auto r = __builtin_amdgcn_permlane32_swap(__float_as_uint(v), __float_as_uint(v), false, false); return __uint_as_float(r[0]) + __uint_as_float(r[1]); }
DI float swap_max(float v) { auto r = __builtin_amdgcn_permlane32_swap(__float_as_uint(v), __float_as_uint(v), false, false); return __builtin_fmaxf(__uint_as_float(r[0]), __uint_as_float(r[1])); }
DI float upper_bcast(float v) { auto r = __builtin_amdgcn_permlane32_swap(__float_as_uint(v), __float_as_uint(v), false, false); return __uint_as_float(r[1]); }
DI void glds16(const void* sbase, unsigned voff, unsigned ldsdst) {
  unsigned keep;
  asm volatile("s_mov_b32 %0, m0\n\ts_mov_b32 m0, %3\n\ts_nop 0\n\tglobal_load_lds_dwordx4 %1, %2\n\ts_mov_b32 m0, %0"
               : "=&s"(keep) : "v"(voff), "s"(sbase), "s"(ldsdst) : "memory");
}
typedef LAS char* lptr;
typedef short v4i16_t __attribute__((ext_vector_type(4)));
DI s16x4 trd(lptr p) { return __builtin_bit_cast(s16x4, __builtin_amdgcn_ds_read_tr16_b64_v4i16((LAS v4i16_t*)p)); }
DI bf16x8 trfrag(lptr p, int second) { s16x4 lo = trd(p), hi = trd(p + second); return __builtin_shufflevector(lo, hi, 0, 1, 2, 3, 4, 5, 6, 7); }
DI bf16x8 ldsb128(lptr p) { return *(LAS bf16x8*)p; }
DI f32x4 ldsf4(lptr p) { return *(LAS f32x4*)p; }
DI bf16x8 asfrag(u32x4 v) { return __builtin_bit_cast(bf16x8, v); }
struct Ev { unsigned base, one; };
template <int E> DI void post(const Ev& e) {
  asm volatile("s_mov_b64 exec, 1\n\tds_add_u32 %0, %1 offset:%2\n\ts_mov_b64 exec, -1" :: "v"(e.base), "v"(e.one), "i"(4 * E) : "memory"); }
template <int E> DI void pwait(const Ev& e) {
  unsigned a;
  asm volatile("1:\n\tds_read_b32 %0, %1 offset:%2\n\ts_waitcnt lgkmcnt(0)\n\tv_cmp_gt_u32 vcc, 4, %0\n\ts_cbranch_vccnz 1b"
               : "=&v"(a) : "v"(e.base), "i"(4 * E) : "vcc", "memory"); }
template <int E> DI void pwait_long(const Ev& e) {
  unsigned a;
  asm volatile("1:\n\ts_sleep 8\n\tds_read_b32 %0, %1 offset:%2\n\ts_waitcnt lgkmcnt(0)\n\tv_cmp_gt_u32 vcc, 4, %0\n\ts_cbranch_vccnz 1b"
               : "=&v"(a) : "v"(e.base), "i"(4 * E) : "vcc", "memory"); }
DI unsigned cnt_add(lptr lds, int c) { unsigned r = 0; if ((threadIdx.x & 63) == 0) r = __hip_atomic_fetch_add((LAS unsigned*)(lds + L_CNT + 4 * c), 1u, __ATOMIC_RELAXED, __HIP_MEMORY_SCOPE_WORKGROUP);
  return __builtin_amdgcn_readfirstlane(r); }
template <int SLEEP> DI void poll_flags(const char* flag, u64 tag) {
  u64 v; unsigned n = SPIN_CAP;
  asm volatile("1:\n\tglobal_load_dwordx2 %0, %2, off sc1\n\ts_sub_u32 %1, %1, 1\n\ts_cbranch_scc1 2f\n\ts_sleep %4\n\ts_waitcnt vmcnt(0)\n\t"
               "v_cmp_ne_u64 vcc, %3, %0\n\ts_cbranch_vccnz 1b\n2:\n\ts_waitcnt vmcnt(0)"
               : "=&v"(v), "+s"(n) : "v"(flag), "s"(tag), "i"(SLEEP) : "vcc", "scc", "memory");
}
DI unsigned poll_bit(const char* flag, u64 tag, int h) {
  u64 v, m; unsigned n = SPIN_CAP;
  asm volatile("1:\n\tglobal_load_dwordx2 %0, %3, off sc1\n\ts_sub_u32 %2, %2, 1\n\ts_cbranch_scc1 2f\n\ts_waitcnt vmcnt(0)\n\t"
               "v_cmp_eq_u64 %1, %4, %0\n\ts_nop 3\n\ts_bitcmp0_b64 %1, %5\n\ts_cbranch_scc1 1b\n2:\n\ts_waitcnt vmcnt(0)"
               : "=&v"(v), "=&s"(m), "+s"(n) : "v"(flag), "s"(tag), "i"(h) : "scc", "memory");
  return (unsigned)m;
}
#define STAMP(i) do {} while (0)
#define CSTAMP(i) do {} while (0)
#define VSTAMP(i, v) do {} while (0)
#define STAMP_OUT() do {} while (0)
#define NRESC
#define STV
#define STA
#define FSTAMP(i) do {} while (0)
#define LSTAMP(i) do {} while (0)
#define BAR() asm volatile("s_waitcnt lgkmcnt(0)\n\ts_barrier" ::: "memory")

#define QPRIO 3
#define LPRIO 2
#define GPRIO 1
#define G2PRIO 0
#define NOCHECK 0
#define QSPLIT 8
#define OLAG 4
#define FBPRIO 3
#define NOFB 0x003fu
static_assert(__builtin_popcount(NOFB) == 6, "w1 has 28 lines");
#define HSLEEP 6
#define P1PRIO 2
#define FDPRIO 0
#define THROT 2

#define ROTG 0
#define ROTK 0
#define LOOK 4
DI float pick(u64 m, float hi, float lo) { float r; asm("v_cndmask_b32 %0, %1, %2, %3" : "=v"(r) : "v"(lo), "v"(hi), "s"(m)); return r; }
DI float gram_diag(const f32x16& g) {
  const u64 m1 = 0xaaaaaaaaaaaaaaaaull, m2 = 0xccccccccccccccccull, m4 = 0xf0f0f0f0f0f0f0f0ull, m8 = 0xff00ff00ff00ff00ull, m16 = 0xffff0000ffff0000ull;
  const float b0 = pick(m2, pick(m1, g[3], g[2]), pick(m1, g[1], g[0])), b1 = pick(m2, pick(m1, g[7], g[6]), pick(m1, g[5], g[4]));
  const float b2 = pick(m2, pick(m1, g[11], g[10]), pick(m1, g[9], g[8])), b3 = pick(m2, pick(m1, g[15], g[14]), pick(m1, g[13], g[12]));
  float d = pick(m16, pick(m8, b3, b2), pick(m8, b1, b0));
  asm volatile("s_nop 1" : "+v"(d));
  auto r = __builtin_amdgcn_permlane32_swap(__float_as_uint(d), __float_as_uint(d), false, false);
  return pick(m4, __uint_as_float(r[1]), __uint_as_float(r[0]));
}
template <int C> DI void x_frags(bf16x8 (&xa)[28], const lptr (&xrd)[4], int xwrap) {
#pragma unroll
  for (int s = 0; s < 4; ++s) xa[4 * C + s] = ldsb128(xrd[s] + p1_slot(3 * C) + (C == 6 ? xwrap : 0));
}
DI void row_norm(float s1, float s2, float& rs, float& nmb) {
  const float mean = sc1(s1 * (1.f / 448.f)), var = __builtin_fmaxf(sc1(s2 * (1.f / 448.f)) - mean * mean, 0.f);
  const float sd = bf16r(__builtin_amdgcn_sqrtf(bf16r(bf16r(var) + 1e-5f))); rs = __builtin_amdgcn_rcpf(sd); nmb = -bf16r(mean);
}
struct P1A { lptr x[4], w0[4], w1[4]; int xwrap; Ev ev; };
struct WF { s16x4 l0, h0, l1, h1; };
DI bf16x8 cat(const s16x4& lo, const s16x4& hi) { return __builtin_shufflevector(lo, hi, 0, 1, 2, 3, 4, 5, 6, 7); }
struct P1R { f32x16 a0, a1; f32x4 c0, c1, s1; f32x16 s2; };
#define MM(VT, acc, xf, wf) do { if constexpr (VT) acc = MFMA32(xf, wf, acc); else acc = MFMA32(wf, xf, acc); PIN(acc); SBAR(); } while (0)
template <bool VT, int ST, int C, int S> DI void p1_group(bf16x8 (&xa)[28], WF (&f)[4], P1R& r, const bf16x8& sel, const P1A& p) {
  constexpr int xo = p1_slot(3 * C) - (C >> 1) * 6 * SLOT, wo = p1_slot(3 * C + 2) - (C >> 1) * 6 * SLOT;
  if constexpr (ST >= 0) MM(VT, r.a0, xa[ST], cat(f[ST & 3].l0, f[ST & 3].h0));
  xa[4 * C + S] = ldsb128(p.x[S] + xo); f[S].l0 = trd(p.w0[S] + wo); SBAR();
  if constexpr (ST >= 0) MM(VT, r.a1, xa[ST], cat(f[ST & 3].l1, f[ST & 3].h1));
  f[S].h0 = trd(p.w0[S] + wo + 512); f[S].l1 = trd(p.w1[S] + wo); SBAR();
  if constexpr (ST >= 0) { r.s2 = MFMA32(xa[ST], xa[ST], r.s2); PIN(r.s2); SBAR(); }
  f[S].h1 = trd(p.w1[S] + wo + 512); SBAR();
  if constexpr (ST >= 0) { r.s1 = MFMA16(sel, xa[ST], r.s1); PIN(r.s1);
    if constexpr ((ST & 3) == 0) { r.c0 = MFMA16(sel, cat(f[0].l0, f[0].h0), r.c0); r.c1 = MFMA16(sel, cat(f[0].l1, f[0].h1), r.c1); PIN(r.c0); PIN(r.c1); }
    SBAR(); }
}
template <bool VT, int C> DI void p1_compute(bf16x8 (&xa)[28], WF (&f)[4], P1R& r, const bf16x8& sel, P1A& p STV) {
  if constexpr (C == 0) pwait<L_LD0>(p.ev);
  if (C == 0) STAMP(12);
  FSTAMP(15 + C);
  if constexpr (C < 6) {
    p1_group<VT, 4 * C - 2, C, 0>(xa, f, r, sel, p); p1_group<VT, 4 * C - 1, C, 1>(xa, f, r, sel, p); p1_group<VT, 4 * C, C, 2>(xa, f, r, sel, p);
    const unsigned nxt = *(volatile LAS unsigned*)(uintptr_t)(p.ev.base + 4 * (L_LD0 + C + 1));
    p1_group<VT, 4 * C + 1, C, 3>(xa, f, r, sel, p);
    post<A_RD0 + C>(p.ev);
    if constexpr (C == 1 || C == 3) {
#pragma unroll
      for (int s = 0; s < 4; ++s) { p.x[s] += 6 * SLOT; p.w0[s] += 6 * SLOT; p.w1[s] += 6 * SLOT; PIN(p.x[s]); PIN(p.w0[s]); PIN(p.w1[s]); }
    }
    if constexpr (C == 5) {
#pragma unroll
      for (int s = 0; s < 4; ++s) { p.x[s] += p.xwrap; p.w0[s] -= 12 * SLOT; p.w1[s] -= 12 * SLOT; PIN(p.x[s]); PIN(p.w0[s]); PIN(p.w1[s]); }
    }
    SBAR();
    if (__builtin_expect(__builtin_amdgcn_readfirstlane(nxt) < 4u, 0)) pwait<L_LD0 + C + 1>(p.ev);
    p1_compute<VT, C + 1>(xa, f, r, sel, p STA);
  }
}
struct Ld { const char *xsrc, *w0src, *wqsrc, *fasrc; unsigned xvo0, xvo1, w0vo, wfvo, wqvo, w0rs, dst, xdst, xdst6, qdst; Ev ev; int ck[7]; };
template <int N, int E> DI void ld_piece(const Ld& k) {
  const unsigned dst = k.dst + p1_slot(N) + E * 1024;
  if constexpr (N < 21) {
    constexpr int c = N / 3, q = N % 3;
    if constexpr (q < 2) glds16(k.xsrc + (q * 16 + E * 8) * 896 + k.ck[c] * 128, E ? k.xvo1 : k.xvo0, (c < 6 ? k.xdst + p1_slot(3 * c) : k.xdst6) + (2 * q + E) * 1024);
    else glds16(k.w0src + (k.ck[c] * 64 + E * 8) * k.w0rs, k.w0vo, dst);
  } else if constexpr (N < 28) glds16(k.fasrc + (k.ck[N - 21] * 64 + E * 8) * 3584, k.wfvo, dst);
  else { constexpr int c = 2 * (N - 28) + E;
    glds16(k.wqsrc + k.ck[c < 7 ? c : 0] * (64 * 2688), k.wqvo, k.qdst + c * 4096); }
}
template <int N> DI void p1_stream(const Ld& k, bf16x8 (&xa)[28], const lptr (&xrd)[4], int xwrap STV) {
  if constexpr (N < 32) {
    if constexpr (N == 18 || (N > 19 && (N - 19) % 3 == 0)) { pwait<A_RD0 + (N - 18) / 3>(k.ev); pwait<L_LD0 + (N - 18) / 3 + 1>(k.ev); }
    ld_piece<N, 0>(k); ld_piece<N, 1>(k);
    if constexpr (N == 2) asm volatile("s_barrier" ::: "memory");
    constexpr int M = N - LOOK;
    if constexpr (M >= 2 && M <= 20 && M % 3 == 2) {
      constexpr int C = M / 3;
      asm volatile("s_waitcnt vmcnt(%0)" :: "i"(2 * LOOK) : "memory");
      post<L_LD0 + C>(k.ev);
      if (C == 0) STAMP(12);
      x_frags<C>(xa, xrd, xwrap); SBAR();
      if (C == 6) STAMP(1);
    } else if constexpr (M == 21) post<L_X6>(k.ev);
    else if constexpr (M == 27) { asm volatile("s_waitcnt vmcnt(%0)" :: "i"(2 * LOOK) : "memory"); post<L_FAL>(k.ev); }
    p1_stream<N + 1>(k, xa, xrd, xwrap STA);
  }
}
DI void cs_put(lptr lds, int area, int c, int t, int lane, const f32x4& cs) {
  if (lane < 16) { *(LAS float*)(lds + area + c * 512 + lane * 16 + t * 4) = cs[0]; *(LAS float*)(lds + area + c * 512 + (lane + 16) * 16 + t * 4) = cs[1]; }
}
DI float cs_get(lptr lds, int area, int c, int r32) { const f32x4 p = ldsf4(lds + area + c * 512 + r32 * 16); return sc1(sc1(p[0] + p[1]) + sc1(p[2] + p[3])); }
DI bf16x8 cs_frag(float c) {
  const unsigned hi = __float_as_uint(c) & 0xffff0000u; const float r = c - __uint_as_float(hi);
  const unsigned mid = __float_as_uint(r) & 0xffff0000u; const float lo = r - __uint_as_float(mid);
  return asfrag((u32x4){(hi >> 16) | mid, __float_as_uint(lo) >> 16, 0u, 0u});
}
DI bf16x8 mean_frag(float nmb, int hh) { const unsigned m = hh ? 0u : __float_as_uint(nmb) >> 16; return asfrag((u32x4){m | (m << 16), m, 0u, 0u}); }
typedef short s16x2 __attribute__((ext_vector_type(2)));
template <bool RELU> DI void epi_T(const f32x16& acc, float sc, u32x4& o0, u32x4& o1) {
#pragma unroll
  for (int p = 0; p < 8; ++p) {
    unsigned w = cvtpk(sc1(acc[2 * p] * sc), sc1(acc[2 * p + 1] * sc));
    if (RELU) w = __builtin_bit_cast(unsigned, __builtin_elementwise_max(__builtin_bit_cast(s16x2, w), (s16x2){0, 0}));
    if (p < 4) o0[p] = w; else o1[p - 4] = w;
  }
}
#define ST16(v, off) __builtin_amdgcn_raw_buffer_store_b128(v, rsw, off, 0, 16)
#define ABL_NOKV 0
#define LDAUX 16
#define LD16(off) __builtin_amdgcn_raw_buffer_load_b128(rsw, off, 0, LDAUX)

DI float set_ref(bf16x8& q1, float m, int hh) {
  const unsigned w = cvtpk(-__builtin_rintf(m), 0.f);
  u32x4 v = __builtin_bit_cast(u32x4, q1); v[2] = hh ? w : v[2]; q1 = __builtin_bit_cast(bf16x8, v);
  return -__uint_as_float(w << 16);
}
DI float tile_max(const f32x16& s) {
  float a = __builtin_fmaxf(__builtin_fmaxf(s[0], s[1]), s[2]), b = __builtin_fmaxf(__builtin_fmaxf(s[3], s[4]), s[5]);
#pragma unroll
  for (int r = 6; r < 14; r += 4) { a = __builtin_fmaxf(__builtin_fmaxf(a, s[r]), s[r + 1]); b = __builtin_fmaxf(__builtin_fmaxf(b, s[r + 2]), s[r + 3]); }
  a = __builtin_fmaxf(__builtin_fmaxf(a, s[14]), s[15]);
  return swap_max(__builtin_fmaxf(a, b));
}

template <int P, int ST> DI void f_steps(const Ev& ev, const bf16x8 (&xa)[28], WF (&f)[3], f32x16& a0, f32x16& a1, f32x4& c0, f32x4& c1, const bf16x8& sel, const lptr (&w0)[4], const lptr (&w1)[4]) {
  if constexpr (ST < 28) {
    constexpr int N = ST + 2, off = p1_slot((P ? 32 : 21) + ((N < 28 ? N : 0) >> 2)) - (P ? L_FB : L_FA);
    SBAR(); MM(false, a0, xa[ST], cat(f[ST % 3].l0, f[ST % 3].h0));
    if constexpr (N < 28) { f[N % 3].l0 = trd(w0[N & 3] + off); f[N % 3].h0 = trd(w0[N & 3] + off + 512); SBAR(); }
    MM(false, a1, xa[ST], cat(f[ST % 3].l1, f[ST % 3].h1));
    if constexpr (N < 28) { f[N % 3].l1 = trd(w1[N & 3] + off); f[N % 3].h1 = trd(w1[N & 3] + off + 512); SBAR(); }
    if constexpr ((ST & 3) == 0) { c0 = MFMA16(sel, cat(f[ST % 3].l0, f[ST % 3].h0), c0); c1 = MFMA16(sel, cat(f[ST % 3].l1, f[ST % 3].h1), c1); PIN(c0); PIN(c1); }
    if constexpr (P == 0 && ST == 6) { WAIT_VM(0); post<L_WQL>(ev); }
    f_steps<P, ST + 1>(ev, xa, f, a0, a1, c0, c1, sel, w0, w1);
  }
}
DI void wf_read(WF& f, lptr p0, lptr p1) { f.l0 = trd(p0); f.h0 = trd(p0 + 512); f.l1 = trd(p1); f.h1 = trd(p1 + 512); }

constexpr int g2_issued(int st) { int n = 28; for (int s = 0; s < st; ++s) { if (s < 14) n += 2; if (s < 24) n += 2; } return n; }
constexpr int g2_pos_h(int s) { return s < 14 ? 2 * s + 1 : g2_issued(s - 14) + 1; }
constexpr int g2_pos_w(int s) { return s < 12 ? -1 : g2_issued(s - 12) + (s - 12 < 14 ? 2 : 0) + 1; }
constexpr int g2_wait(int st) { const int a = g2_pos_h(st), b = st + 2 < 28 ? g2_pos_w(st + 2) : -1; const int n = g2_issued(st) - 1 - (a > b ? a : b); return n > 63 ? 63 : n; }
DI void ldg_sc1(u32x4& d, const char* sbase, unsigned voff) { asm volatile("global_load_dwordx4 %0, %1, %2 sc1" : "=v"(d) : "v"(voff), "s"(sbase) : "memory"); }
struct G2 { int rot; const char *hb0, *hb1, *w2s, *wos; unsigned lv, w2vo, ringdst; Ev ev; int t; lptr br0, br1; };
DI int g2_k(const G2& g, int st) { const int k = st + g.rot; return k >= 28 ? k - 28 : k; }
template <int ST, int E> DI void g2_w(const G2& g) {
  const unsigned dst = g.ringdst + (ST % 12) * 2048 + E * 1024;
  if constexpr (ST < 28) glds16(g.w2s + (g2_k(g, ST) * 16 + E * 8) * 896, g.w2vo, dst);
  else if constexpr (ST < 35) glds16(g.wos + ((ST - 28) * 16 + E * 8) * 896, g.w2vo, dst);
  else glds16(g.wos + (g.t == 3 ? 104 : 112 + E * 8) * 896, g.w2vo, dst);
}
template <int A, int B> DI void g2_ws(const G2& g) { if constexpr (A < B) { g2_w<A, 0>(g); g2_w<A, 1>(g); asm volatile("s_waitcnt vmcnt(%0)" :: "i"(THROT) : "memory"); g2_ws<A + 1, B>(g); } }
template <int ST> DI void g2_steps(const G2& g, u32x4 (&hf)[14][2], WF (&f)[3], f32x16& a00, f32x16& a01, f32x16& a10, f32x16& a11) {
  if constexpr (ST < 28) {
    if constexpr (ST == G2_SPLIT) { __builtin_amdgcn_s_setprio(0); pwait_long<A_DONE>(g.ev); __builtin_amdgcn_s_setprio(GPRIO); }
    asm volatile("s_waitcnt vmcnt(%2)" : "+v"(hf[ST % 14][0]), "+v"(hf[ST % 14][1]) : "i"(g2_wait(ST)) : "memory");
    constexpr int N = ST + 2, off = (N % 12) * 2048;
    const bf16x8 h0 = asfrag(hf[ST % 14][0]), h1 = asfrag(hf[ST % 14][1]), w0 = cat(f[ST % 3].l0, f[ST % 3].h0), w1 = cat(f[ST % 3].l1, f[ST % 3].h1);
    SBAR(); MM(false, a00, h0, w0);
    if constexpr (N < 28) { f[N % 3].l0 = trd(g.br0 + off); f[N % 3].h0 = trd(g.br0 + off + 512); SBAR(); }
    MM(false, a01, h1, w0);
    if constexpr (N < 28) { f[N % 3].l1 = trd(g.br1 + off); f[N % 3].h1 = trd(g.br1 + off + 512); SBAR(); }
    MM(false, a10, h0, w1);
    MM(false, a11, h1, w1);
    if constexpr (ST < 14) { ldg_sc1(hf[ST][0], g.hb0 + g2_k(g, ST + 14) * 1024, g.lv); ldg_sc1(hf[ST][1], g.hb1 + g2_k(g, ST + 14) * 1024, g.lv); }
    if constexpr (ST < 24) { g2_w<ST + 12, 0>(g); g2_w<ST + 12, 1>(g); }
    if constexpr (ST == 26) { WAIT_VM(0); post<L_WO>(g.ev); }
    g2_steps<ST + 1>(g, hf, f, a00, a01, a10, a11);
  }
}

__global__ void __launch_bounds__(512, 2) fused_block(const char* x, const char* wqkv, const char* wo, const char* w1, const char* w2, char* out, char* ws) {
  asm volatile("" : "+s"(x), "+s"(wqkv), "+s"(wo), "+s"(w1), "+s"(w2), "+s"(out), "+s"(ws));
  __shared__ __attribute__((aligned(16))) char smem[LDS_BYTES];
  u64 pc; asm volatile("s_getpc_b64 %0" : "=s"(pc));
  const lptr lds = (lptr)smem; const unsigned lds0 = (unsigned)(uintptr_t)lds;
  const int tid = threadIdx.x, lane = tid & 63, r32 = lane & 31, hh = lane >> 5;
  const int w = __builtin_amdgcn_readfirstlane(tid >> 6), t = w & 3, c = w >> 2;
  const int b = blockIdx.x, xg = b & 7, bi = b >> 3;
  const int j = (xg & 3) * 4 + (bi >> 3), rb = (xg >> 2) * 8 + (bi & 7);
  const int fme = (rb * 16 + j) * 128, rt = rb * 4 + t;
  const bool hasFb = !((NOFB >> j) & 1);
  const int fbl = 18 + __builtin_popcount(~NOFB & ((1u << j) - 1u));
  STAMP(0); CSTAMP(30); VSTAMP(29, (unsigned)__builtin_amdgcn_s_getreg(20 | (3 << 11)) & 0xfu);
  const int rot = t, r7 = ROT7 ? (rb + 2 * j) % 7 : 0;
  const int q4 = (lane & 15) >> 2, p4 = lane & 3, g16 = (lane >> 4) & 1;
  const int pi_std = 4 * g16 + 2 * (p4 & 1) + (p4 >> 1);
  const lptr wrd0 = lds + (8 * hh + q4) * 128 + pi_std * 8;
  const lptr priv = lds + L_PRIV + w * 256;
  Ev ev = {lds0 + L_CNT, 1u}; PIN(ev.base); PIN(ev.one);
  const __amdgpu_buffer_rsrc_t rsw = __builtin_amdgcn_make_buffer_rsrc(ws, 0, WS_BYTES, 0x00020000);
#define COMMON_SETUP() \
  lptr xrd[4], wr0[4], wr1[4]; \
  _Pragma("unroll") for (int s = 0; s < 4; ++s) { xrd[s] = lds + (t * 32 + r32) * 128 + (((2 * (s ^ rot) + hh) ^ ((r32 >> 1) & 7)) << 4); \
    wr0[s] = wrd0 + ((q4 >> 1) << 6) + (s ^ rot) * 2048; wr1[s] = wrd0 + ((1 ^ (q4 >> 1)) << 6) + (s ^ rot) * 2048; } \
  const int xwrap = (t >> 1) ? -NSLOT * SLOT : 0; \
  bf16x8 sel; { const short one = ((lane & 15) == ((lane >> 4) & 1)) ? (short)0x3f80 : (short)0; sel = (bf16x8){one, one, one, one, one, one, one, one}; } \
  bf16x8 xa[28]; float rs, nmb
#define GET_TAG() PIN(tagraw); const u64 tag = (((u64)(unsigned)__builtin_amdgcn_readfirstlane((unsigned)(tagraw >> 32)) << 32) | (unsigned)__builtin_amdgcn_readfirstlane((unsigned)tagraw)) + 1

  if (c == 0) {
    unsigned cpf0;
    asm volatile("global_load_dword %0, %1, %2" : "=v"(cpf0) : "v"(min((unsigned)(bi * 16 + (tid & 15)) * 128u, (unsigned)CODE_BYTES - 128u)), "s"(pc) : "memory");
    unsigned tch = 0;
    u64 tagraw;
    asm volatile("global_load_dwordx2 %0, %1, %2 sc1" : "=v"(tagraw) : "v"(WS_EPOCH), "s"(ws) : "memory");
    if (tid < N_EV) *(LAS unsigned*)(lds + L_CNT + 4 * tid) = 0u;
    BAR();
    __builtin_amdgcn_s_setprio(P1PRIO);
    const bool isK = j < 7, isV = j >= 7 && j < 14;
    bf16x8 qf[4][2]; u64 tag;
    {
      COMMON_SETUP();
      unsigned ad[4]; bool strad[4] = {false, false, false, false};
      if (isV) { const int rho = r32 >> 2, col0 = (j - 7) * 64 + 4 * ((rho & 4) + 2 * (rho & 1) + ((rho >> 1) & 1)) + (r32 & 3);
#pragma unroll
        for (int cc = 0; cc < 2; ++cc) { const int col = col0 + cc * 32, hd = col / 28, d = col - hd * 28;
          ad[cc] = WS_KV + hd * 262144 + rt * 4096 + 2048 + (hh * 32 + d) * 16; ad[2 + cc] = d >= 24 ? ad[cc] + 64 : 0x80000000u; strad[cc] = d == 24; }
      } else if (isK) {
#pragma unroll
        for (int s = 0; s < 4; ++s) { const int P = j * 8 + 2 * s + hh, hp = P / 7, rr = P - hp * 7, hd = 2 * hp + (rr > 3), sg = rr > 3 ? rr - 4 : rr;
          ad[s] = WS_KV + hd * 262144 + rt * 4096 + sg * 512 + r32 * 16; strad[s] = rr == 3; }
      } else {
#pragma unroll
        for (int s = 0; s < 4; ++s) ad[s] = WS_H + rt * 114688 + (j - 14) * 4096 + s * 1024 + lane * 16;
      }
      PIN(ad[0]); PIN(ad[1]); PIN(ad[2]); PIN(ad[3]);
      P1R r = {}; bf16x8 mf;
      { P1A p; p.xwrap = xwrap; p.ev = ev; WF f[4];
#pragma unroll
        for (int s = 0; s < 4; ++s) { p.x[s] = xrd[s]; p.w0[s] = wr0[s]; p.w1[s] = wr1[s]; }
        const lptr csw = lds + L_CS0 + lane * 16 + t * 4;
#define GS(st) do { r.s2 = MFMA32(xa[st], xa[st], r.s2); PIN(r.s2); r.s1 = MFMA16(sel, xa[st], r.s1); PIN(r.s1); SBAR(); } while (0)
#define MA(VT, st) MM(VT, r.a0, xa[st], cat(f[st & 3].l0, f[st & 3].h0))
#define MB(VT, st) MM(VT, r.a1, xa[st], cat(f[st & 3].l1, f[st & 3].h1))
#define WRD(s) do { wf_read(f[s], p.w0[s] + SLOT, p.w1[s] + SLOT); SBAR(); } while (0)
#define P1_TAIL(VT) do { \
        p1_compute<VT, 0>(xa, f, r, sel, p STA); \
        __builtin_amdgcn_s_setprio(3); \
        _Pragma("unroll") for (int s = 0; s < 4; ++s) xa[24 + s] = ldsb128(p.x[s] + 6 * SLOT); \
        SBAR(); GS(22); WRD(0); GS(23); WRD(1); GS(24); \
        r.c0 = MFMA16(sel, cat(f[0].l0, f[0].h0), r.c0); r.c1 = MFMA16(sel, cat(f[0].l1, f[0].h1), r.c1); PIN(r.c0); PIN(r.c1); SBAR(); \
        GS(25); GS(26); \
        if (lane < 16) { *(LAS float*)csw = r.c0[0]; *(LAS float*)(csw + 256) = r.c0[1]; *(LAS float*)(csw + 512) = r.c1[0]; *(LAS float*)(csw + 768) = r.c1[1]; } \
        post<A_CSX>(ev); SBAR(); \
        GS(27); MA(VT, 22); MB(VT, 22); WRD(2); MA(VT, 23); \
        if (lane < 16) { *(LAS float*)(priv + lane * 4) = r.s1[0]; *(LAS float*)(priv + 64 + lane * 4) = r.s1[1]; } \
        const float sx_ = *(LAS float*)(priv + r32 * 4); SBAR(); \
        MB(VT, 23); WRD(3); post<A_CS0>(ev); MA(VT, 24); \
        const float sq_ = gram_diag(r.s2); SBAR(); \
        MB(VT, 24); \
        pwait<A_CSX>(ev); \
        const f32x4 p0_ = ldsf4(lds + L_CS0 + r32 * 16), p1_ = ldsf4(lds + L_CS0 + 512 + r32 * 16); SBAR(); \
        MA(VT, 25); \
        row_norm(sx_, sq_, rs, nmb); PIN(rs); PIN(nmb); SBAR(); \
        MB(VT, 25); \
        bf16x8 k0_ = cs_frag(sc1(sc1(p0_[0] + p0_[1]) + sc1(p0_[2] + p0_[3]))); PIN(k0_); \
        *(LAS float*)(lds + L_STAT + t * 256 + lane * 4) = hh ? nmb : rs; post<A_ST>(ev); SBAR();     \
        MA(VT, 26); \
        mf = mean_frag(nmb, hh); PIN(mf); SBAR(); \
        MA(VT, 27); \
        bf16x8 k1_ = cs_frag(sc1(sc1(p1_[0] + p1_[1]) + sc1(p1_[2] + p1_[3]))); PIN(k1_); SBAR(); \
        MM(VT, r.a0, mf, k0_); MB(VT, 26); MB(VT, 27); MM(VT, r.a1, mf, k1_); } while (0)
        if (isV) P1_TAIL(true); else P1_TAIL(false); }
      STAMP(1);
      u32x4 o[4];
      if (isV) {
#pragma unroll
        for (int i = 0; i < 4; ++i) { const f32x4 rr = ldsf4(lds + L_STAT + t * 256 + (8 * i + 4 * hh) * 4);
#pragma unroll
          for (int e = 0; e < 2; ++e) { const int n = 4 * i + 2 * e;
            o[i >> 1][2 * (i & 1) + e] = cvtpk(sc1(r.a0[n] * rr[2 * e]), sc1(r.a0[n + 1] * rr[2 * e + 1]));
            o[2 + (i >> 1)][2 * (i & 1) + e] = cvtpk(sc1(r.a1[n] * rr[2 * e]), sc1(r.a1[n + 1] * rr[2 * e + 1])); } }
        ST16(o[0], ad[0]); ST16(o[1], ad[0] + 1024); ST16(o[2], ad[1]); ST16(o[3], ad[1] + 1024);
#pragma unroll
        for (int cc = 0; cc < 2; ++cc) { const unsigned e = strad[cc] ? 0x3f803f80u : 0u; const u32x4 pad = {e, e, e, e}; ST16(pad, ad[2 + cc]); ST16(pad, ad[2 + cc] + 1024); }
      } else {
        if (isK) {
          epi_T<false>(r.a0, rs, o[0], o[1]);
#pragma unroll
          for (int s = 0; s < 2; ++s) { u32x4 v = o[s]; if (strad[s]) { v[2] = 0x3f803f80u; v[3] = 0x00003f80u; } ST16(v, ad[s]); }
          epi_T<false>(r.a1, rs, o[2], o[3]);
#pragma unroll
          for (int s = 2; s < 4; ++s) { u32x4 v = o[s]; if (strad[s]) { v[2] = 0x3f803f80u; v[3] = 0x00003f80u; } ST16(v, ad[s]); }
#pragma unroll
          for (int s = 0; s < 4; ++s) if (strad[s]) { const u32x4 e = {o[s][2], o[s][3], 0x3f803f80u, 0x00003f80u}; ST16(e, ad[s] + 262144); }
        } else {
          epi_T<true>(r.a0, rs, o[0], o[1]); epi_T<true>(r.a1, rs, o[2], o[3]);
#pragma unroll
          for (int s = 0; s < 4; ++s) ST16(o[s], ad[s]);
        }
      }
      STAMP(13);
      if (hasFb) {
        pwait<A_CS0>(ev); pwait<L_X6>(ev);
        __builtin_amdgcn_s_setprio(FDPRIO);
        const unsigned vo = (16 * t + (lane >> 3)) * 3584 + (((lane & 7) ^ (((lane >> 4) & 1) << 2)) << 4);
#pragma unroll
        for (int i = 0; i < 14; ++i) { const int c7 = (i >> 1) + r7 >= 7 ? (i >> 1) + r7 - 7 : (i >> 1) + r7;
          glds16(w1 + fbl * 128 + (c7 * 64 + (i & 1) * 8) * 3584, vo, lds0 + t * 2048 + p1_slot(32 + (i >> 1)) + (i & 1) * 1024); }
        asm volatile("s_waitcnt vmcnt(14)" ::: "memory");
        __builtin_amdgcn_s_setprio(3);
      } else WAIT_VM(0);
      asm volatile("" : "+v"(cpf0), "+v"(tch), "+v"(tagraw) :: "memory");
      *(LAS u64*)(lds + L_TAG) = tagraw;
      tag = (((u64)(unsigned)__builtin_amdgcn_readfirstlane((unsigned)(tagraw >> 32)) << 32) | (unsigned)__builtin_amdgcn_readfirstlane((unsigned)tagraw)) + 1;
      if (cnt_add(lds, C_KV) == 3u && lane == 0) __hip_atomic_store((GAS u64*)(ws + WS_FKV + fme), tag, RLX_AGENT);
      if (cnt_add(lds, C_H) == 7u && lane == 0) __hip_atomic_store((GAS u64*)(ws + WS_FH + fme), tag, RLX_AGENT);
      STAMP(2);
      __builtin_amdgcn_s_setprio(QPRIO);
      f32x16 a0, a1; f32x4 c0;
      const int rho = 4 * g16 + p4, sg = 2 * (rho >> 2) + (rho & 1), half = (rho >> 1) & 1;
      const int g = (j & 1) ? (sg < 3 ? 2 * sg + 1 + half : (half ? 7 : 0)) : 2 * sg + half;
      lptr qb[4];
#pragma unroll
      for (int s = 0; s < 4; ++s) { qb[s] = lds + L_WQ + (8 * hh + q4) * 64 + g * 8 + (s ^ rot) * 1024; PIN(qb[s]); }
      a0 = (f32x16){}; a1 = (f32x16){}; c0 = (f32x4){};
      pwait<L_WQL>(ev);
      s16x4 ql[6], qh[6];
#pragma unroll
      for (int st = 0; st < 4; ++st) { ql[st] = trd(qb[st]); qh[st] = trd(qb[st] + 256); }
#pragma unroll
      for (int st = 0; st < 28; ++st) {
        if (st == QSPLIT) {
          WAIT_VM(0); post<A_FB>(ev); }
        SBAR();
        if (st & 1) { a1 = MFMA32(cat(ql[st % 6], qh[st % 6]), xa[st], a1); PIN(a1); } else { a0 = MFMA32(cat(ql[st % 6], qh[st % 6]), xa[st], a0); PIN(a0); }
        SBAR();
        if (st + 4 < 28) { const int o_ = ((st + 4) >> 2) * 4096; ql[(st + 4) % 6] = trd(qb[st & 3] + o_); qh[(st + 4) % 6] = trd(qb[st & 3] + o_ + 256); }
        SBAR();
        c0 = MFMA16(sel, cat(ql[st % 6], qh[st % 6]), c0); PIN(c0);
      }
      if (lane < 16) { *(LAS float*)(priv + lane * 4) = c0[0]; *(LAS float*)(priv + 64 + lane * 4) = c0[1]; }
      a1 = MFMA32(cs_frag(*(LAS float*)(priv + r32 * 4)), mf, a1);
#pragma unroll
      for (int i = 0; i < 16; ++i) a0[i] = sc1(a0[i] + a1[i]);
      epi_T<false>(a0, rs * QSCALE, o[0], o[1]);
      if (hh) { o[1][2] = 0u; o[1][3] = 0u; }
      *(LAS u32x4*)(lds + L_QX + t * 2048 + lane * 16) = o[0]; *(LAS u32x4*)(lds + L_QX + t * 2048 + 1024 + lane * 16) = o[1];
    }
    post<A_QW>(ev);
    const f32x16 zero = {};
    f32x16 o[4]; u32x4 kv[4][4], pA0 = {}, pA1 = {}, pB0 = {}, pB1 = {};
#pragma unroll
    for (int q = 0; q < 4; ++q) { o[q] = zero; PIN(o[q]); }
    kv[3][2] = (u32x4){}; kv[3][3] = (u32x4){}; PIN(kv[3][2]); PIN(kv[3][3]); PIN(pB0); PIN(pB1);
    STAMP(4);
    { const int c0 = (28 * j) >> 6, c1 = (28 * j + 27) >> 6, sel4 = lane >> 4;
      int jj = sel4 == 0 ? c0 : sel4 == 1 ? c1 : sel4 == 2 ? 7 + c0 : 7 + c1;
      if (j == 0 && (sel4 & 1)) jj = 14 + (sel4 >> 1);
      poll_flags<0>(ws + WS_FKV + ((lane & 15) * 16 + jj) * 128, tag); }
    STAMP(5);
    const int rotk = ROTK ? 2 * rb : 0;
    const unsigned kvb = WS_KV + j * 262144 + t * 65536 + lane * 16;
#pragma unroll
    for (int u = 0; u < 3; ++u)
#pragma unroll
      for (int e = 0; e < 4; ++e) kv[u][e] = __builtin_amdgcn_raw_buffer_load_b128(rsw, kvb + e * 1024, ((u + rotk) & 15) * 4096, LDAUX);
    pwait<A_QW>(ev);
#pragma unroll
    for (int q = 0; q < 4; ++q) { const lptr a = lds + L_QX + ((q + t + 1) & 3) * 2048 + lane * 16; qf[q][0] = ldsb128(a); qf[q][1] = ldsb128(a + 1024); }
    float mref[4]; bool redo;
#pragma unroll
    for (int q = 0; q < 4; ++q) {
      f32x16 s = MFMA32(asfrag(kv[0][0]), qf[q][0], zero); s = MFMA32(asfrag(kv[0][1]), qf[q][1], s);
      mref[q] = set_ref(qf[q][1], tile_max(s) + REF_MARGIN, hh);
    }
    redo = !(__builtin_fmaxf(__builtin_fmaxf(__builtin_fabsf(mref[0]), __builtin_fabsf(mref[1])), __builtin_fmaxf(__builtin_fabsf(mref[2]), __builtin_fabsf(mref[3]))) < REF_LIMIT);
    __builtin_amdgcn_s_setprio(LPRIO);
    f32x16 sA, sB;
    sA = MFMA32(asfrag(kv[0][0]), qf[0][0], zero); sA = MFMA32(asfrag(kv[0][1]), qf[0][1], sA);
    PIN(sA); STAMP(6);
    auto area = [&](int dst, int e) -> lptr { const int n = dst * 3 + e; return lds + (n < 10 ? 8 * SLOT + n * 4096 : (n - 10) * 4096) + lane * 16; };
    auto put_o = [&](const f32x16& v, float m, int q) { const lptr a = area((q + t + 1) & 3, q);
      *(LAS f32x4*)a = (f32x4){v[0], v[1], v[2], v[3]}; *(LAS f32x4*)(a + 1024) = (f32x4){v[4], v[5], v[6], v[7]};
      *(LAS f32x4*)(a + 2048) = (f32x4){v[8], v[9], v[10], v[11]}; *(LAS f32x4*)(a + 3072) = (f32x4){v[12], hh ? m : v[13], v[14], v[15]}; };
#define PUT_O(q) put_o(o[q], mref[q], q)
#define EX4(S, i) do { S[i] = __builtin_amdgcn_exp2f(S[i]); S[i + 1] = __builtin_amdgcn_exp2f(S[i + 1]); S[i + 2] = __builtin_amdgcn_exp2f(S[i + 2]); S[i + 3] = __builtin_amdgcn_exp2f(S[i + 3]); } while (0)
#define UNIT(SC, SN, PC0, PC1, PP0, PP1, KN0, KN1, QN, QP, VV0, VV1, EXTRA) do { \
      SBAR(); if (QN >= 0) { SN = MFMA32(asfrag(KN0), qf[QN & 3][0], zero); PIN(SN); } SBAR(); EX4(SC, 0); PIN(SC); SBAR(); \
      if (QN >= 0) { SN = MFMA32(asfrag(KN1), qf[QN & 3][1], SN); PIN(SN); } SBAR(); EX4(SC, 4); PIN(SC); SBAR(); \
      o[QP] = MFMA32(asfrag(VV0), asfrag(PP0), o[QP]); PIN(o[QP]); SBAR(); EXTRA; EX4(SC, 8); PIN(SC); SBAR(); \
      o[QP] = MFMA32(asfrag(VV1), asfrag(PP1), o[QP]); PIN(o[QP]); SBAR(); EX4(SC, 12); SBAR(); \
      PC0 = (u32x4){cvtpk(SC[0], SC[1]), cvtpk(SC[2], SC[3]), cvtpk(SC[4], SC[5]), cvtpk(SC[6], SC[7])}; \
      PC1 = (u32x4){cvtpk(SC[8], SC[9]), cvtpk(SC[10], SC[11]), cvtpk(SC[12], SC[13]), cvtpk(SC[14], SC[15])}; PIN(PC0); PIN(PC1); SBAR(); } while (0)
    const float lthr = hh ? 16777216.f : __builtin_inff();
#define CHECK3 do { float m_; asm volatile("v_max3_f32 %0, %2, %3, %4\n\tv_cmp_lt_f32 %1, %5, %0" : "=&v"(m_), "=s"(g0) : "v"(o[1][12]), "v"(o[2][12]), "v"(o[3][12]), "v"(lthr)); } while (0)
#define CHECK1 asm volatile("v_cmp_lt_f32 %0, %2, %1" : "=s"(g1) : "v"(o[0][12]), "v"(lthr))
#define LDKV(UL, e) kv[UL][e] = __builtin_amdgcn_raw_buffer_load_b128(rsw, kvb + (e) * 1024, nt, LDAUX)
#define KEYTILE(U, UN, UL, LOAD, QLAST, X1, X2, X3) do { const int nt = ((kt + (U) + 3 + rotk) & 15) * 4096; \
      UNIT(sA, sB, pA0, pA1, pB0, pB1, kv[U][0], kv[U][1], 1, 3, kv[UL][2], kv[UL][3], if (LOAD) { LDKV(UL, 0); LDKV(UL, 1); }); \
      UNIT(sB, sA, pB0, pB1, pA0, pA1, kv[U][0], kv[U][1], 2, 0, kv[U][2], kv[U][3], if (LOAD) { LDKV(UL, 2); LDKV(UL, 3); } X1); \
      UNIT(sA, sB, pA0, pA1, pB0, pB1, kv[U][0], kv[U][1], 3, 1, kv[U][2], kv[U][3], X2); \
      UNIT(sB, sA, pB0, pB1, pA0, pA1, kv[UN][0], kv[UN][1], QLAST, 2, kv[U][2], kv[U][3], X3); \
    } while (0)
    for (int kt = 0; kt < 12; kt += 4) {
      LSTAMP(16 + (kt >> 1)); KEYTILE(0, 1, 3, true, 0, , , ); KEYTILE(1, 2, 0, true, 0, , , ); LSTAMP(17 + (kt >> 1)); KEYTILE(2, 3, 1, true, 0, , , ); u64 g0, g1;
      KEYTILE(3, 0, 2, true, 0, CHECK3, CHECK1, );
      if (!NOCHECK && __builtin_expect((g0 | g1) != 0ull, 0)) {
        NRESC;
        o[3] = MFMA32(asfrag(kv[3][2]), asfrag(pB0), o[3]); o[3] = MFMA32(asfrag(kv[3][3]), asfrag(pB1), o[3]); pB0 = (u32x4){}; pB1 = (u32x4){};
#pragma unroll
        for (int q = 0; q < 4; ++q) { const float l = upper_bcast(o[q][12]); redo |= !(l < 1.2676506002282294e30f);
          const float mn = set_ref(qf[q][1], mref[q] + (float)max(__builtin_amdgcn_frexp_expf(l) + (int)REF_MARGIN, 0), hh);
          const int e = (int)(mn - mref[q]); mref[q] = mn; redo |= !(__builtin_fabsf(mn) < REF_LIMIT);
#pragma unroll
          for (int r = 0; r < 16; ++r) o[q][r] = __builtin_ldexpf(o[q][r], -e);
          if (q == 0) {
#pragma unroll
            for (int r = 0; r < 16; ++r) sA[r] -= (float)e; } } }
    }
    { const int kt = 12;
      if (__builtin_expect(__any(redo), 0)) { mref[0] = mref[1] = mref[2] = mref[3] = __builtin_nanf(""); }
      pwait<L_CSBP>(ev);
      LSTAMP(22); KEYTILE(0, 1, 3, true, 0, , , ); KEYTILE(1, 2, 0, false, 0, , , ); LSTAMP(23); KEYTILE(2, 3, 1, false, 0, , , );
      post<A_DONE>(ev); __builtin_amdgcn_s_setprio(3);
      KEYTILE(3, 0, 2, false, -1, , PUT_O(0), PUT_O(1)); }
    o[3] = MFMA32(asfrag(kv[3][2]), asfrag(pB0), o[3]); o[3] = MFMA32(asfrag(kv[3][3]), asfrag(pB1), o[3]); PIN(o[3]); SBAR();
    PUT_O(2);
    STAMP(7);
    VSTAMP(28, nresc + (__any(redo) ? 1000 : 0));
    auto combine = [&]() -> bool {
      f32x4 pp[3][4]; f32x16 of;
#pragma unroll
      for (int e = 0; e < 3; ++e) { const lptr a = area(t, e); pp[e][3] = ldsf4(a + 3072); }
#pragma unroll
      for (int e = 0; e < 3; ++e) { const lptr a = area(t, e);
#pragma unroll
        for (int i = 0; i < 3; ++i) pp[e][i] = ldsf4(a + i * 1024); }
      const float l0 = o[3][12], l1 = pp[0][3][0], l2 = pp[1][3][0], l3 = pp[2][3][0], m1 = pp[0][3][1], m2 = pp[1][3][1], m3 = pp[2][3][1];
      const float mx = __builtin_fmaxf(__builtin_fmaxf(mref[3] + (float)__builtin_amdgcn_frexp_expf(l0), m1 + (float)__builtin_amdgcn_frexp_expf(l1)),
                                       __builtin_fmaxf(m2 + (float)__builtin_amdgcn_frexp_expf(l2), m3 + (float)__builtin_amdgcn_frexp_expf(l3)));
      float f0 = __builtin_amdgcn_exp2f(mref[3] - mx), f1 = __builtin_amdgcn_exp2f(m1 - mx), f2 = __builtin_amdgcn_exp2f(m2 - mx), f3 = __builtin_amdgcn_exp2f(m3 - mx);
      const float inv = __builtin_amdgcn_rcpf(__builtin_fmaf(l3, f3, __builtin_fmaf(l2, f2, __builtin_fmaf(l1, f1, l0 * f0))));
      f0 = upper_bcast(f0 * inv); f1 = upper_bcast(f1 * inv); f2 = upper_bcast(f2 * inv); f3 = upper_bcast(f3 * inv);
      const bool big = hh && !(__builtin_fmaxf(__builtin_fmaxf(l0, l1), __builtin_fmaxf(l2, l3)) < 1.2676506002282294e30f);
#pragma unroll
      for (int r = 0; r < 16; ++r) of[r] = sc1(__builtin_fmaf(pp[2][r >> 2][r & 3], f3, sc1(__builtin_fmaf(pp[1][r >> 2][r & 3], f2, sc1(__builtin_fmaf(pp[0][r >> 2][r & 3], f1, sc1(o[3][r] * f0)))))));
      u32x4 e0, e1;
#pragma unroll
      for (int p = 0; p < 4; ++p) { e0[p] = cvtpk(of[2 * p], of[2 * p + 1]); e1[p] = cvtpk(of[8 + 2 * p], of[9 + 2 * p]); }
      if (hh) { e1[2] = 0u; e1[3] = 0u; }
      const unsigned a = WS_O + rt * 32768 + j * 2048 + lane * 16;
      ST16(e0, a); ST16(e1, a + 1024);
      unsigned ex = 0;
#pragma unroll
      for (int p = 0; p < 4; ++p) ex |= ((e0[p] & 0x7f807f80u) + 0x00800080u) | ((e1[p] & 0x7f807f80u) + 0x00800080u);
      return __any((ex & 0x80008000u) != 0u || big);
    };
    post<A_CMB>(ev); STAMP(9); pwait<A_CMB>(ev);
    if (__builtin_expect(combine(), 0)) *(LAS unsigned*)(lds + L_CNT + 4 * C_BAD) = 1u;
    STAMP(10);
    WAIT_VM(0);
    const unsigned arr = cnt_add(lds, C_O);
    __builtin_amdgcn_s_setprio(0);
    if (arr != 3u) pwait_long<C_O>(ev);
    const bool bad = __builtin_amdgcn_readfirstlane(*(volatile LAS unsigned*)(lds + L_CNT + 4 * C_BAD)) != 0u;
    if (!bad && arr == 3u && lane == 0) __hip_atomic_store((GAS u64*)(ws + WS_FO + fme), tag, RLX_AGENT);
    if (__builtin_expect(bad, 0)) {
#pragma unroll
      for (int q = 0; q < 4; ++q) { set_ref(qf[q][1], 0.f, hh); o[q] = zero; mref[q] = -3.0e38f; }
#pragma nounroll
      for (int kt = 0; kt < 16; ++kt) {
        const u32x4 k0 = LD16(kvb + kt * 4096), k1 = LD16(kvb + kt * 4096 + 1024), v0 = LD16(kvb + kt * 4096 + 2048), v1 = LD16(kvb + kt * 4096 + 3072);
#pragma unroll
        for (int q = 0; q < 4; ++q) {
          f32x16 s = MFMA32(asfrag(k0), qf[q][0], zero); s = MFMA32(asfrag(k1), qf[q][1], s);
          const float mn = __builtin_fmaxf(mref[q], tile_max(s)), al = __builtin_amdgcn_exp2f(mref[q] - mn); mref[q] = mn;
#pragma unroll
          for (int r = 0; r < 16; ++r) { o[q][r] *= al; s[r] = __builtin_amdgcn_exp2f(s[r] - mn); }
          const u32x4 p0 = {cvtpk(s[0], s[1]), cvtpk(s[2], s[3]), cvtpk(s[4], s[5]), cvtpk(s[6], s[7])}, p1 = {cvtpk(s[8], s[9]), cvtpk(s[10], s[11]), cvtpk(s[12], s[13]), cvtpk(s[14], s[15])};
          o[q] = MFMA32(asfrag(v0), asfrag(p0), o[q]); o[q] = MFMA32(asfrag(v1), asfrag(p1), o[q]);
        }
      }
      PUT_O(0); PUT_O(1); PUT_O(2);
      post<A_CMB2>(ev); pwait<A_CMB2>(ev);
      combine();
      WAIT_VM(0);
      if (cnt_add(lds, C_O2) == 3u && lane == 0) __hip_atomic_store((GAS u64*)(ws + WS_FO + fme), tag, RLX_AGENT);
    }
    STAMP(8); CSTAMP(31); STAMP_OUT();
  } else {
    Ld k;
    k.xsrc = x + (rb * 128 + t * 32) * 896; k.xvo0 = (lane >> 3) * 896 + (((lane & 7) ^ (lane >> 4)) << 4); k.xvo1 = k.xvo0 ^ 64u;
    k.xdst = lds0 + t * 4096; k.xdst6 = lds0 + (t < 2 ? 18 * SLOT + t * 4096 : (t - 2) * 4096);
#pragma unroll
    for (int i = 0; i < 7; ++i) k.ck[i] = i + r7 >= 7 ? i + r7 - 7 : i + r7;
    { const int row = 16 * t + (lane >> 3); const unsigned piece = ((lane & 7) ^ (((lane >> 4) & 1) << 2)) << 4;
      k.w0rs = j < 14 ? 2688u : 3584u; k.w0src = j < 14 ? wqkv + 896 + j * 128 : w1 + (j - 14) * 128;
      k.fasrc = w1 + (2 + j) * 128; k.dst = lds0 + t * 2048;
      k.w0vo = row * k.w0rs + piece; k.wfvo = row * 3584 + piece;
      k.wqsrc = wqkv + j * 56; k.wqvo = (16 * t + (lane >> 2)) * 2688 + (lane & 3) * 16; k.qdst = lds0 + L_WQ + t * 1024; k.ev = ev; }
    {
      COMMON_SETUP();
      STAMP(14);
      p1_stream<0>(k, xa, xrd, xwrap STA);
      STAMP(15);
      __builtin_amdgcn_s_setprio(GPRIO);
#pragma unroll
      for (int s = 0; s < 4; ++s) { wr0[s] += L_FA; wr1[s] += L_FA; PIN(wr0[s]); PIN(wr1[s]); }
      WF f[3]; f32x16 a0 = {}, a1 = {}; f32x4 c0 = {}, c1 = {}; u32x4 o[2];
      pwait<L_FAL>(ev);
      wf_read(f[0], wr0[0], wr1[0]); wf_read(f[1], wr0[1], wr1[1]);
      f_steps<0, 0>(ev, xa, f, a0, a1, c0, c1, sel, wr0, wr1);
      cs_put(lds, L_CSA, 0, t, lane, c0); cs_put(lds, L_CSA, 1, t, lane, c1);
      pwait<A_ST>(ev);
      rs = *(LAS float*)(lds + L_STAT + t * 256 + r32 * 4); nmb = *(LAS float*)(lds + L_STAT + t * 256 + 128 + r32 * 4);
      const bf16x8 mf = mean_frag(nmb, hh);
      post<L_CSAP>(ev); pwait<L_CSAP>(ev);
      STAMP(2);
      __builtin_amdgcn_s_setprio(FBPRIO);
      { const unsigned ad = WS_H + rt * 114688 + (2 + j) * 4096 + lane * 16;
        a0 = MFMA32(cs_frag(cs_get(lds, L_CSA, 0, r32)), mf, a0); a1 = MFMA32(cs_frag(cs_get(lds, L_CSA, 1, r32)), mf, a1);
        epi_T<true>(a0, rs, o[0], o[1]); ST16(o[0], ad); ST16(o[1], ad + 1024);
        epi_T<true>(a1, rs, o[0], o[1]); ST16(o[0], ad + 2048); ST16(o[1], ad + 3072); }
      STAMP(3);
      if (hasFb) {
#pragma unroll
        for (int s = 0; s < 4; ++s) { wr0[s] += L_FB - L_FA; wr1[s] += L_FB - L_FA; PIN(wr0[s]); PIN(wr1[s]); }
        a0 = (f32x16){}; a1 = (f32x16){}; c0 = (f32x4){}; c1 = (f32x4){};
        pwait<A_FB>(ev);
        wf_read(f[0], wr0[0], wr1[0]); wf_read(f[1], wr0[1], wr1[1]);
        f_steps<1, 0>(ev, xa, f, a0, a1, c0, c1, sel, wr0, wr1);
        cs_put(lds, L_CSB, 0, t, lane, c0); cs_put(lds, L_CSB, 1, t, lane, c1);
        post<L_CSBP>(ev); pwait<L_CSBP>(ev);
        const unsigned ad = WS_H + rt * 114688 + fbl * 4096 + lane * 16;
        a0 = MFMA32(cs_frag(cs_get(lds, L_CSB, 0, r32)), mf, a0); a1 = MFMA32(cs_frag(cs_get(lds, L_CSB, 1, r32)), mf, a1);
        epi_T<true>(a0, rs, o[0], o[1]); ST16(o[0], ad); ST16(o[1], ad + 1024);
        epi_T<true>(a1, rs, o[0], o[1]); ST16(o[0], ad + 2048); ST16(o[1], ad + 3072);
      } else post<L_CSBP>(ev);
    }
    pwait<A_FB>(ev);
    u64 tagraw = *(LAS u64*)(lds + L_TAG);
    GET_TAG();
    WAIT_VM(0);
    if (cnt_add(lds, C_H) == 7u && lane == 0) __hip_atomic_store((GAS u64*)(ws + WS_FH + fme), tag, RLX_AGENT);
    STAMP(4);
    const int cg = bi >> 2;
    if (cg < 7) {
      const int rt0 = 8 * xg + 2 * (bi & 3), rb3 = rt0 >> 2;
      const int ring = ring_of(t);
      const int ct = t >> 1, rtl = t & 1;
      G2 g; g.rot = ROTG ? (4 * cg + 7 * (bi & 3)) % 28 : 0; g.ev = ev; g.t = t; g.ringdst = lds0 + ring; g.lv = lane * 16;
      g.w2vo = (lane >> 3) * 896 + (((lane & 7) ^ (((lane >> 4) & 1) << 2)) << 4);
      g.w2s = w2 + t * 448 * 896 + cg * 128; g.wos = wo + t * 112 * 896 + cg * 128;
      g.hb0 = ws + WS_H + (rt0 + rtl) * 114688 + t * 28672; g.hb1 = ws + WS_H + (rt0 + (rtl ^ 1)) * 114688 + t * 28672;
      g.br0 = wrd0 + ring + ((ct ^ (q4 >> 1)) << 6); g.br1 = wrd0 + ring + ((ct ^ 1 ^ (q4 >> 1)) << 6);
      unsigned oo = ((rt0 + rtl) * 32 + r32) * 896 + cg * 128 + ct * 64 + hh * 16;
      const char* hfl = ws + WS_FH + (rb3 * 16 + (lane & 15)) * 128;
      f32x16 a00, a01 = {}, a10 = {}, a11 = {};
      PIN(g.br0); PIN(g.br1); PIN(g.w2vo); PIN(g.lv); PIN(oo); PIN(hfl); PIN(a01); PIN(a10); PIN(a11);
      __builtin_amdgcn_s_setprio(0);
      g2_ws<0, 12>(g);
      u32x4 xr0 = *(const GAS u32x4*)(x + oo), xr1 = *(const GAS u32x4*)(x + oo + 32);
      poll_flags<HSLEEP>(hfl, tag);
      PIN(xr0); PIN(xr1);
      STAMP(5);
      u32x4 hf[14][2];
#pragma unroll
      for (int s = 0; s < 14; ++s) { ldg_sc1(hf[s][0], g.hb0 + g2_k(g, s) * 1024, g.lv); ldg_sc1(hf[s][1], g.hb1 + g2_k(g, s) * 1024, g.lv); asm volatile("s_waitcnt vmcnt(%0)" :: "i"(THROT) : "memory"); }
      __builtin_amdgcn_s_setprio(3);
#pragma unroll
      for (int p = 0; p < 4; ++p) { a00[2 * p] = bflo(xr0[p]); a00[2 * p + 1] = bfhi(xr0[p]); a00[8 + 2 * p] = bflo(xr1[p]); a00[9 + 2 * p] = bfhi(xr1[p]); }
      PIN(a00);
      __builtin_amdgcn_s_setprio(G2PRIO);
      { WF f[3]; wf_read(f[0], g.br0, g.br1); wf_read(f[1], g.br0 + 2048, g.br1 + 2048);
        g2_steps<0>(g, hf, f, a00, a01, a10, a11); }
      STAMP(6);
      pwait<L_WO>(ev);
      bf16x8 wf[16][2];
#pragma unroll
      for (int g2 = 0; g2 < 2; ++g2) { const lptr o0 = lds + ring_of(2 * ct + g2) + SLOT + (4 * hh + q4) * 128 + pi_std * 8;
#pragma unroll
        for (int e = 0; e < 8; ++e)
#pragma unroll
          for (int cc = 0; cc < 2; ++cc) wf[8 * g2 + e][cc] = trfrag(o0 + ((cc ^ ct ^ (q4 >> 1)) << 6) + ((e >> 1) * 28 + (e & 1) * 16) * 128, 1024); }
      auto put = [&](const f32x16& a, int e) { const lptr m = lds + ring + e * 4096 + lane * 16;
#pragma unroll
        for (int i = 0; i < 4; ++i) *(LAS f32x4*)(m + i * 1024) = (f32x4){a[4 * i], a[4 * i + 1], a[4 * i + 2], a[4 * i + 3]}; };
      auto add = [&](f32x16& a, int src, int e) { const lptr m = lds + ring_of(src) + e * 4096 + lane * 16;
#pragma unroll
        for (int i = 0; i < 4; ++i) { const f32x4 v = ldsf4(m + i * 1024);
#pragma unroll
          for (int n = 0; n < 4; ++n) a[4 * i + n] = sc1(a[4 * i + n] + v[n]); } };
      put(a01, 0); put(a11, 1);
      post<L_RED>(ev); pwait<L_RED>(ev);
      add(a00, t ^ 1, 0); add(a10, t ^ 1, 1);
      PIN(a00); PIN(a10);
      STAMP(11);
      { const char* fl = ws + WS_FO + (rb3 * 16 + ct * 8 + (lane & 7)) * 128; const unsigned ob = WS_O + (rt0 + rtl) * 32768 + ct * 16384 + lane * 16;
        unsigned ready = 0; u32x4 of[8][2];
#pragma unroll
        for (int h = 0; h < 8 + OLAG; ++h) {
          if (h < 8) {
            if (__builtin_expect(!((ready >> h) & 1u), 0)) ready = poll_bit(fl, tag, h);
            if (h == 7) STAMP(7);
            of[h][0] = LD16(ob + h * 2048); of[h][1] = LD16(ob + h * 2048 + 1024); }
          if (h >= OLAG) {
#pragma unroll
            for (int s = 0; s < 2; ++s) { const bf16x8 o_ = asfrag(of[h - OLAG][s]); a10 = MFMA32(wf[2 * (h - OLAG) + s][1], o_, a10); a00 = MFMA32(wf[2 * (h - OLAG) + s][0], o_, a00); }
            PIN(a10); PIN(a00); }
          SBAR(); } }
      STAMP(9);
      put(a10, 2);
      post<L_FIN>(ev);
      STAMP(10); pwait<L_FIN>(ev);
      add(a00, t ^ 2, 2);
      u32x4 r0, r1;
#pragma unroll
      for (int p = 0; p < 4; ++p) { r0[p] = cvtpk(a00[2 * p], a00[2 * p + 1]); r1[p] = cvtpk(a00[8 + 2 * p], a00[9 + 2 * p]); }
      *(GAS u32x4*)(out + oo) = r0; *(GAS u32x4*)(out + oo + 32) = r1;
      if (b == 0 && tid == 256) __hip_atomic_store((GAS u64*)(ws + WS_EPOCH), tag, RLX_AGENT);
      STAMP(8);
    }
    STAMP_OUT();
  }
}

extern "C" void kernel_launch(void* const* d_in, const int* in_sizes, int n_in, void* d_out, int out_size, void* d_ws, size_t ws_size, hipStream_t stream) {
  fused_block<<<256, 512, 0, stream>>>((const char*)d_in[0], (const char*)d_in[1], (const char*)d_in[2], (const char*)d_in[4], (const char*)d_in[5], (char*)d_out, (char*)d_ws + WS_BASE);
}
```
